```python
import torch

def _softmax(x, axis=-1):
    return torch.softmax(x, dim=axis)

def _gelu(x):
    return torch.nn.functional.gelu(x, approximate='tanh')

def _silu(x):
    return torch.nn.functional.silu(x)

def _sigmoid(x):
    return torch.sigmoid(x)

def _layernorm(x, axis=-1, eps=1e-05):
    m = x.mean(dim=axis, keepdim=True)
    v = x.var(dim=axis, keepdim=True, unbiased=False)
    return (x - m) / torch.sqrt(v + eps)

def _scatter_add(tgt, idx, src, axis=0):
    return tgt.clone().index_add_(axis, idx, src)

def _segment_sum(src, seg, n):
    out = torch.zeros((n,) + src.shape[1:], dtype=src.dtype)
    return out.index_add_(0, seg, src)

def _pad(x, pw):
    flat = tuple((v for pair in reversed(pw) for v in pair))
    return torch.nn.functional.pad(x, flat)
kernel_name = 'concat_heads__ssm_conv_s5__memory_aug_recurrent_mem_tr__prompt'

def setup_inputs():

    _w = lambda a: (lambda t: t.to(torch.bfloat16) if t.is_floating_point() else t)(torch.as_tensor(a))

    def _A_setup():
        B, S, D = (1, 2048, 1024)
        P = D
        Lambda_re = -0.5 * (lambda _x: torch.exp(torch.as_tensor(_x)))((lambda *s: torch.randn(*s, dtype=torch.bfloat16) * 0.8)(P))
        Lambda_im = (lambda *s: torch.randn(*s, dtype=torch.bfloat16) * 0.8)(P) * 1.0
        B_re = (lambda *s: torch.randn(*s, dtype=torch.bfloat16) * 0.8)(P, D) * D ** (-0.5)
        B_im = (lambda *s: torch.randn(*s, dtype=torch.bfloat16) * 0.8)(P, D) * D ** (-0.5)
        C_re = (lambda *s: torch.randn(*s, dtype=torch.bfloat16) * 0.8)(D, P) * P ** (-0.5)
        C_im = (lambda *s: torch.randn(*s, dtype=torch.bfloat16) * 0.8)(D, P) * P ** (-0.5)
        log_dt = (lambda *s: torch.randn(*s, dtype=torch.bfloat16) * 0.8)(P) * 0.3 - 2.0
        Dskip = (lambda *s: torch.randn(*s, dtype=torch.bfloat16) * 0.8)(D)
        return {'x': _w((lambda *s: torch.randn(*s, dtype=torch.bfloat16) * 0.05)(B, S, D)), 'Lambda_re': _w(Lambda_re), 'Lambda_im': _w(Lambda_im), 'B_re': _w(B_re), 'B_im': _w(B_im), 'C_re': _w(C_re), 'C_im': _w(C_im), 'log_dt': _w(log_dt), 'Dskip': _w(Dskip)}
    _A_in = _A_setup()

    def _B_setup():
        B, S, Dm = (1, 2048, 1024)
        H, Dh = (32, 128)
        M, seg = (8, 32)
        seg = min(seg, S)
        S = S // seg * seg
        return {'x': _w((lambda *s: torch.randn(*s, dtype=torch.bfloat16) * 0.05)(B, S, Dm)), 'mem0': _w((lambda *s: torch.randn(*s, dtype=torch.bfloat16) * 0.8)(B, M, Dm)), 'Wq': _w((lambda *s: torch.randn(*s, dtype=torch.bfloat16) * 3.2)(Dm, H, Dh) * Dm ** (-0.5)), 'Wk': _w((lambda *s: torch.randn(*s, dtype=torch.bfloat16) * 3.2)(Dm, H, Dh) * Dm ** (-0.5)), 'Wv': _w((lambda *s: torch.randn(*s, dtype=torch.bfloat16) * 0.8)(Dm, H, Dh) * Dm ** (-0.5)), 'Wo': _w((lambda *s: torch.randn(*s, dtype=torch.bfloat16) * 0.8)(H, Dh, Dm) * (H * Dh) ** (-0.5)), 'seg_len': seg}
    _B_in = _B_setup()
    return {'A_x': _A_in['x'], 'A_Lambda_re': _A_in['Lambda_re'], 'A_Lambda_im': _A_in['Lambda_im'], 'A_B_re': _A_in['B_re'], 'A_B_im': _A_in['B_im'], 'A_C_re': _A_in['C_re'], 'A_C_im': _A_in['C_im'], 'A_log_dt': _A_in['log_dt'], 'A_Dskip': _A_in['Dskip'], 'B_x': _B_in['x'], 'B_mem0': _B_in['mem0'], 'B_Wq': _B_in['Wq'], 'B_Wk': _B_in['Wk'], 'B_Wv': _B_in['Wv'], 'B_Wo': _B_in['Wo'], 'B_seg_len': _B_in['seg_len']}

def reference(A_x, A_Lambda_re, A_Lambda_im, A_B_re, A_B_im, A_C_re, A_C_im, A_log_dt, A_Dskip, B_x, B_mem0, B_Wq, B_Wk, B_Wv, B_Wo, B_seg_len):

    def _A_ref(x, Lambda_re, Lambda_im, B_re, B_im, C_re, C_im, log_dt, Dskip):
        Bsz, L, H = x.shape
        Lam = Lambda_re + 1j * Lambda_im
        Bmat = B_re + 1j * B_im
        Cmat = C_re + 1j * C_im
        dt = torch.exp(log_dt)
        Lam_bar = torch.exp(dt * Lam)
        B_bar = ((Lam_bar - 1.0) / Lam)[:, None] * Bmat
        Bu = torch.einsum('bld,pd->blp', x + 0j, B_bar)
        h = torch.zeros_like(Bu[:, 0, :])
        hs = []
        for t in range(L):
            h = Lam_bar[None, :] * h + Bu[:, t, :]
            hs.append(h)
        hseq = torch.stack(hs, 1)
        y = 2.0 * torch.einsum('blp,dp->bld', hseq, Cmat).real + x * Dskip[None, None, :]
        return y

    def _B_ref(x, mem0, Wq, Wk, Wv, Wo, seg_len):
        B, S, Dm = x.shape
        H, Dh = (Wq.shape[1], Wq.shape[2])
        M = mem0.shape[1]
        scale = Dh ** (-0.5)
        n_seg = S // seg_len
        L = M + seg_len
        idx = torch.arange(L)
        mask = (idx[:, None] < M) | (idx[:, None] >= idx[None, :])
        mem = mem0
        outs = []
        for s in range(n_seg):
            xs = x[:, s * seg_len:(s + 1) * seg_len, :].clone()
            h = torch.concatenate([mem, xs], axis=1)
            q = torch.einsum('btd,dhe->bhte', h, Wq)
            k = torch.einsum('btd,dhe->bhte', h, Wk)
            v = torch.einsum('btd,dhe->bhte', h, Wv)
            scores = torch.einsum('bhqe,bhke->bhqk', q, k) * scale
            scores = torch.where(mask[None, None], scores, -1000000000.0)
            p = _softmax(scores, axis=-1)
            o = torch.einsum('bhqk,bhke->bhqe', p, v)
            o = torch.einsum('bhte,hed->btd', o, Wo)
            mem = o[:, :M, :].clone()
            outs.append(o[:, M:, :])
        return torch.concatenate(outs, axis=1)
    a = _A_ref(A_x, A_Lambda_re, A_Lambda_im, A_B_re, A_B_im, A_C_re, A_C_im, A_log_dt, A_Dskip)
    b = _B_ref(B_x, B_mem0, B_Wq, B_Wk, B_Wv, B_Wo, B_seg_len)
    return torch.concatenate([a, b], axis=-1)
```

```cpp
#include <hip/hip_runtime.h>
#include <hip/hip_bf16.h>
#include <stdint.h>

#define GAS __attribute__((address_space(1)))
#define LAS __attribute__((address_space(3)))
#define DI __device__ __forceinline__
typedef unsigned u32;
typedef unsigned long long u64;
typedef u32 v2u __attribute__((ext_vector_type(2)));
typedef u32 v4u __attribute__((ext_vector_type(4)));
typedef float f2 __attribute__((ext_vector_type(2)));
typedef float f4 __attribute__((ext_vector_type(4)));
typedef short s4 __attribute__((ext_vector_type(4)));
typedef __bf16 b2 __attribute__((ext_vector_type(2)));
typedef __bf16 b8 __attribute__((ext_vector_type(8)));
typedef GAS u32 gu32;
typedef LAS unsigned char* lp;
typedef __amdgpu_buffer_rsrc_t rsrc_t;
#define RLX_AGENT __ATOMIC_RELAXED, __HIP_MEMORY_SCOPE_AGENT

constexpr u32 EX_SLOT = 327680;
constexpr u32 L_MEMA = 0, L_MEMB = 16384, L_O = 32768, L_VM = 98304;
constexpr u32 OFF_STAMP = 3 * EX_SLOT + 65536;
constexpr bool ROT = 1, PUB32 = 1;
constexpr u32 OFF_CTL = 0x180000, CTL_BYTES = 98304;
constexpr u32 OFF_KF = 2u << 20, OFF_VF = OFF_KF + (16u << 20), OFF_KVM = OFF_VF + (16u << 20), OFF_DBG = OFF_KVM + (8u << 20);
constexpr u32 OFF_OF = OFF_DBG + (8u << 20), OFF_H = OFF_OF + (16u << 20), OFF_AGG = OFF_H + (8u << 20), WS_END = OFF_AGG + (1u << 20);
constexpr int C_TMO = 0, C_EP = 64, C_F = 128, C_SEEN = 960, C_PROG = 1024, C_KVF = 4096, C_OCNT = 12288, C_AGF = 16448, C_HCNT = 20544, C_END = 21568;
constexpr u32 SENT = 0xFFFFFFFFu, OOB = 0x80000000u;
constexpr int SC1 = 16;
constexpr int LDS_MEM = 131072, LDS_QK = LDS_MEM + 16384, LDS_TOTAL = 163840;

struct Params {
  const uint16_t *xA, *Lre, *Lim, *Bre, *Bim, *Cre, *Cim, *ldt, *Dsk, *xB, *mem0, *Wq, *Wk, *Wv, *Wo;
  float* out;
  unsigned char* wb;
  int da, db, dr, mode;
  u32 plan[20];
};

DI constexpr u32 physo(u32 lo) { return ((lo >> 13) << 14) | (lo & 8191u); }
DI u32 ct() { u64 t; asm volatile("s_memtime %0\n\ts_waitcnt lgkmcnt(0)" : "=s"(t)::"memory"); return (u32)t; }
DI u32 rt32() { u64 t; asm volatile("s_memrealtime %0\n\ts_waitcnt lgkmcnt(0)" : "=s"(t)::"memory"); return (u32)t; }
DI rsrc_t mkrs(const void* p, u32 n) { return __builtin_amdgcn_make_buffer_rsrc((void*)p, 0, n, 0x00020000); }
DI v4u mkrv(const void* p, u32 n) { const u64 a = (u64)p; return (v4u){(u32)a, (u32)(a >> 32) & 0xffffu, n, 0x00020000u}; }
DI void stx4(v4u v, rsrc_t r, u32 vo, u32 so) { __builtin_amdgcn_raw_buffer_store_b128(v, r, vo + so, 0, SC1); }
#define WAITV(n) asm volatile("s_waitcnt vmcnt(" #n ")" ::: "memory")
#define BARRIER() do { asm volatile("s_waitcnt lgkmcnt(0)" ::: "memory"); __builtin_amdgcn_s_barrier(); asm volatile("" ::: "memory"); } while (0)

DI void role_A(const Params& p, u32 lb, int h, int w, int lane) {
  const u32 c = lane & 15, g = lane >> 4;
  const rsrc_t rx = mkrs(p.wb, WS_END);
  const u32 o_off = physo(L_O + ((c >> 2) & 1) * 32768 + h * 1024) + (w * 16 + g * 4 + (c & 3)) * 16;
#pragma unroll
  for (int sl = 0; sl < 3; ++sl) stx4((v4u){SENT, SENT, SENT, SENT}, rx, o_off + (c >> 3) * 8192, sl * EX_SLOT);
  { const u32 now = rt32() * 8 - 0x800000u;
    __builtin_amdgcn_raw_buffer_store_b32(now, rx, lane < 4 ? OFF_STAMP + (lane >> 1) * 8192 + h * 128 + (lane & 1) * 16 + w * 4 : OOB, 0, SC1); }
  const u32 vo_rdy = threadIdx.x == 0 ? OFF_CTL + 4 * C_F + 8 * h : OOB;
  const u32 vo_mem = physo(L_MEMA) + (g >> 1) * 512 + (c >> 3) * 256 + ((g & 1) * 8 + (c & 7)) * 16;
  const u32 vo_vm = physo(L_VM + h * 2048) + w * 512 + ((g & 1) * 16 + c) * 16;
  const u32 vo_kf = OFF_KF + h * 8192 + w * 2048 + lane * 16, vo_vf = OFF_VF + h * 8192 + w * 2048 + lane * 16;
  const u32 vo_km = c < 8 ? OFF_KVM + h * 4096 + w * 512 + (g * 8 + c) * 16 : OOB;
  const u32 vo_vmp = g < 2 ? OFF_KVM + h * 4096 + 2048 + w * 512 + (g * 16 + c) * 16 : OOB;
  const u32 vo_o = c < 8 ? o_off : OOB;
  const u32 idx = (c & 2) ? 0xEEEEu : 0x4444u;
  const u32 a_m0 = lb + LDS_MEM + w * 4096 + lane * 16, a_m1 = lb + LDS_MEM + (w ^ 1) * 4096 + lane * 16;
  const u32 a_m2 = lb + LDS_MEM + (w ^ 2) * 4096 + lane * 16, a_m3 = lb + LDS_MEM + (w ^ 3) * 4096 + lane * 16;
  const u32 a_w = lb + (w >> 1) * 65536 + (w & 1) * 16384 + lane * 16;
  const u32 a_qw = lb + LDS_QK + (w * 32 + c) * 16 + g * 4;
  const u32 a_qr = lb + LDS_QK + (4 * g + (c >> 2)) * 16 + (c & 1) * 8;
  const u32 vo_prog = lane == 0 ? OFF_STAMP + h * 128 + w * 4 : threadIdx.x == 1 ? OFF_CTL + 4 * (C_PROG + 64 * h) : OOB;
  const u32 vo_flag = OFF_CTL + 4 * C_KVF + ((lane & 1) * 16 + (h >> 1)) * 64;
  const u32 vo_src = (lane >> 3) * 8192 + (h * 128 + 64 * (w >> 1)) * 2 + (((lane & 7) * 16) ^ ((lane >> 5) * 32));
  const u32 a_tr = lb + (w >> 1) * 65536 + (w & 1) * 16384 + (16 * (g >> 1) + 4 * (g & 1) + ((c >> 2) & 1) + 8 * (c >> 3)) * 128 + ((2 * (w & 1)) ^ ((g & 1) + 2 * (c >> 3))) * 32 + (c & 3) * 8;
  const u32 vo_dbg = lane == 0 ? OFF_DBG + (h * 4 + w) * 128 : OOB;
  const v4u rxv = mkrv(p.wb, WS_END), rw = mkrv((w & 1) ? p.Wk : p.Wq, 8u << 20);
  const u32 d0 = p.da;
  asm volatile(
    "v_mov_b32 v0, v64\n"
    "v_mov_b32 v1, v65\n"
    "v_mov_b32 v2, v66\n"
    "v_mov_b32 v3, v67\n"
    "v_mov_b32 v4, v68\n"
    "v_mov_b32 v5, v69\n"
    "v_mov_b32 v6, v70\n"
    "v_mov_b32 v7, v71\n"
    "v_mov_b32 v8, v72\n"
    "v_mov_b32 v9, v73\n"
    "v_mov_b32 v10, v74\n"
    "v_mov_b32 v11, v75\n"
    "v_mov_b32 v12, v76\n"
    "v_mov_b32 v13, v77\n"
    "v_mov_b32 v14, v78\n"
    "v_mov_b32 v15, v79\n"
    "v_mov_b32 v16, v80\n"
    "v_mov_b32 v20, v81\n"
    "v_mov_b32 v22, v82\n"
    "v_mov_b32 v19, v83\n"
    "v_mov_b32 v125, v84\n"
    "s_nop 4\n"
    "s_and_b32 s69, s56, 2\n"
    "s_mov_b32 s71, s69\n"
    "s_lshl_b32 s74, s69, 15\n"
    "s_and_b32 s69, s56, 1\n"
    "s_lshl_b32 s72, s69, 14\n"
    "s_add_u32 s72, s72, s74\n"
    "s_lshl_b32 s69, s69, 13\n"
    "s_add_u32 s74, s74, s69\n"
    "v_xor_b32 v23, 32, v22\n"
    "v_xor_b32 v24, 0x4000, v22\n"
    "v_xor_b32 v25, 0x4020, v22\n"
    "v_xor_b32 v21, 64, v20\n"
    "v_mov_b32 v58, 1573120\n"
    "buffer_load_dwordx2 v[56:57], v58, s[40:43], 0 offen sc1\n"
    "s_xor_b32 s69, s71, 0\n"
    "s_lshl_b32 s69, s69, 2\n"
    "s_add_u32 s69, s69, 1\n"
    "s_lshl_b32 s69, s69, 19\n"
    "s_add_u32 m0, s74, 0\n"
    "s_nop 0\n"
    "buffer_load_dwordx4 v20, s[44:47], s69 offen lds\n"
    "s_add_u32 m0, m0, 1024\n"
    "s_add_u32 s69, s69, 65536\n"
    "buffer_load_dwordx4 v21, s[44:47], s69 offen lds\n"
    "s_add_u32 m0, m0, 1024\n"
    "s_add_u32 s69, s69, 65536\n"
    "buffer_load_dwordx4 v20, s[44:47], s69 offen lds\n"
    "s_add_u32 m0, m0, 1024\n"
    "s_add_u32 s69, s69, 65536\n"
    "buffer_load_dwordx4 v21, s[44:47], s69 offen lds\n"
    "s_add_u32 m0, m0, 1024\n"
    "s_add_u32 s69, s69, 65536\n"
    "buffer_load_dwordx4 v20, s[44:47], s69 offen lds\n"
    "s_add_u32 m0, m0, 1024\n"
    "s_add_u32 s69, s69, 65536\n"
    "buffer_load_dwordx4 v21, s[44:47], s69 offen lds\n"
    "s_add_u32 m0, m0, 1024\n"
    "s_add_u32 s69, s69, 65536\n"
    "buffer_load_dwordx4 v20, s[44:47], s69 offen lds\n"
    "s_add_u32 m0, m0, 1024\n"
    "s_add_u32 s69, s69, 65536\n"
    "buffer_load_dwordx4 v21, s[44:47], s69 offen lds\n"
    "s_xor_b32 s69, s71, 1\n"
    "s_lshl_b32 s69, s69, 2\n"
    "s_add_u32 s69, s69, 1\n"
    "s_lshl_b32 s69, s69, 19\n"
    "s_add_u32 m0, s74, 16384\n"
    "s_nop 0\n"
    "buffer_load_dwordx4 v20, s[44:47], s69 offen lds\n"
    "s_add_u32 m0, m0, 1024\n"
    "s_add_u32 s69, s69, 65536\n"
    "buffer_load_dwordx4 v21, s[44:47], s69 offen lds\n"
    "s_add_u32 m0, m0, 1024\n"
    "s_add_u32 s69, s69, 65536\n"
    "buffer_load_dwordx4 v20, s[44:47], s69 offen lds\n"
    "s_add_u32 m0, m0, 1024\n"
    "s_add_u32 s69, s69, 65536\n"
    "buffer_load_dwordx4 v21, s[44:47], s69 offen lds\n"
    "s_add_u32 m0, m0, 1024\n"
    "s_add_u32 s69, s69, 65536\n"
    "buffer_load_dwordx4 v20, s[44:47], s69 offen lds\n"
    "s_add_u32 m0, m0, 1024\n"
    "s_add_u32 s69, s69, 65536\n"
    "buffer_load_dwordx4 v21, s[44:47], s69 offen lds\n"
    "s_add_u32 m0, m0, 1024\n"
    "s_add_u32 s69, s69, 65536\n"
    "buffer_load_dwordx4 v20, s[44:47], s69 offen lds\n"
    "s_add_u32 m0, m0, 1024\n"
    "s_add_u32 s69, s69, 65536\n"
    "buffer_load_dwordx4 v21, s[44:47], s69 offen lds\n"
    "s_xor_b32 s69, s71, 2\n"
    "s_lshl_b32 s69, s69, 2\n"
    "s_add_u32 s69, s69, 1\n"
    "s_lshl_b32 s69, s69, 19\n"
    "s_add_u32 m0, s74, 32768\n"
    "s_nop 0\n"
    "buffer_load_dwordx4 v20, s[44:47], s69 offen lds\n"
    "s_add_u32 m0, m0, 1024\n"
    "s_add_u32 s69, s69, 65536\n"
    "buffer_load_dwordx4 v21, s[44:47], s69 offen lds\n"
    "s_add_u32 m0, m0, 1024\n"
    "s_add_u32 s69, s69, 65536\n"
    "buffer_load_dwordx4 v20, s[44:47], s69 offen lds\n"
    "s_add_u32 m0, m0, 1024\n"
    "s_add_u32 s69, s69, 65536\n"
    "buffer_load_dwordx4 v21, s[44:47], s69 offen lds\n"
    "s_add_u32 m0, m0, 1024\n"
    "s_add_u32 s69, s69, 65536\n"
    "buffer_load_dwordx4 v20, s[44:47], s69 offen lds\n"
    "s_add_u32 m0, m0, 1024\n"
    "s_add_u32 s69, s69, 65536\n"
    "buffer_load_dwordx4 v21, s[44:47], s69 offen lds\n"
    "s_add_u32 m0, m0, 1024\n"
    "s_add_u32 s69, s69, 65536\n"
    "buffer_load_dwordx4 v20, s[44:47], s69 offen lds\n"
    "s_add_u32 m0, m0, 1024\n"
    "s_add_u32 s69, s69, 65536\n"
    "buffer_load_dwordx4 v21, s[44:47], s69 offen lds\n"
    "s_xor_b32 s69, s71, 3\n"
    "s_lshl_b32 s69, s69, 2\n"
    "s_add_u32 s69, s69, 1\n"
    "s_lshl_b32 s69, s69, 19\n"
    "s_add_u32 m0, s74, 49152\n"
    "s_nop 0\n"
    "buffer_load_dwordx4 v20, s[44:47], s69 offen lds\n"
    "s_add_u32 m0, m0, 1024\n"
    "s_add_u32 s69, s69, 65536\n"
    "buffer_load_dwordx4 v21, s[44:47], s69 offen lds\n"
    "s_add_u32 m0, m0, 1024\n"
    "s_add_u32 s69, s69, 65536\n"
    "buffer_load_dwordx4 v20, s[44:47], s69 offen lds\n"
    "s_add_u32 m0, m0, 1024\n"
    "s_add_u32 s69, s69, 65536\n"
    "buffer_load_dwordx4 v21, s[44:47], s69 offen lds\n"
    "s_add_u32 m0, m0, 1024\n"
    "s_add_u32 s69, s69, 65536\n"
    "buffer_load_dwordx4 v20, s[44:47], s69 offen lds\n"
    "s_add_u32 m0, m0, 1024\n"
    "s_add_u32 s69, s69, 65536\n"
    "buffer_load_dwordx4 v21, s[44:47], s69 offen lds\n"
    "s_add_u32 m0, m0, 1024\n"
    "s_add_u32 s69, s69, 65536\n"
    "buffer_load_dwordx4 v20, s[44:47], s69 offen lds\n"
    "s_add_u32 m0, m0, 1024\n"
    "s_add_u32 s69, s69, 65536\n"
    "buffer_load_dwordx4 v21, s[44:47], s69 offen lds\n"
    "s_waitcnt vmcnt(16)\n"
    "s_barrier\n"
    "v_readfirstlane_b32 s64, v56\n"
    "v_readfirstlane_b32 s65, v57\n"
    "s_nop 0\n"
    "s_mul_i32 s63, s65, 0x85ebca77\n"
    "s_mul_i32 s64, s64, 0x9e3779b1\n"
    "s_add_u32 s64, s64, s63\n"
    "s_add_u32 s64, s64, 0x7f4a7c15\n"
    "s_lshr_b32 s63, s64, 15\n"
    "s_xor_b32 s64, s64, s63\n"
    "s_mul_i32 s63, s64, 0x27d4eb2f\n"
    "s_mul_i32 s65, s65, 0xc2b2ae3d\n"
    "s_add_u32 s65, s65, s63\n"
    "s_add_u32 s65, s65, 0x165667b1\n"
    "s_lshr_b32 s63, s65, 13\n"
    "s_xor_b32 s65, s65, s63\n"
    "s_andn2_b32 s64, s64, 1\n"
    "v_mov_b32 v56, s64\n"
    "v_mov_b32 v57, s65\n"
    "buffer_store_dwordx2 v[56:57], v125, s[40:43], 0 offen sc1\n"
    "ds_read_b64_tr_b16 a[0:1], v22 offset:0\n"
    "ds_read_b64_tr_b16 a[2:3], v22 offset:256\n"
    "ds_read_b64_tr_b16 a[4:5], v22 offset:4096\n"
    "ds_read_b64_tr_b16 a[6:7], v22 offset:4352\n"
    "ds_read_b64_tr_b16 a[8:9], v23 offset:0\n"
    "ds_read_b64_tr_b16 a[10:11], v23 offset:256\n"
    "ds_read_b64_tr_b16 a[12:13], v23 offset:4096\n"
    "ds_read_b64_tr_b16 a[14:15], v23 offset:4352\n"
    "s_waitcnt lgkmcnt(0)\n"
    "ds_read_b64_tr_b16 a[16:17], v22 offset:8192\n"
    "ds_read_b64_tr_b16 a[18:19], v22 offset:8448\n"
    "ds_read_b64_tr_b16 a[20:21], v22 offset:12288\n"
    "ds_read_b64_tr_b16 a[22:23], v22 offset:12544\n"
    "ds_read_b64_tr_b16 a[24:25], v23 offset:8192\n"
    "ds_read_b64_tr_b16 a[26:27], v23 offset:8448\n"
    "ds_read_b64_tr_b16 a[28:29], v23 offset:12288\n"
    "ds_read_b64_tr_b16 a[30:31], v23 offset:12544\n"
    "s_waitcnt lgkmcnt(0)\n"
    "ds_read_b64_tr_b16 a[96:97], v24 offset:0\n"
    "ds_read_b64_tr_b16 a[98:99], v24 offset:256\n"
    "ds_read_b64_tr_b16 a[100:101], v24 offset:4096\n"
    "ds_read_b64_tr_b16 a[102:103], v24 offset:4352\n"
    "ds_read_b64_tr_b16 a[104:105], v25 offset:0\n"
    "ds_read_b64_tr_b16 a[106:107], v25 offset:256\n"
    "ds_read_b64_tr_b16 a[108:109], v25 offset:4096\n"
    "ds_read_b64_tr_b16 a[110:111], v25 offset:4352\n"
    "s_waitcnt lgkmcnt(0)\n"
    "ds_read_b64_tr_b16 a[112:113], v24 offset:8192\n"
    "ds_read_b64_tr_b16 a[114:115], v24 offset:8448\n"
    "ds_read_b64_tr_b16 a[116:117], v24 offset:12288\n"
    "ds_read_b64_tr_b16 a[118:119], v24 offset:12544\n"
    "ds_read_b64_tr_b16 a[120:121], v25 offset:8192\n"
    "ds_read_b64_tr_b16 a[122:123], v25 offset:8448\n"
    "ds_read_b64_tr_b16 a[124:125], v25 offset:12288\n"
    "ds_read_b64_tr_b16 a[126:127], v25 offset:12544\n"
    "s_waitcnt lgkmcnt(0)\n"
    "s_barrier\n"
    "s_xor_b32 s69, s71, 0\n"
    "s_lshl_b32 s69, s69, 2\n"
    "s_add_u32 s69, s69, 2\n"
    "s_lshl_b32 s69, s69, 19\n"
    "s_add_u32 m0, s74, 0\n"
    "s_nop 0\n"
    "buffer_load_dwordx4 v20, s[44:47], s69 offen lds\n"
    "s_add_u32 m0, m0, 1024\n"
    "s_add_u32 s69, s69, 65536\n"
    "buffer_load_dwordx4 v21, s[44:47], s69 offen lds\n"
    "s_add_u32 m0, m0, 1024\n"
    "s_add_u32 s69, s69, 65536\n"
    "buffer_load_dwordx4 v20, s[44:47], s69 offen lds\n"
    "s_add_u32 m0, m0, 1024\n"
    "s_add_u32 s69, s69, 65536\n"
    "buffer_load_dwordx4 v21, s[44:47], s69 offen lds\n"
    "s_add_u32 m0, m0, 1024\n"
    "s_add_u32 s69, s69, 65536\n"
    "buffer_load_dwordx4 v20, s[44:47], s69 offen lds\n"
    "s_add_u32 m0, m0, 1024\n"
    "s_add_u32 s69, s69, 65536\n"
    "buffer_load_dwordx4 v21, s[44:47], s69 offen lds\n"
    "s_add_u32 m0, m0, 1024\n"
    "s_add_u32 s69, s69, 65536\n"
    "buffer_load_dwordx4 v20, s[44:47], s69 offen lds\n"
    "s_add_u32 m0, m0, 1024\n"
    "s_add_u32 s69, s69, 65536\n"
    "buffer_load_dwordx4 v21, s[44:47], s69 offen lds\n"
    "s_xor_b32 s69, s71, 1\n"
    "s_lshl_b32 s69, s69, 2\n"
    "s_add_u32 s69, s69, 2\n"
    "s_lshl_b32 s69, s69, 19\n"
    "s_add_u32 m0, s74, 16384\n"
    "s_nop 0\n"
    "buffer_load_dwordx4 v20, s[44:47], s69 offen lds\n"
    "s_add_u32 m0, m0, 1024\n"
    "s_add_u32 s69, s69, 65536\n"
    "buffer_load_dwordx4 v21, s[44:47], s69 offen lds\n"
    "s_add_u32 m0, m0, 1024\n"
    "s_add_u32 s69, s69, 65536\n"
    "buffer_load_dwordx4 v20, s[44:47], s69 offen lds\n"
    "s_add_u32 m0, m0, 1024\n"
    "s_add_u32 s69, s69, 65536\n"
    "buffer_load_dwordx4 v21, s[44:47], s69 offen lds\n"
    "s_add_u32 m0, m0, 1024\n"
    "s_add_u32 s69, s69, 65536\n"
    "buffer_load_dwordx4 v20, s[44:47], s69 offen lds\n"
    "s_add_u32 m0, m0, 1024\n"
    "s_add_u32 s69, s69, 65536\n"
    "buffer_load_dwordx4 v21, s[44:47], s69 offen lds\n"
    "s_add_u32 m0, m0, 1024\n"
    "s_add_u32 s69, s69, 65536\n"
    "buffer_load_dwordx4 v20, s[44:47], s69 offen lds\n"
    "s_add_u32 m0, m0, 1024\n"
    "s_add_u32 s69, s69, 65536\n"
    "buffer_load_dwordx4 v21, s[44:47], s69 offen lds\n"
    "s_waitcnt vmcnt(16)\n"
    "s_barrier\n"
    "ds_read_b64_tr_b16 a[192:193], v22 offset:32768\n"
    "ds_read_b64_tr_b16 a[194:195], v22 offset:33024\n"
    "ds_read_b64_tr_b16 a[196:197], v22 offset:36864\n"
    "ds_read_b64_tr_b16 a[198:199], v22 offset:37120\n"
    "ds_read_b64_tr_b16 a[200:201], v23 offset:32768\n"
    "ds_read_b64_tr_b16 a[202:203], v23 offset:33024\n"
    "ds_read_b64_tr_b16 a[204:205], v23 offset:36864\n"
    "ds_read_b64_tr_b16 a[206:207], v23 offset:37120\n"
    "s_waitcnt lgkmcnt(0)\n"
    "ds_read_b64_tr_b16 a[208:209], v22 offset:40960\n"
    "ds_read_b64_tr_b16 a[210:211], v22 offset:41216\n"
    "ds_read_b64_tr_b16 a[212:213], v22 offset:45056\n"
    "ds_read_b64_tr_b16 a[214:215], v22 offset:45312\n"
    "ds_read_b64_tr_b16 a[216:217], v23 offset:40960\n"
    "ds_read_b64_tr_b16 a[218:219], v23 offset:41216\n"
    "ds_read_b64_tr_b16 a[220:221], v23 offset:45056\n"
    "ds_read_b64_tr_b16 a[222:223], v23 offset:45312\n"
    "s_waitcnt lgkmcnt(0)\n"
    "ds_read_b64_tr_b16 v[160:161], v24 offset:32768\n"
    "ds_read_b64_tr_b16 v[162:163], v24 offset:33024\n"
    "ds_read_b64_tr_b16 v[164:165], v24 offset:36864\n"
    "ds_read_b64_tr_b16 v[166:167], v24 offset:37120\n"
    "ds_read_b64_tr_b16 v[168:169], v25 offset:32768\n"
    "ds_read_b64_tr_b16 v[170:171], v25 offset:33024\n"
    "ds_read_b64_tr_b16 v[172:173], v25 offset:36864\n"
    "ds_read_b64_tr_b16 v[174:175], v25 offset:37120\n"
    "s_waitcnt lgkmcnt(0)\n"
    "ds_read_b64_tr_b16 v[176:177], v24 offset:40960\n"
    "ds_read_b64_tr_b16 v[178:179], v24 offset:41216\n"
    "ds_read_b64_tr_b16 v[180:181], v24 offset:45056\n"
    "ds_read_b64_tr_b16 v[182:183], v24 offset:45312\n"
    "ds_read_b64_tr_b16 v[184:185], v25 offset:40960\n"
    "ds_read_b64_tr_b16 v[186:187], v25 offset:41216\n"
    "ds_read_b64_tr_b16 v[188:189], v25 offset:45056\n"
    "ds_read_b64_tr_b16 v[190:191], v25 offset:45312\n"
    "s_waitcnt lgkmcnt(0)\n"
    "s_barrier\n"
    "s_xor_b32 s69, s71, 2\n"
    "s_lshl_b32 s69, s69, 2\n"
    "s_add_u32 s69, s69, 2\n"
    "s_lshl_b32 s69, s69, 19\n"
    "s_add_u32 m0, s74, 32768\n"
    "s_nop 0\n"
    "buffer_load_dwordx4 v20, s[44:47], s69 offen lds\n"
    "s_add_u32 m0, m0, 1024\n"
    "s_add_u32 s69, s69, 65536\n"
    "buffer_load_dwordx4 v21, s[44:47], s69 offen lds\n"
    "s_add_u32 m0, m0, 1024\n"
    "s_add_u32 s69, s69, 65536\n"
    "buffer_load_dwordx4 v20, s[44:47], s69 offen lds\n"
    "s_add_u32 m0, m0, 1024\n"
    "s_add_u32 s69, s69, 65536\n"
    "buffer_load_dwordx4 v21, s[44:47], s69 offen lds\n"
    "s_add_u32 m0, m0, 1024\n"
    "s_add_u32 s69, s69, 65536\n"
    "buffer_load_dwordx4 v20, s[44:47], s69 offen lds\n"
    "s_add_u32 m0, m0, 1024\n"
    "s_add_u32 s69, s69, 65536\n"
    "buffer_load_dwordx4 v21, s[44:47], s69 offen lds\n"
    "s_add_u32 m0, m0, 1024\n"
    "s_add_u32 s69, s69, 65536\n"
    "buffer_load_dwordx4 v20, s[44:47], s69 offen lds\n"
    "s_add_u32 m0, m0, 1024\n"
    "s_add_u32 s69, s69, 65536\n"
    "buffer_load_dwordx4 v21, s[44:47], s69 offen lds\n"
    "s_xor_b32 s69, s71, 3\n"
    "s_lshl_b32 s69, s69, 2\n"
    "s_add_u32 s69, s69, 2\n"
    "s_lshl_b32 s69, s69, 19\n"
    "s_add_u32 m0, s74, 49152\n"
    "s_nop 0\n"
    "buffer_load_dwordx4 v20, s[44:47], s69 offen lds\n"
    "s_add_u32 m0, m0, 1024\n"
    "s_add_u32 s69, s69, 65536\n"
    "buffer_load_dwordx4 v21, s[44:47], s69 offen lds\n"
    "s_add_u32 m0, m0, 1024\n"
    "s_add_u32 s69, s69, 65536\n"
    "buffer_load_dwordx4 v20, s[44:47], s69 offen lds\n"
    "s_add_u32 m0, m0, 1024\n"
    "s_add_u32 s69, s69, 65536\n"
    "buffer_load_dwordx4 v21, s[44:47], s69 offen lds\n"
    "s_add_u32 m0, m0, 1024\n"
    "s_add_u32 s69, s69, 65536\n"
    "buffer_load_dwordx4 v20, s[44:47], s69 offen lds\n"
    "s_add_u32 m0, m0, 1024\n"
    "s_add_u32 s69, s69, 65536\n"
    "buffer_load_dwordx4 v21, s[44:47], s69 offen lds\n"
    "s_add_u32 m0, m0, 1024\n"
    "s_add_u32 s69, s69, 65536\n"
    "buffer_load_dwordx4 v20, s[44:47], s69 offen lds\n"
    "s_add_u32 m0, m0, 1024\n"
    "s_add_u32 s69, s69, 65536\n"
    "buffer_load_dwordx4 v21, s[44:47], s69 offen lds\n"
    "s_waitcnt vmcnt(16)\n"
    "s_barrier\n"
    "ds_read_b64_tr_b16 a[32:33], v22 offset:0\n"
    "ds_read_b64_tr_b16 a[34:35], v22 offset:256\n"
    "ds_read_b64_tr_b16 a[36:37], v22 offset:4096\n"
    "ds_read_b64_tr_b16 a[38:39], v22 offset:4352\n"
    "ds_read_b64_tr_b16 a[40:41], v23 offset:0\n"
    "ds_read_b64_tr_b16 a[42:43], v23 offset:256\n"
    "ds_read_b64_tr_b16 a[44:45], v23 offset:4096\n"
    "ds_read_b64_tr_b16 a[46:47], v23 offset:4352\n"
    "s_waitcnt lgkmcnt(0)\n"
    "ds_read_b64_tr_b16 a[48:49], v22 offset:8192\n"
    "ds_read_b64_tr_b16 a[50:51], v22 offset:8448\n"
    "ds_read_b64_tr_b16 a[52:53], v22 offset:12288\n"
    "ds_read_b64_tr_b16 a[54:55], v22 offset:12544\n"
    "ds_read_b64_tr_b16 a[56:57], v23 offset:8192\n"
    "ds_read_b64_tr_b16 a[58:59], v23 offset:8448\n"
    "ds_read_b64_tr_b16 a[60:61], v23 offset:12288\n"
    "ds_read_b64_tr_b16 a[62:63], v23 offset:12544\n"
    "s_waitcnt lgkmcnt(0)\n"
    "ds_read_b64_tr_b16 a[128:129], v24 offset:0\n"
    "ds_read_b64_tr_b16 a[130:131], v24 offset:256\n"
    "ds_read_b64_tr_b16 a[132:133], v24 offset:4096\n"
    "ds_read_b64_tr_b16 a[134:135], v24 offset:4352\n"
    "ds_read_b64_tr_b16 a[136:137], v25 offset:0\n"
    "ds_read_b64_tr_b16 a[138:139], v25 offset:256\n"
    "ds_read_b64_tr_b16 a[140:141], v25 offset:4096\n"
    "ds_read_b64_tr_b16 a[142:143], v25 offset:4352\n"
    "s_waitcnt lgkmcnt(0)\n"
    "ds_read_b64_tr_b16 a[144:145], v24 offset:8192\n"
    "ds_read_b64_tr_b16 a[146:147], v24 offset:8448\n"
    "ds_read_b64_tr_b16 a[148:149], v24 offset:12288\n"
    "ds_read_b64_tr_b16 a[150:151], v24 offset:12544\n"
    "ds_read_b64_tr_b16 a[152:153], v25 offset:8192\n"
    "ds_read_b64_tr_b16 a[154:155], v25 offset:8448\n"
    "ds_read_b64_tr_b16 a[156:157], v25 offset:12288\n"
    "ds_read_b64_tr_b16 a[158:159], v25 offset:12544\n"
    "s_waitcnt lgkmcnt(0)\n"
    "s_barrier\n"
    "s_xor_b32 s69, s71, 0\n"
    "s_lshl_b32 s69, s69, 2\n"
    "s_add_u32 s69, s69, 3\n"
    "s_lshl_b32 s69, s69, 19\n"
    "s_add_u32 m0, s74, 0\n"
    "s_nop 0\n"
    "buffer_load_dwordx4 v20, s[44:47], s69 offen lds\n"
    "s_add_u32 m0, m0, 1024\n"
    "s_add_u32 s69, s69, 65536\n"
    "buffer_load_dwordx4 v21, s[44:47], s69 offen lds\n"
    "s_add_u32 m0, m0, 1024\n"
    "s_add_u32 s69, s69, 65536\n"
    "buffer_load_dwordx4 v20, s[44:47], s69 offen lds\n"
    "s_add_u32 m0, m0, 1024\n"
    "s_add_u32 s69, s69, 65536\n"
    "buffer_load_dwordx4 v21, s[44:47], s69 offen lds\n"
    "s_add_u32 m0, m0, 1024\n"
    "s_add_u32 s69, s69, 65536\n"
    "buffer_load_dwordx4 v20, s[44:47], s69 offen lds\n"
    "s_add_u32 m0, m0, 1024\n"
    "s_add_u32 s69, s69, 65536\n"
    "buffer_load_dwordx4 v21, s[44:47], s69 offen lds\n"
    "s_add_u32 m0, m0, 1024\n"
    "s_add_u32 s69, s69, 65536\n"
    "buffer_load_dwordx4 v20, s[44:47], s69 offen lds\n"
    "s_add_u32 m0, m0, 1024\n"
    "s_add_u32 s69, s69, 65536\n"
    "buffer_load_dwordx4 v21, s[44:47], s69 offen lds\n"
    "s_xor_b32 s69, s71, 1\n"
    "s_lshl_b32 s69, s69, 2\n"
    "s_add_u32 s69, s69, 3\n"
    "s_lshl_b32 s69, s69, 19\n"
    "s_add_u32 m0, s74, 16384\n"
    "s_nop 0\n"
    "buffer_load_dwordx4 v20, s[44:47], s69 offen lds\n"
    "s_add_u32 m0, m0, 1024\n"
    "s_add_u32 s69, s69, 65536\n"
    "buffer_load_dwordx4 v21, s[44:47], s69 offen lds\n"
    "s_add_u32 m0, m0, 1024\n"
    "s_add_u32 s69, s69, 65536\n"
    "buffer_load_dwordx4 v20, s[44:47], s69 offen lds\n"
    "s_add_u32 m0, m0, 1024\n"
    "s_add_u32 s69, s69, 65536\n"
    "buffer_load_dwordx4 v21, s[44:47], s69 offen lds\n"
    "s_add_u32 m0, m0, 1024\n"
    "s_add_u32 s69, s69, 65536\n"
    "buffer_load_dwordx4 v20, s[44:47], s69 offen lds\n"
    "s_add_u32 m0, m0, 1024\n"
    "s_add_u32 s69, s69, 65536\n"
    "buffer_load_dwordx4 v21, s[44:47], s69 offen lds\n"
    "s_add_u32 m0, m0, 1024\n"
    "s_add_u32 s69, s69, 65536\n"
    "buffer_load_dwordx4 v20, s[44:47], s69 offen lds\n"
    "s_add_u32 m0, m0, 1024\n"
    "s_add_u32 s69, s69, 65536\n"
    "buffer_load_dwordx4 v21, s[44:47], s69 offen lds\n"
    "s_waitcnt vmcnt(16)\n"
    "s_barrier\n"
    "ds_read_b64_tr_b16 a[224:225], v22 offset:32768\n"
    "ds_read_b64_tr_b16 a[226:227], v22 offset:33024\n"
    "ds_read_b64_tr_b16 a[228:229], v22 offset:36864\n"
    "ds_read_b64_tr_b16 a[230:231], v22 offset:37120\n"
    "ds_read_b64_tr_b16 a[232:233], v23 offset:32768\n"
    "ds_read_b64_tr_b16 a[234:235], v23 offset:33024\n"
    "ds_read_b64_tr_b16 a[236:237], v23 offset:36864\n"
    "ds_read_b64_tr_b16 a[238:239], v23 offset:37120\n"
    "s_waitcnt lgkmcnt(0)\n"
    "ds_read_b64_tr_b16 a[240:241], v22 offset:40960\n"
    "ds_read_b64_tr_b16 a[242:243], v22 offset:41216\n"
    "ds_read_b64_tr_b16 a[244:245], v22 offset:45056\n"
    "ds_read_b64_tr_b16 a[246:247], v22 offset:45312\n"
    "ds_read_b64_tr_b16 a[248:249], v23 offset:40960\n"
    "ds_read_b64_tr_b16 a[250:251], v23 offset:41216\n"
    "ds_read_b64_tr_b16 a[252:253], v23 offset:45056\n"
    "ds_read_b64_tr_b16 a[254:255], v23 offset:45312\n"
    "s_waitcnt lgkmcnt(0)\n"
    "ds_read_b64_tr_b16 v[192:193], v24 offset:32768\n"
    "ds_read_b64_tr_b16 v[194:195], v24 offset:33024\n"
    "ds_read_b64_tr_b16 v[196:197], v24 offset:36864\n"
    "ds_read_b64_tr_b16 v[198:199], v24 offset:37120\n"
    "ds_read_b64_tr_b16 v[200:201], v25 offset:32768\n"
    "ds_read_b64_tr_b16 v[202:203], v25 offset:33024\n"
    "ds_read_b64_tr_b16 v[204:205], v25 offset:36864\n"
    "ds_read_b64_tr_b16 v[206:207], v25 offset:37120\n"
    "s_waitcnt lgkmcnt(0)\n"
    "ds_read_b64_tr_b16 v[208:209], v24 offset:40960\n"
    "ds_read_b64_tr_b16 v[210:211], v24 offset:41216\n"
    "ds_read_b64_tr_b16 v[212:213], v24 offset:45056\n"
    "ds_read_b64_tr_b16 v[214:215], v24 offset:45312\n"
    "ds_read_b64_tr_b16 v[216:217], v25 offset:40960\n"
    "ds_read_b64_tr_b16 v[218:219], v25 offset:41216\n"
    "ds_read_b64_tr_b16 v[220:221], v25 offset:45056\n"
    "ds_read_b64_tr_b16 v[222:223], v25 offset:45312\n"
    "s_waitcnt lgkmcnt(0)\n"
    "s_barrier\n"
    "s_xor_b32 s69, s71, 2\n"
    "s_lshl_b32 s69, s69, 2\n"
    "s_add_u32 s69, s69, 3\n"
    "s_lshl_b32 s69, s69, 19\n"
    "s_add_u32 m0, s74, 32768\n"
    "s_nop 0\n"
    "buffer_load_dwordx4 v20, s[44:47], s69 offen lds\n"
    "s_add_u32 m0, m0, 1024\n"
    "s_add_u32 s69, s69, 65536\n"
    "buffer_load_dwordx4 v21, s[44:47], s69 offen lds\n"
    "s_add_u32 m0, m0, 1024\n"
    "s_add_u32 s69, s69, 65536\n"
    "buffer_load_dwordx4 v20, s[44:47], s69 offen lds\n"
    "s_add_u32 m0, m0, 1024\n"
    "s_add_u32 s69, s69, 65536\n"
    "buffer_load_dwordx4 v21, s[44:47], s69 offen lds\n"
    "s_add_u32 m0, m0, 1024\n"
    "s_add_u32 s69, s69, 65536\n"
    "buffer_load_dwordx4 v20, s[44:47], s69 offen lds\n"
    "s_add_u32 m0, m0, 1024\n"
    "s_add_u32 s69, s69, 65536\n"
    "buffer_load_dwordx4 v21, s[44:47], s69 offen lds\n"
    "s_add_u32 m0, m0, 1024\n"
    "s_add_u32 s69, s69, 65536\n"
    "buffer_load_dwordx4 v20, s[44:47], s69 offen lds\n"
    "s_add_u32 m0, m0, 1024\n"
    "s_add_u32 s69, s69, 65536\n"
    "buffer_load_dwordx4 v21, s[44:47], s69 offen lds\n"
    "s_xor_b32 s69, s71, 3\n"
    "s_lshl_b32 s69, s69, 2\n"
    "s_add_u32 s69, s69, 3\n"
    "s_lshl_b32 s69, s69, 19\n"
    "s_add_u32 m0, s74, 49152\n"
    "s_nop 0\n"
    "buffer_load_dwordx4 v20, s[44:47], s69 offen lds\n"
    "s_add_u32 m0, m0, 1024\n"
    "s_add_u32 s69, s69, 65536\n"
    "buffer_load_dwordx4 v21, s[44:47], s69 offen lds\n"
    "s_add_u32 m0, m0, 1024\n"
    "s_add_u32 s69, s69, 65536\n"
    "buffer_load_dwordx4 v20, s[44:47], s69 offen lds\n"
    "s_add_u32 m0, m0, 1024\n"
    "s_add_u32 s69, s69, 65536\n"
    "buffer_load_dwordx4 v21, s[44:47], s69 offen lds\n"
    "s_add_u32 m0, m0, 1024\n"
    "s_add_u32 s69, s69, 65536\n"
    "buffer_load_dwordx4 v20, s[44:47], s69 offen lds\n"
    "s_add_u32 m0, m0, 1024\n"
    "s_add_u32 s69, s69, 65536\n"
    "buffer_load_dwordx4 v21, s[44:47], s69 offen lds\n"
    "s_add_u32 m0, m0, 1024\n"
    "s_add_u32 s69, s69, 65536\n"
    "buffer_load_dwordx4 v20, s[44:47], s69 offen lds\n"
    "s_add_u32 m0, m0, 1024\n"
    "s_add_u32 s69, s69, 65536\n"
    "buffer_load_dwordx4 v21, s[44:47], s69 offen lds\n"
    "s_waitcnt vmcnt(16)\n"
    "s_barrier\n"
    "ds_read_b64_tr_b16 a[64:65], v22 offset:0\n"
    "ds_read_b64_tr_b16 a[66:67], v22 offset:256\n"
    "ds_read_b64_tr_b16 a[68:69], v22 offset:4096\n"
    "ds_read_b64_tr_b16 a[70:71], v22 offset:4352\n"
    "ds_read_b64_tr_b16 a[72:73], v23 offset:0\n"
    "ds_read_b64_tr_b16 a[74:75], v23 offset:256\n"
    "ds_read_b64_tr_b16 a[76:77], v23 offset:4096\n"
    "ds_read_b64_tr_b16 a[78:79], v23 offset:4352\n"
    "s_waitcnt lgkmcnt(0)\n"
    "ds_read_b64_tr_b16 a[80:81], v22 offset:8192\n"
    "ds_read_b64_tr_b16 a[82:83], v22 offset:8448\n"
    "ds_read_b64_tr_b16 a[84:85], v22 offset:12288\n"
    "ds_read_b64_tr_b16 a[86:87], v22 offset:12544\n"
    "ds_read_b64_tr_b16 a[88:89], v23 offset:8192\n"
    "ds_read_b64_tr_b16 a[90:91], v23 offset:8448\n"
    "ds_read_b64_tr_b16 a[92:93], v23 offset:12288\n"
    "ds_read_b64_tr_b16 a[94:95], v23 offset:12544\n"
    "s_waitcnt lgkmcnt(0)\n"
    "ds_read_b64_tr_b16 a[160:161], v24 offset:0\n"
    "ds_read_b64_tr_b16 a[162:163], v24 offset:256\n"
    "ds_read_b64_tr_b16 a[164:165], v24 offset:4096\n"
    "ds_read_b64_tr_b16 a[166:167], v24 offset:4352\n"
    "ds_read_b64_tr_b16 a[168:169], v25 offset:0\n"
    "ds_read_b64_tr_b16 a[170:171], v25 offset:256\n"
    "ds_read_b64_tr_b16 a[172:173], v25 offset:4096\n"
    "ds_read_b64_tr_b16 a[174:175], v25 offset:4352\n"
    "s_waitcnt lgkmcnt(0)\n"
    "ds_read_b64_tr_b16 a[176:177], v24 offset:8192\n"
    "ds_read_b64_tr_b16 a[178:179], v24 offset:8448\n"
    "ds_read_b64_tr_b16 a[180:181], v24 offset:12288\n"
    "ds_read_b64_tr_b16 a[182:183], v24 offset:12544\n"
    "ds_read_b64_tr_b16 a[184:185], v25 offset:8192\n"
    "ds_read_b64_tr_b16 a[186:187], v25 offset:8448\n"
    "ds_read_b64_tr_b16 a[188:189], v25 offset:12288\n"
    "ds_read_b64_tr_b16 a[190:191], v25 offset:12544\n"
    "s_waitcnt lgkmcnt(0)\n"
    "s_barrier\n"
    "s_xor_b32 s69, s71, 0\n"
    "s_lshl_b32 s69, s69, 2\n"
    "s_add_u32 s69, s69, 0\n"
    "s_lshl_b32 s69, s69, 19\n"
    "s_add_u32 m0, s74, 0\n"
    "s_nop 0\n"
    "buffer_load_dwordx4 v20, s[44:47], s69 offen lds\n"
    "s_add_u32 m0, m0, 1024\n"
    "s_add_u32 s69, s69, 65536\n"
    "buffer_load_dwordx4 v21, s[44:47], s69 offen lds\n"
    "s_add_u32 m0, m0, 1024\n"
    "s_add_u32 s69, s69, 65536\n"
    "buffer_load_dwordx4 v20, s[44:47], s69 offen lds\n"
    "s_add_u32 m0, m0, 1024\n"
    "s_add_u32 s69, s69, 65536\n"
    "buffer_load_dwordx4 v21, s[44:47], s69 offen lds\n"
    "s_add_u32 m0, m0, 1024\n"
    "s_add_u32 s69, s69, 65536\n"
    "buffer_load_dwordx4 v20, s[44:47], s69 offen lds\n"
    "s_add_u32 m0, m0, 1024\n"
    "s_add_u32 s69, s69, 65536\n"
    "buffer_load_dwordx4 v21, s[44:47], s69 offen lds\n"
    "s_add_u32 m0, m0, 1024\n"
    "s_add_u32 s69, s69, 65536\n"
    "buffer_load_dwordx4 v20, s[44:47], s69 offen lds\n"
    "s_add_u32 m0, m0, 1024\n"
    "s_add_u32 s69, s69, 65536\n"
    "buffer_load_dwordx4 v21, s[44:47], s69 offen lds\n"
    "s_xor_b32 s69, s71, 1\n"
    "s_lshl_b32 s69, s69, 2\n"
    "s_add_u32 s69, s69, 0\n"
    "s_lshl_b32 s69, s69, 19\n"
    "s_add_u32 m0, s74, 16384\n"
    "s_nop 0\n"
    "buffer_load_dwordx4 v20, s[44:47], s69 offen lds\n"
    "s_add_u32 m0, m0, 1024\n"
    "s_add_u32 s69, s69, 65536\n"
    "buffer_load_dwordx4 v21, s[44:47], s69 offen lds\n"
    "s_add_u32 m0, m0, 1024\n"
    "s_add_u32 s69, s69, 65536\n"
    "buffer_load_dwordx4 v20, s[44:47], s69 offen lds\n"
    "s_add_u32 m0, m0, 1024\n"
    "s_add_u32 s69, s69, 65536\n"
    "buffer_load_dwordx4 v21, s[44:47], s69 offen lds\n"
    "s_add_u32 m0, m0, 1024\n"
    "s_add_u32 s69, s69, 65536\n"
    "buffer_load_dwordx4 v20, s[44:47], s69 offen lds\n"
    "s_add_u32 m0, m0, 1024\n"
    "s_add_u32 s69, s69, 65536\n"
    "buffer_load_dwordx4 v21, s[44:47], s69 offen lds\n"
    "s_add_u32 m0, m0, 1024\n"
    "s_add_u32 s69, s69, 65536\n"
    "buffer_load_dwordx4 v20, s[44:47], s69 offen lds\n"
    "s_add_u32 m0, m0, 1024\n"
    "s_add_u32 s69, s69, 65536\n"
    "buffer_load_dwordx4 v21, s[44:47], s69 offen lds\n"
    "s_waitcnt vmcnt(16)\n"
    "s_barrier\n"
    "ds_read_b64_tr_b16 v[128:129], v22 offset:32768\n"
    "ds_read_b64_tr_b16 v[130:131], v22 offset:33024\n"
    "ds_read_b64_tr_b16 v[132:133], v22 offset:36864\n"
    "ds_read_b64_tr_b16 v[134:135], v22 offset:37120\n"
    "ds_read_b64_tr_b16 v[136:137], v23 offset:32768\n"
    "ds_read_b64_tr_b16 v[138:139], v23 offset:33024\n"
    "ds_read_b64_tr_b16 v[140:141], v23 offset:36864\n"
    "ds_read_b64_tr_b16 v[142:143], v23 offset:37120\n"
    "s_waitcnt lgkmcnt(0)\n"
    "ds_read_b64_tr_b16 v[144:145], v22 offset:40960\n"
    "ds_read_b64_tr_b16 v[146:147], v22 offset:41216\n"
    "ds_read_b64_tr_b16 v[148:149], v22 offset:45056\n"
    "ds_read_b64_tr_b16 v[150:151], v22 offset:45312\n"
    "ds_read_b64_tr_b16 v[152:153], v23 offset:40960\n"
    "ds_read_b64_tr_b16 v[154:155], v23 offset:41216\n"
    "ds_read_b64_tr_b16 v[156:157], v23 offset:45056\n"
    "ds_read_b64_tr_b16 v[158:159], v23 offset:45312\n"
    "s_waitcnt lgkmcnt(0)\n"
    "ds_read_b64_tr_b16 v[224:225], v24 offset:32768\n"
    "ds_read_b64_tr_b16 v[226:227], v24 offset:33024\n"
    "ds_read_b64_tr_b16 v[228:229], v24 offset:36864\n"
    "ds_read_b64_tr_b16 v[230:231], v24 offset:37120\n"
    "ds_read_b64_tr_b16 v[232:233], v25 offset:32768\n"
    "ds_read_b64_tr_b16 v[234:235], v25 offset:33024\n"
    "ds_read_b64_tr_b16 v[236:237], v25 offset:36864\n"
    "ds_read_b64_tr_b16 v[238:239], v25 offset:37120\n"
    "s_waitcnt lgkmcnt(0)\n"
    "ds_read_b64_tr_b16 v[240:241], v24 offset:40960\n"
    "ds_read_b64_tr_b16 v[242:243], v24 offset:41216\n"
    "ds_read_b64_tr_b16 v[244:245], v24 offset:45056\n"
    "ds_read_b64_tr_b16 v[246:247], v24 offset:45312\n"
    "ds_read_b64_tr_b16 v[248:249], v25 offset:40960\n"
    "ds_read_b64_tr_b16 v[250:251], v25 offset:41216\n"
    "ds_read_b64_tr_b16 v[252:253], v25 offset:45056\n"
    "ds_read_b64_tr_b16 v[254:255], v25 offset:45312\n"
    "s_waitcnt lgkmcnt(0)\n"
    "s_barrier\n"
    "v_mbcnt_lo_u32_b32 v32, -1, 0\n"
    "v_mbcnt_hi_u32_b32 v32, -1, v32\n"
    "v_min_u32 v32, 48, v32\n"
    "v_lshlrev_b32 v32, 4, v32\n"
    "v_add_u32 v32, 1573376, v32\n"
    "buffer_load_dwordx4 v[28:31], v32, s[40:43], 0 offen sc1\n"
    "s_getpc_b64 s[100:101]\n"
    ".LA_tc%=:\n"
    "v_mbcnt_lo_u32_b32 v33, -1, 0\n"
    "v_mbcnt_hi_u32_b32 v33, -1, v33\n"
    "v_lshlrev_b32 v33, 7, v33\n"
    "v_min_u32 v33, .LA_fin%=-.LA_tc%=, v33\n"
    "global_load_dword v33, v33, s[100:101]\n"
    "s_xor_b32 s69, s71, 2\n"
    "s_lshl_b32 s69, s69, 2\n"
    "s_add_u32 s69, s69, 0\n"
    "s_lshl_b32 s69, s69, 19\n"
    "s_add_u32 m0, s74, 32768\n"
    "s_nop 0\n"
    "buffer_load_dwordx4 v20, s[44:47], s69 offen lds\n"
    "s_add_u32 m0, m0, 1024\n"
    "s_add_u32 s69, s69, 65536\n"
    "buffer_load_dwordx4 v21, s[44:47], s69 offen lds\n"
    "s_add_u32 m0, m0, 1024\n"
    "s_add_u32 s69, s69, 65536\n"
    "buffer_load_dwordx4 v20, s[44:47], s69 offen lds\n"
    "s_add_u32 m0, m0, 1024\n"
    "s_add_u32 s69, s69, 65536\n"
    "buffer_load_dwordx4 v21, s[44:47], s69 offen lds\n"
    "s_add_u32 m0, m0, 1024\n"
    "s_add_u32 s69, s69, 65536\n"
    "buffer_load_dwordx4 v20, s[44:47], s69 offen lds\n"
    "s_add_u32 m0, m0, 1024\n"
    "s_add_u32 s69, s69, 65536\n"
    "buffer_load_dwordx4 v21, s[44:47], s69 offen lds\n"
    "s_add_u32 m0, m0, 1024\n"
    "s_add_u32 s69, s69, 65536\n"
    "buffer_load_dwordx4 v20, s[44:47], s69 offen lds\n"
    "s_add_u32 m0, m0, 1024\n"
    "s_add_u32 s69, s69, 65536\n"
    "buffer_load_dwordx4 v21, s[44:47], s69 offen lds\n"
    "s_xor_b32 s69, s71, 3\n"
    "s_lshl_b32 s69, s69, 2\n"
    "s_add_u32 s69, s69, 0\n"
    "s_lshl_b32 s69, s69, 19\n"
    "s_add_u32 m0, s74, 49152\n"
    "s_nop 0\n"
    "buffer_load_dwordx4 v20, s[44:47], s69 offen lds\n"
    "s_add_u32 m0, m0, 1024\n"
    "s_add_u32 s69, s69, 65536\n"
    "buffer_load_dwordx4 v21, s[44:47], s69 offen lds\n"
    "s_add_u32 m0, m0, 1024\n"
    "s_add_u32 s69, s69, 65536\n"
    "buffer_load_dwordx4 v20, s[44:47], s69 offen lds\n"
    "s_add_u32 m0, m0, 1024\n"
    "s_add_u32 s69, s69, 65536\n"
    "buffer_load_dwordx4 v21, s[44:47], s69 offen lds\n"
    "s_add_u32 m0, m0, 1024\n"
    "s_add_u32 s69, s69, 65536\n"
    "buffer_load_dwordx4 v20, s[44:47], s69 offen lds\n"
    "s_add_u32 m0, m0, 1024\n"
    "s_add_u32 s69, s69, 65536\n"
    "buffer_load_dwordx4 v21, s[44:47], s69 offen lds\n"
    "s_add_u32 m0, m0, 1024\n"
    "s_add_u32 s69, s69, 65536\n"
    "buffer_load_dwordx4 v20, s[44:47], s69 offen lds\n"
    "s_add_u32 m0, m0, 1024\n"
    "s_add_u32 s69, s69, 65536\n"
    "buffer_load_dwordx4 v21, s[44:47], s69 offen lds\n"
    "s_waitcnt vmcnt(16)\n"
    "s_barrier\n"
    "s_mov_b32 s68, 0\n"
    ".LA_rdy%=:\n"
    "v_readlane_b32 s69, v30, 48\n"
    "v_xor_b32 v28, s64, v28\n"
    "v_xor_b32 v29, s65, v29\n"
    "v_xor_b32 v30, s64, v30\n"
    "v_xor_b32 v31, s65, v31\n"
    "v_or_b32 v28, v28, v30\n"
    "v_lshrrev_b32 v28, 1, v28\n"
    "v_or3_b32 v28, v28, v29, v31\n"
    "v_cmp_ne_u32 vcc, 0, v28\n"
    "s_cbranch_vccz .LA_rdyok%=\n"
    "buffer_load_dwordx4 v[28:31], v32, s[40:43], 0 offen sc1\n"
    "s_waitcnt vmcnt(0)\n"
    "s_add_u32 s68, s68, 1\n"
    "s_cmp_lt_u32 s68, 65536\n"
    "s_cbranch_scc1 .LA_rdy%=\n"
    "s_branch .LA_tmo%=\n"
    ".LA_rdyok%=:\n"
    "s_and_b32 s69, s69, 1\n"
    "s_lshl_b32 s61, s69, 13\n"
    "s_add_u32 s64, s61, 327680\n"
    "s_add_u32 s63, s61, 655360\n"
    "s_mov_b32 s60, 0\n"
    "s_xor_b32 s69, s56, 0\n"
    "s_and_b32 s71, s69, 1\n"
    "s_lshl_b32 s71, s71, 12\n"
    "s_lshr_b32 s69, s69, 1\n"
    "s_lshl_b32 s69, s69, 14\n"
    "s_add_u32 s69, s69, s71\n"
    "s_add_u32 s34, s69, s61\n"
    "s_xor_b32 s69, s56, 1\n"
    "s_and_b32 s71, s69, 1\n"
    "s_lshl_b32 s71, s71, 12\n"
    "s_lshr_b32 s69, s69, 1\n"
    "s_lshl_b32 s69, s69, 14\n"
    "s_add_u32 s69, s69, s71\n"
    "s_add_u32 s35, s69, s61\n"
    "s_xor_b32 s69, s56, 2\n"
    "s_and_b32 s71, s69, 1\n"
    "s_lshl_b32 s71, s71, 12\n"
    "s_lshr_b32 s69, s69, 1\n"
    "s_lshl_b32 s69, s69, 14\n"
    "s_add_u32 s69, s69, s71\n"
    "s_add_u32 s36, s69, s61\n"
    "s_xor_b32 s69, s56, 3\n"
    "s_and_b32 s71, s69, 1\n"
    "s_lshl_b32 s71, s71, 12\n"
    "s_lshr_b32 s69, s69, 1\n"
    "s_lshl_b32 s69, s69, 14\n"
    "s_add_u32 s69, s69, s71\n"
    "s_add_u32 s37, s69, s61\n"
    "v_readlane_b32 s32, v15, 0\n"
    "s_nop 0\n"
    "s_add_u32 s32, s32, s61\n"
    "v_writelane_b32 v15, s32, 0\n"
    "s_add_u32 s39, s61, 1048592\n"
    "s_lshl_b32 s69, s72, 3\n"
    "s_sub_u32 s39, s39, s69\n"
    "s_add_u32 s48, s40, s39\n"
    "s_addc_u32 s49, s41, 0x80000\n"
    "s_mov_b32 s51, s43\n"
    "s_add_u32 s50, s72, 1024\n"
    "s_cmp_eq_u32 s56, 3\n"
    "s_cselect_b32 s50, s50, 0\n"
    "s_memrealtime s[100:101]\n"
    "s_waitcnt lgkmcnt(0)\n"
    "s_lshl_b32 s100, s100, 3\n"
    "s_sub_u32 s59, s100, 0x400000\n"
    "s_mov_b32 s75, 600\n"
    "s_mov_b32 s62, 1040\n"
    "s_mov_b32 s65, 0\n"
    "s_mov_b32 s67, 0\n"
    "s_mov_b32 s70, 0x3e0293ee\n"
    "s_mov_b32 s73, 0\n"
    "s_mov_b64 s[44:45], s[40:41]\n"
    "s_lshl_b32 s69, s56, 11\n"
    "s_add_u32 m0, s69, 151552\n"
    "ds_read_b64_tr_b16 v[60:61], v22 offset:0\n"
    "ds_read_b64_tr_b16 v[62:63], v22 offset:256\n"
    "ds_read_b64_tr_b16 v[64:65], v22 offset:4096\n"
    "ds_read_b64_tr_b16 v[66:67], v22 offset:4352\n"
    "ds_read_b64_tr_b16 v[68:69], v23 offset:0\n"
    "ds_read_b64_tr_b16 v[70:71], v23 offset:256\n"
    "ds_read_b64_tr_b16 v[72:73], v23 offset:4096\n"
    "ds_read_b64_tr_b16 v[74:75], v23 offset:4352\n"
    "s_waitcnt lgkmcnt(0)\n"
    "ds_read_b64_tr_b16 v[76:77], v22 offset:8192\n"
    "ds_read_b64_tr_b16 v[78:79], v22 offset:8448\n"
    "ds_read_b64_tr_b16 v[80:81], v22 offset:12288\n"
    "ds_read_b64_tr_b16 v[82:83], v22 offset:12544\n"
    "ds_read_b64_tr_b16 v[84:85], v23 offset:8192\n"
    "ds_read_b64_tr_b16 v[86:87], v23 offset:8448\n"
    "ds_read_b64_tr_b16 v[88:89], v23 offset:12288\n"
    "ds_read_b64_tr_b16 v[90:91], v23 offset:12544\n"
    "s_waitcnt lgkmcnt(0)\n"
    "ds_read_b64_tr_b16 v[92:93], v24 offset:0\n"
    "ds_read_b64_tr_b16 v[94:95], v24 offset:256\n"
    "ds_read_b64_tr_b16 v[96:97], v24 offset:4096\n"
    "ds_read_b64_tr_b16 v[98:99], v24 offset:4352\n"
    "ds_read_b64_tr_b16 v[100:101], v25 offset:0\n"
    "ds_read_b64_tr_b16 v[102:103], v25 offset:256\n"
    "ds_read_b64_tr_b16 v[104:105], v25 offset:4096\n"
    "ds_read_b64_tr_b16 v[106:107], v25 offset:4352\n"
    "s_waitcnt lgkmcnt(0)\n"
    "ds_read_b64_tr_b16 v[108:109], v24 offset:8192\n"
    "ds_read_b64_tr_b16 v[110:111], v24 offset:8448\n"
    "ds_read_b64_tr_b16 v[112:113], v24 offset:12288\n"
    "ds_read_b64_tr_b16 v[114:115], v24 offset:12544\n"
    "ds_read_b64_tr_b16 v[116:117], v25 offset:8192\n"
    "ds_read_b64_tr_b16 v[118:119], v25 offset:8448\n"
    "ds_read_b64_tr_b16 v[120:121], v25 offset:12288\n"
    "ds_read_b64_tr_b16 v[122:123], v25 offset:12544\n"
    "s_waitcnt lgkmcnt(0)\n"
    "s_barrier\n"
    "ds_write_b128 v12, v[60:63] offset:0\n"
    "ds_write_b128 v12, v[64:67] offset:1024\n"
    "ds_write_b128 v12, v[68:71] offset:2048\n"
    "ds_write_b128 v12, v[72:75] offset:3072\n"
    "ds_write_b128 v12, v[76:79] offset:4096\n"
    "ds_write_b128 v12, v[80:83] offset:5120\n"
    "ds_write_b128 v12, v[84:87] offset:6144\n"
    "ds_write_b128 v12, v[88:91] offset:7168\n"
    "s_waitcnt lgkmcnt(0)\n"
    "ds_write_b128 v12, v[92:95] offset:8192\n"
    "ds_write_b128 v12, v[96:99] offset:9216\n"
    "ds_write_b128 v12, v[100:103] offset:10240\n"
    "ds_write_b128 v12, v[104:107] offset:11264\n"
    "ds_write_b128 v12, v[108:111] offset:12288\n"
    "ds_write_b128 v12, v[112:115] offset:13312\n"
    "ds_write_b128 v12, v[116:119] offset:14336\n"
    "ds_write_b128 v12, v[120:123] offset:15360\n"
    "s_waitcnt lgkmcnt(0)\n"
    "buffer_load_dword v35, v16, s[40:43], s73 offen sc1\n"
    "s_waitcnt vmcnt(0)\n"
    "s_barrier\n"
    "v_cmp_gt_u32 vcc, 2, v35\n"
    "s_cbranch_vccnz .LA_nkv%=\n"
    "buffer_load_dwordx4 v2, s[40:43], s65 offen offset:0 sc1 lds\n"
    "buffer_load_dwordx4 v2, s[40:43], s65 offen offset:1024 sc1 lds\n"
    "buffer_load_dwordx4 v[52:55], v3, s[40:43], s65 offen offset:0 sc1\n"
    "buffer_load_dwordx4 v[56:59], v3, s[40:43], s65 offen offset:1024 sc1\n"
    ".LA_nkv%=:\n"
    "ds_read_b64_tr_b16 v[60:61], v22 offset:32768\n"
    "ds_read_b64_tr_b16 v[62:63], v22 offset:33024\n"
    "ds_read_b64_tr_b16 v[64:65], v22 offset:36864\n"
    "ds_read_b64_tr_b16 v[66:67], v22 offset:37120\n"
    "ds_read_b64_tr_b16 v[68:69], v23 offset:32768\n"
    "ds_read_b64_tr_b16 v[70:71], v23 offset:33024\n"
    "ds_read_b64_tr_b16 v[72:73], v23 offset:36864\n"
    "ds_read_b64_tr_b16 v[74:75], v23 offset:37120\n"
    "s_waitcnt lgkmcnt(0)\n"
    "ds_read_b64_tr_b16 v[76:77], v22 offset:40960\n"
    "ds_read_b64_tr_b16 v[78:79], v22 offset:41216\n"
    "ds_read_b64_tr_b16 v[80:81], v22 offset:45056\n"
    "ds_read_b64_tr_b16 v[82:83], v22 offset:45312\n"
    "ds_read_b64_tr_b16 v[84:85], v23 offset:40960\n"
    "ds_read_b64_tr_b16 v[86:87], v23 offset:41216\n"
    "ds_read_b64_tr_b16 v[88:89], v23 offset:45056\n"
    "ds_read_b64_tr_b16 v[90:91], v23 offset:45312\n"
    "s_waitcnt lgkmcnt(0)\n"
    "ds_read_b64_tr_b16 v[92:93], v24 offset:32768\n"
    "ds_read_b64_tr_b16 v[94:95], v24 offset:33024\n"
    "ds_read_b64_tr_b16 v[96:97], v24 offset:36864\n"
    "ds_read_b64_tr_b16 v[98:99], v24 offset:37120\n"
    "ds_read_b64_tr_b16 v[100:101], v25 offset:32768\n"
    "ds_read_b64_tr_b16 v[102:103], v25 offset:33024\n"
    "ds_read_b64_tr_b16 v[104:105], v25 offset:36864\n"
    "ds_read_b64_tr_b16 v[106:107], v25 offset:37120\n"
    "s_waitcnt lgkmcnt(0)\n"
    "ds_read_b64_tr_b16 v[108:109], v24 offset:40960\n"
    "ds_read_b64_tr_b16 v[110:111], v24 offset:41216\n"
    "ds_read_b64_tr_b16 v[112:113], v24 offset:45056\n"
    "ds_read_b64_tr_b16 v[114:115], v24 offset:45312\n"
    "ds_read_b64_tr_b16 v[116:117], v25 offset:40960\n"
    "ds_read_b64_tr_b16 v[118:119], v25 offset:41216\n"
    "ds_read_b64_tr_b16 v[120:121], v25 offset:45056\n"
    "ds_read_b64_tr_b16 v[122:123], v25 offset:45312\n"
    "s_waitcnt lgkmcnt(0)\n"
    "s_barrier\n"
    "ds_write_b128 v12, v[60:63] offset:32768\n"
    "ds_write_b128 v12, v[64:67] offset:33792\n"
    "ds_write_b128 v12, v[68:71] offset:34816\n"
    "ds_write_b128 v12, v[72:75] offset:35840\n"
    "ds_write_b128 v12, v[76:79] offset:36864\n"
    "ds_write_b128 v12, v[80:83] offset:37888\n"
    "ds_write_b128 v12, v[84:87] offset:38912\n"
    "ds_write_b128 v12, v[88:91] offset:39936\n"
    "s_waitcnt lgkmcnt(0)\n"
    "ds_write_b128 v12, v[92:95] offset:40960\n"
    "ds_write_b128 v12, v[96:99] offset:41984\n"
    "ds_write_b128 v12, v[100:103] offset:43008\n"
    "ds_write_b128 v12, v[104:107] offset:44032\n"
    "ds_write_b128 v12, v[108:111] offset:45056\n"
    "ds_write_b128 v12, v[112:115] offset:46080\n"
    "ds_write_b128 v12, v[116:119] offset:47104\n"
    "ds_write_b128 v12, v[120:123] offset:48128\n"
    "s_waitcnt lgkmcnt(0)\n"
    "s_mov_b32 s68, 0\n"
    "s_cbranch_vccz .LA_kv1%=\n"
    ".LA_f0%=:\n"
    "v_cmp_gt_u32 vcc, 2, v35\n"
    "s_cbranch_vccnz .LA_f0r%=\n"
    "s_add_u32 s73, s73, 2048\n"
    "buffer_load_dwordx4 v2, s[40:43], s65 offen offset:0 sc1 lds\n"
    "buffer_load_dwordx4 v2, s[40:43], s65 offen offset:1024 sc1 lds\n"
    "buffer_load_dwordx4 v[52:55], v3, s[40:43], s65 offen offset:0 sc1\n"
    "buffer_load_dwordx4 v[56:59], v3, s[40:43], s65 offen offset:1024 sc1\n"
    ".LA_kv1%=:\n"
    "s_mov_b32 s73, 2048\n"
    ".LA_top%=:\n"
    "s_mov_b32 s68, 0\n"
    "ds_read_b128 v[104:107], v12 offset:0\n"
    "ds_read_b128 v[108:111], v12 offset:1024\n"
    "ds_read_b128 v[112:115], v12 offset:2048\n"
    "ds_read_b128 v[116:119], v12 offset:3072\n"
    "ds_read_b128 v[120:123], v12 offset:4096\n"
    "ds_read_b128 v[124:127], v12 offset:5120\n"
    "v_mov_b64 v[64:65], 0\n"
    "v_mov_b64 v[66:67], 0\n"
    "v_mov_b64 v[68:69], 0\n"
    "v_mov_b64 v[70:71], 0\n"
    "v_mov_b64 v[72:73], 0\n"
    "v_mov_b64 v[74:75], 0\n"
    "v_mov_b64 v[76:77], 0\n"
    "v_mov_b64 v[78:79], 0\n"
    "v_subrev_u32 v18, s72, v12\n"
    "v_add_u32 v18, 151552, v18\n"
    "s_cmp_eq_u32 s60, 0\n"
    "s_cbranch_scc1 .LA_w0%=\n"
    "s_cmp_eq_u32 s56, 3\n"
    "s_cbranch_scc0 .LA_w0%=\n"
    "s_mov_b32 s33, 0\n"
    "s_waitcnt vmcnt(5)\n"
    ".LA_stc%=:\n"
    "v_subrev_u32 v28, s59, v28\n"
    "v_subrev_u32 v29, s59, v29\n"
    "v_subrev_u32 v30, s59, v30\n"
    "v_subrev_u32 v31, s59, v31\n"
    "v_min3_i32 v32, v28, v29, v30\n"
    "v_max3_i32 v33, v28, v29, v30\n"
    "v_min_i32 v32, v32, v31\n"
    "v_max_i32 v33, v33, v31\n"
    "v_cmp_lt_i32 vcc, 0, v32\n"
    "s_nop 0\n"
    "v_max_i32_dpp v33, v33, v33 row_ror:1 row_mask:0xf bank_mask:0xf\n"
    "s_nop 1\n"
    "v_max_i32_dpp v33, v33, v33 row_ror:2 row_mask:0xf bank_mask:0xf\n"
    "s_nop 1\n"
    "v_max_i32_dpp v33, v33, v33 row_ror:4 row_mask:0xf bank_mask:0xf\n"
    "s_nop 1\n"
    "v_max_i32_dpp v33, v33, v33 row_ror:8 row_mask:0xf bank_mask:0xf\n"
    "s_cmp_eq_u32 vcc_lo, -1\n"
    "s_cbranch_scc0 .LA_str%=\n"
    "v_readlane_b32 s69, v33, 0\n"
    "v_readlane_b32 s74, v33, 16\n"
    "v_readlane_b32 s71, v33, 32\n"
    "v_readlane_b32 s100, v33, 48\n"
    "s_max_i32 s69, s69, s74\n"
    "s_max_i32 s71, s71, s100\n"
    "s_cmp_lt_u32 s60, 3\n"
    "s_cselect_b32 s71, s75, s71\n"
    "s_sub_u32 s71, s71, s75\n"
    "s_max_i32 s71, s71, -48\n"
    "s_cmp_lt_u32 s60, 8\n"
    "s_cselect_b32 s74, 1, 4\n"
    "s_ashr_i32 s74, s71, s74\n"
    "s_max_i32 s74, s74, s71\n"
    "s_cmp_gt_i32 s71, 60\n"
    "s_cselect_b32 s74, 8, s74\n"
    "s_add_u32 s75, s75, s74\n"
    "s_add_u32 s59, s59, s69\n"
    "s_add_u32 s69, s59, s75\n"
    "s_add_u32 s69, s69, s57\n"
    "s_mov_b32 s33, 1\n"
    "s_branch .LA_w2%=\n"
    ".LA_w0%=:\n"
    "s_mov_b32 s33, 0\n"
    "s_waitcnt vmcnt(0)\n"
    ".LA_w2%=:\n"
    "s_and_b32 s71, s60, 3\n"
    "s_cmp_eq_u32 s71, 3\n"
    "s_cbranch_scc0 .LA_fok%=\n"
    "s_cmp_eq_u32 s60, 63\n"
    "s_cbranch_scc1 .LA_fok%=\n"
    ".LA_fl%=:\n"
    "v_cmp_gt_u32 vcc, 2, v35\n"
    "s_cbranch_vccnz .LA_flr%=\n"
    "s_add_u32 s73, s73, 2048\n"
    ".LA_fok%=:\n"
    "s_add_u32 s65, s65, 0x40000\n"
    "s_cmp_eq_u32 s33, 0\n"
    "s_cbranch_scc1 .LA_wce%=\n"
    "s_mov_b32 s74, 0\n"
    "s_sub_u32 s71, s69, 160\n"
    "s_mov_b32 s33, 0\n"
    ".LA_wc%=:\n"
    "s_memrealtime s[100:101]\n"
    "s_add_u32 s74, s74, 0x1000000\n"
    "s_cbranch_scc1 .LA_wce%=\n"
    "s_waitcnt lgkmcnt(0)\n"
    "s_lshl_b32 s100, s100, 3\n"
    "s_sub_u32 s100, s100, s71\n"
    "s_cmp_lt_i32 s100, 0\n"
    "s_cbranch_scc1 .LA_wc%=\n"
    "s_cmp_gt_i32 s100, 130\n"
    "s_cbranch_scc1 .LA_wce%=\n"
    "s_memrealtime s[76:77]\n"
    "s_nop 0\n"
    "s_nop 0\n"
    "s_memrealtime s[78:79]\n"
    "s_nop 0\n"
    "s_nop 0\n"
    "s_memrealtime s[80:81]\n"
    "s_nop 0\n"
    "s_nop 0\n"
    "s_memrealtime s[82:83]\n"
    "s_nop 0\n"
    "s_nop 0\n"
    "s_memrealtime s[84:85]\n"
    "s_nop 0\n"
    "s_nop 0\n"
    "s_memrealtime s[86:87]\n"
    "s_nop 0\n"
    "s_nop 0\n"
    "s_memrealtime s[88:89]\n"
    "s_nop 0\n"
    "s_nop 0\n"
    "s_memrealtime s[90:91]\n"
    "s_nop 0\n"
    "s_nop 0\n"
    "s_waitcnt lgkmcnt(0)\n"
    "s_add_u32 s71, s76, s78\n"
    "s_add_u32 s71, s71, s80\n"
    "s_add_u32 s71, s71, s82\n"
    "s_add_u32 s71, s71, s84\n"
    "s_add_u32 s71, s71, s86\n"
    "s_add_u32 s71, s71, s88\n"
    "s_add_u32 s71, s71, s90\n"
    "s_sub_u32 s71, s69, s71\n"
    "s_sub_u32 s71, s71, 70\n"
    "s_cmp_ge_i32 s71, -8\n"
    "s_cselect_b32 s33, 1, 0\n"
    "s_max_i32 s71, s71, 0\n"
    "s_mul_i32 s71, s71, 91\n"
    "s_lshr_b32 s71, s71, 8\n"
    "s_min_u32 s71, s71, 48\n"
    "s_lshl_b32 s71, s71, 2\n"
    "s_sub_u32 s71, 204, s71\n"
    "s_getpc_b64 s[100:101]\n"
    "s_add_u32 s100, s100, s71\n"
    "s_addc_u32 s101, s101, 0\n"
    "s_setpc_b64 s[100:101]\n"
    "s_nop 1\n"
    "s_nop 1\n"
    "s_nop 1\n"
    "s_nop 1\n"
    "s_nop 1\n"
    "s_nop 1\n"
    "s_nop 1\n"
    "s_nop 1\n"
    "s_nop 1\n"
    "s_nop 1\n"
    "s_nop 1\n"
    "s_nop 1\n"
    "s_nop 1\n"
    "s_nop 1\n"
    "s_nop 1\n"
    "s_nop 1\n"
    "s_nop 1\n"
    "s_nop 1\n"
    "s_nop 1\n"
    "s_nop 1\n"
    "s_nop 1\n"
    "s_nop 1\n"
    "s_nop 1\n"
    "s_nop 1\n"
    "s_nop 1\n"
    "s_nop 1\n"
    "s_nop 1\n"
    "s_nop 1\n"
    "s_nop 1\n"
    "s_nop 1\n"
    "s_nop 1\n"
    "s_nop 1\n"
    "s_nop 1\n"
    "s_nop 1\n"
    "s_nop 1\n"
    "s_nop 1\n"
    "s_nop 1\n"
    "s_nop 1\n"
    "s_nop 1\n"
    "s_nop 1\n"
    "s_nop 1\n"
    "s_nop 1\n"
    "s_nop 1\n"
    "s_nop 1\n"
    "s_nop 1\n"
    "s_nop 1\n"
    "s_nop 1\n"
    "s_nop 1\n"
    ".LA_wce%=:\n"
    "s_waitcnt lgkmcnt(0)\n"
    "s_waitcnt vmcnt(0)\n"
    "s_barrier\n"
    "buffer_load_dwordx4 v[80:83], v0, s[40:43], s34 offen offset:0 sc1\n"
    "s_memrealtime s[38:39]\n"
    "ds_read_b128 v[20:23], v18 offset:0\n"
    "ds_read_b128 v[24:27], v18 offset:1024\n"
    "s_nop 4\n"
    "buffer_load_dwordx4 v[84:87], v0, s[40:43], s34 offen offset:1024 sc1\n"
    "ds_read_b128 v[28:31], v18 offset:2048\n"
    "ds_read_b128 v[32:35], v18 offset:3072\n"
    "s_nop 4\n"
    "buffer_load_dwordx4 v[88:91], v0, s[40:43], s34 offen offset:2048 sc1\n"
    "ds_read_b128 v[36:39], v18 offset:4096\n"
    "ds_read_b128 v[40:43], v18 offset:5120\n"
    "s_nop 4\n"
    "buffer_load_dwordx4 v[92:95], v0, s[40:43], s34 offen offset:3072 sc1\n"
    "ds_read_b128 v[44:47], v18 offset:6144\n"
    "ds_read_b128 v[48:51], v18 offset:7168\n"
    "s_nop 4\n"
    "buffer_load_dwordx4 v[96:99], v0, s[40:43], s35 offen offset:0 sc1\n"
    "s_nop 7\n"
    "buffer_load_dwordx4 v[100:103], v0, s[40:43], s35 offen offset:1024 sc1\n"
    "s_waitcnt lgkmcnt(0)\n"
    "s_lshl_b32 s38, s38, 3\n"
    "s_sub_u32 s38, s38, 0\n"
    "s_sub_u32 s71, s38, s69\n"
    "s_cmp_gt_i32 s71, 50\n"
    "s_cselect_b32 s33, 0, s33\n"
    "s_cmp_eq_u32 s33, 1\n"
    "s_cselect_b32 s38, s69, s38\n"
    "s_cselect_b32 s39, 0, 1\n"
    "s_cmp_eq_u32 s56, 3\n"
    "s_cselect_b32 s58, s62, 0\n"
    "s_cselect_b32 s39, s39, 0\n"
    "v_mov_b32 v17, 0\n"
    "s_nop 2\n"
    "v_mov_b32 v17, 0\n"
    "s_nop 2\n"
    "v_mov_b32 v17, 0\n"
    "s_nop 2\n"
    "s_mov_b32 s66, 0\n"
    "s_mov_b32 s33, 0\n"
    ".LA_ag0%=:\n"
    "v_mov_b32 v18, s60\n"
    "s_add_u32 s69, s38, s58\n"
    "v_writelane_b32 v18, s69, 0\n"
    "buffer_store_dword v18, v15, s[40:43], 0 offen sc1\n"
    "s_cmp_eq_u32 s33, 1\n"
    ".LA_ago%=:\n"
    "s_waitcnt vmcnt(6)\n"
    "v_smfmac_f32_16x16x64_bf16 v[64:67], v[80:83], v[104:111], v7\n"
    "ds_read_b128 v[104:107], v12 offset:6144\n"
    "ds_read_b128 v[108:111], v12 offset:7168\n"
    "v_smfmac_f32_16x16x64_bf16 v[68:71], v[80:83], v[112:119], v7\n"
    "ds_read_b128 v[112:115], v12 offset:8192\n"
    "ds_read_b128 v[116:119], v12 offset:9216\n"
    "v_cmp_eq_u32 vcc, -1, v80\n"
    "v_smfmac_f32_16x16x64_bf16 v[72:75], v[80:83], v[120:127], v7\n"
    "ds_write_b128 v8, v[80:83] offset:0\n"
    "s_waitcnt lgkmcnt(3)\n"
    "v_smfmac_f32_16x16x64_bf16 v[76:79], v[80:83], v[104:111], v7\n"
    "s_cbranch_vccnz .LA_ar0%=\n"
    "ds_read_b128 v[120:123], v12 offset:10240\n"
    "ds_read_b128 v[124:127], v12 offset:11264\n"
    "ds_read_b128 v[104:107], v12 offset:12288\n"
    "ds_read_b128 v[108:111], v12 offset:13312\n"
    "s_waitcnt vmcnt(5)\n"
    "v_smfmac_f32_16x16x64_bf16 v[64:67], v[84:87], a[0:7], v7\n"
    "ds_write_b128 v8, v[84:87] offset:1024\n"
    "v_smfmac_f32_16x16x64_bf16 v[68:71], v[84:87], a[8:15], v7\n"
    "v_smfmac_f32_16x16x64_bf16 v[72:75], v[84:87], a[16:23], v7\n"
    "v_smfmac_f32_16x16x64_bf16 v[76:79], v[84:87], a[24:31], v7\n"
    "s_waitcnt vmcnt(4)\n"
    "v_smfmac_f32_16x16x64_bf16 v[64:67], v[88:91], a[32:39], v7\n"
    "ds_write_b128 v8, v[88:91] offset:2048\n"
    "v_smfmac_f32_16x16x64_bf16 v[68:71], v[88:91], a[40:47], v7\n"
    "v_smfmac_f32_16x16x64_bf16 v[72:75], v[88:91], a[48:55], v7\n"
    "v_smfmac_f32_16x16x64_bf16 v[76:79], v[88:91], a[56:63], v7\n"
    "s_waitcnt vmcnt(3)\n"
    "v_smfmac_f32_16x16x64_bf16 v[64:67], v[92:95], a[64:71], v7\n"
    "ds_write_b128 v8, v[92:95] offset:3072\n"
    "v_smfmac_f32_16x16x64_bf16 v[68:71], v[92:95], a[72:79], v7\n"
    "v_smfmac_f32_16x16x64_bf16 v[72:75], v[92:95], a[80:87], v7\n"
    "v_smfmac_f32_16x16x64_bf16 v[76:79], v[92:95], a[88:95], v7\n"
    "s_waitcnt vmcnt(2)\n"
    "s_waitcnt lgkmcnt(8)\n"
    "v_smfmac_f32_16x16x64_bf16 v[64:67], v[96:99], v[112:119], v7\n"
    "s_waitcnt lgkmcnt(0)\n"
    "s_cbranch_scc1 .LA_nba%=\n"
    "s_barrier\n"
    ".LA_nba%=:\n"
    "ds_read_b128 v[112:115], v12 offset:14336\n"
    "v_smfmac_f32_16x16x64_bf16 v[68:71], v[96:99], v[120:127], v7\n"
    "ds_read_b128 v[116:119], v12 offset:15360\n"
    "ds_read_b128 v[80:83], v9 offset:2048\n"
    "v_smfmac_f32_16x16x64_bf16 v[72:75], v[96:99], v[104:111], v7\n"
    "ds_read_b128 v[84:87], v9 offset:3072\n"
    "s_waitcnt vmcnt(1)\n"
    "v_smfmac_f32_16x16x64_bf16 v[64:67], v[100:103], a[96:103], v7\n"
    "ds_read_b128 v[120:123], v12 offset:32768\n"
    "v_max_u32 v17, v96, v100\n"
    "v_smfmac_f32_16x16x64_bf16 v[68:71], v[100:103], a[104:111], v7\n"
    "ds_read_b128 v[124:127], v12 offset:33792\n"
    "v_cmp_eq_u32 vcc, -1, v17\n"
    "v_smfmac_f32_16x16x64_bf16 v[72:75], v[100:103], a[112:119], v7\n"
    "ds_read_b128 v[88:91], v10 offset:0\n"
    "buffer_load_dwordx4 v2, s[40:43], s65 offen offset:0 sc1 lds\n"
    "v_smfmac_f32_16x16x64_bf16 v[76:79], v[100:103], a[120:127], v7\n"
    "s_cbranch_vccnz .LA_ar2%=\n"
    "ds_read_b128 v[92:95], v10 offset:1024\n"
    "s_waitcnt lgkmcnt(6)\n"
    "v_smfmac_f32_16x16x64_bf16 v[76:79], v[96:99], v[112:119], v7\n"
    "ds_read_b128 v[104:107], v12 offset:34816\n"
    "s_waitcnt lgkmcnt(6)\n"
    "v_smfmac_f32_16x16x64_bf16 v[64:67], v[80:83], a[128:135], v7\n"
    "ds_read_b128 v[108:111], v12 offset:35840\n"
    "buffer_load_dwordx4 v2, s[40:43], s65 offen offset:1024 sc1 lds\n"
    "v_smfmac_f32_16x16x64_bf16 v[68:71], v[80:83], a[136:143], v7\n"
    "ds_read_b128 v[100:103], v10 offset:2048\n"
    "v_smfmac_f32_16x16x64_bf16 v[72:75], v[80:83], a[144:151], v7\n"
    "ds_read_b128 v[112:115], v12 offset:36864\n"
    "v_smfmac_f32_16x16x64_bf16 v[76:79], v[80:83], a[152:159], v7\n"
    "ds_read_b128 v[116:119], v12 offset:37888\n"
    "s_waitcnt lgkmcnt(9)\n"
    "v_smfmac_f32_16x16x64_bf16 v[64:67], v[84:87], a[160:167], v7\n"
    "ds_read_b128 v[96:99], v10 offset:3072\n"
    "v_smfmac_f32_16x16x64_bf16 v[68:71], v[84:87], a[168:175], v7\n"
    "ds_read_b128 v[80:83], v11 offset:0\n"
    "v_smfmac_f32_16x16x64_bf16 v[72:75], v[84:87], a[176:183], v7\n"
    "v_smfmac_f32_16x16x64_bf16 v[76:79], v[84:87], a[184:191], v7\n"
    "ds_read_b128 v[84:87], v11 offset:1024\n"
    "s_waitcnt lgkmcnt(9)\n"
    "v_smfmac_f32_16x16x64_bf16 v[64:67], v[88:91], v[120:127], v7\n"
    "ds_read_b128 v[120:123], v12 offset:38912\n"
    "s_waitcnt lgkmcnt(9)\n"
    "v_smfmac_f32_16x16x64_bf16 v[64:67], v[92:95], a[192:199], v7\n"
    "ds_read_b128 v[124:127], v12 offset:39936\n"
    "v_smfmac_f32_16x16x64_bf16 v[68:71], v[92:95], a[200:207], v7\n"
    "v_smfmac_f32_16x16x64_bf16 v[72:75], v[92:95], a[208:215], v7\n"
    "v_smfmac_f32_16x16x64_bf16 v[76:79], v[92:95], a[216:223], v7\n"
    "ds_read_b128 v[92:95], v11 offset:2048\n"
    "s_waitcnt lgkmcnt(9)\n"
    "v_smfmac_f32_16x16x64_bf16 v[68:71], v[88:91], v[104:111], v7\n"
    "ds_read_b128 v[104:107], v12 offset:40960\n"
    "s_waitcnt lgkmcnt(9)\n"
    "v_smfmac_f32_16x16x64_bf16 v[64:67], v[100:103], a[224:231], v7\n"
    "ds_read_b128 v[108:111], v12 offset:41984\n"
    "v_smfmac_f32_16x16x64_bf16 v[68:71], v[100:103], a[232:239], v7\n"
    "v_smfmac_f32_16x16x64_bf16 v[72:75], v[100:103], a[240:247], v7\n"
    "v_smfmac_f32_16x16x64_bf16 v[76:79], v[100:103], a[248:255], v7\n"
    "ds_read_b128 v[100:103], v11 offset:3072\n"
    "s_waitcnt lgkmcnt(9)\n"
    "v_smfmac_f32_16x16x64_bf16 v[72:75], v[88:91], v[112:119], v7\n"
    "ds_read_b128 v[112:115], v12 offset:43008\n"
    "s_waitcnt lgkmcnt(9)\n"
    "v_smfmac_f32_16x16x64_bf16 v[64:67], v[96:99], v[128:135], v7\n"
    "ds_read_b128 v[116:119], v12 offset:44032\n"
    "v_smfmac_f32_16x16x64_bf16 v[68:71], v[96:99], v[136:143], v7\n"
    "v_smfmac_f32_16x16x64_bf16 v[72:75], v[96:99], v[144:151], v7\n"
    "v_smfmac_f32_16x16x64_bf16 v[76:79], v[96:99], v[152:159], v7\n"
    "s_waitcnt lgkmcnt(6)\n"
    "v_smfmac_f32_16x16x64_bf16 v[76:79], v[88:91], v[120:127], v7\n"
    "ds_read_b128 v[120:123], v12 offset:45056\n"
    "s_waitcnt lgkmcnt(4)\n"
    "v_smfmac_f32_16x16x64_bf16 v[64:67], v[80:83], v[104:111], v7\n"
    "ds_read_b128 v[124:127], v12 offset:46080\n"
    "v_smfmac_f32_16x16x64_bf16 v[64:67], v[84:87], v[160:167], v7\n"
    "ds_read_b128 v[104:107], v12 offset:47104\n"
    "v_smfmac_f32_16x16x64_bf16 v[68:71], v[84:87], v[168:175], v7\n"
    "ds_read_b128 v[108:111], v12 offset:48128\n"
    "s_waitcnt lgkmcnt(4)\n"
    "v_smfmac_f32_16x16x64_bf16 v[68:71], v[80:83], v[112:119], v7\n"
    "v_smfmac_f32_16x16x64_bf16 v[64:67], v[92:95], v[192:199], v7\n"
    "v_smfmac_f32_16x16x64_bf16 v[68:71], v[92:95], v[200:207], v7\n"
    "v_smfmac_f32_16x16x64_bf16 v[64:67], v[100:103], v[224:231], v7\n"
    "s_cmp_eq_u32 s60, 63\n"
    "v_smfmac_f32_16x16x64_bf16 v[68:71], v[100:103], v[232:239], v7\n"
    "v_smfmac_f32_16x16x64_bf16 v[72:75], v[84:87], v[176:183], v7\n"
    "v_smfmac_f32_16x16x64_bf16 v[76:79], v[84:87], v[184:191], v7\n"
    "v_add_f32 v64, v64, v66\n"
    "v_smfmac_f32_16x16x64_bf16 v[72:75], v[92:95], v[208:215], v7\n"
    "v_add_f32 v65, v65, v67\n"
    "v_smfmac_f32_16x16x64_bf16 v[76:79], v[92:95], v[216:223], v7\n"
    "v_add_f32 v68, v68, v70\n"
    "s_waitcnt lgkmcnt(2)\n"
    "v_smfmac_f32_16x16x64_bf16 v[72:75], v[80:83], v[120:127], v7\n"
    "v_add_f32 v69, v69, v71\n"
    "v_smfmac_f32_16x16x64_bf16 v[72:75], v[100:103], v[240:247], v7\n"
    "v_cvt_pk_bf16_f32 v64, v64, v65\n"
    "v_smfmac_f32_16x16x64_bf16 v[76:79], v[100:103], v[248:255], v7\n"
    "v_cvt_pk_bf16_f32 v68, v68, v69\n"
    "s_waitcnt lgkmcnt(0)\n"
    "v_smfmac_f32_16x16x64_bf16 v[76:79], v[80:83], v[104:111], v7\n"
    "buffer_load_dwordx4 v[60:63], v1, s[40:43], s61 offen sc1\n"
    "ds_write2st64_b32 v13, v64, v68 offset0:0 offset1:1\n"
    "v_mov_b32 v125, 0x3f803f80\n"
    "v_mov_b32 v124, 0x3f803f80\n"
    "v_mov_b32 v123, 0x3f803f80\n"
    "v_pk_add_f32 v[72:73], v[72:73], v[74:75]\n"
    "v_cvt_pk_bf16_f32 v72, v72, v73\n"
    "v_mov_b32 v122, 0x3f803f80\n"
    "v_pk_add_f32 v[76:77], v[76:77], v[78:79]\n"
    "v_cvt_pk_bf16_f32 v76, v76, v77\n"
    "ds_write2st64_b32 v13, v72, v76 offset0:8 offset1:9\n"
    "s_waitcnt lgkmcnt(1)\n"
    "s_barrier\n"
    "ds_read_b64_tr_b16 v[80:81], v14 offset:0\n"
    "ds_read_b64_tr_b16 v[82:83], v14 offset:256\n"
    "ds_read_b64_tr_b16 v[84:85], v14 offset:512\n"
    "ds_read_b64_tr_b16 v[86:87], v14 offset:768\n"
    "s_waitcnt lgkmcnt(2)\n"
    "v_mfma_f32_16x16x32_bf16 v[64:67], v[20:23], v[80:83], 0\n"
    "ds_read_b64_tr_b16 v[88:89], v14 offset:1024\n"
    "ds_read_b64_tr_b16 v[90:91], v14 offset:1280\n"
    "v_mfma_f32_16x16x32_bf16 v[68:71], v[36:39], v[80:83], 0\n"
    "ds_read_b64_tr_b16 v[92:93], v14 offset:1536\n"
    "ds_read_b64_tr_b16 v[94:95], v14 offset:1792\n"
    "s_barrier\n"
    "s_cbranch_scc1 .LA_last%=\n"
    "s_waitcnt lgkmcnt(4)\n"
    "v_mfma_f32_16x16x32_bf16 v[64:67], v[24:27], v[84:87], v[64:67]\n"
    "ds_read_b64_tr_b16 v[96:97], v14 offset:2048\n"
    "ds_read_b64_tr_b16 v[98:99], v14 offset:2304\n"
    "v_mfma_f32_16x16x32_bf16 v[68:71], v[40:43], v[84:87], v[68:71]\n"
    "ds_read_b64_tr_b16 v[100:101], v14 offset:2560\n"
    "ds_read_b64_tr_b16 v[102:103], v14 offset:2816\n"
    "s_waitcnt lgkmcnt(6)\n"
    "v_mfma_f32_16x16x32_bf16 v[64:67], v[28:31], v[88:91], v[64:67]\n"
    "ds_read_b64_tr_b16 v[104:105], v14 offset:3072\n"
    "ds_read_b64_tr_b16 v[106:107], v14 offset:3328\n"
    "v_mfma_f32_16x16x32_bf16 v[68:71], v[44:47], v[88:91], v[68:71]\n"
    "ds_read_b64_tr_b16 v[108:109], v14 offset:3584\n"
    "ds_read_b64_tr_b16 v[110:111], v14 offset:3840\n"
    "s_waitcnt lgkmcnt(8)\n"
    "v_mfma_f32_16x16x32_bf16 v[64:67], v[32:35], v[92:95], v[64:67]\n"
    "v_mfma_f32_16x16x32_bf16 v[68:71], v[48:51], v[92:95], v[68:71]\n"
    "s_waitcnt lgkmcnt(6)\n"
    "v_mfma_f32_16x16x32_bf16 v[72:75], v[96:99], v[80:83], 0\n"
    "v_mov_b64 v[120:121], 0\n"
    "s_waitcnt lgkmcnt(4)\n"
    "v_mfma_f32_16x16x32_bf16 v[72:75], v[100:103], v[84:87], v[72:75]\n"
    "v_mov_b64 v[116:117], 0\n"
    "s_waitcnt lgkmcnt(2)\n"
    "v_mfma_f32_16x16x32_bf16 v[72:75], v[104:107], v[88:91], v[72:75]\n"
    "s_waitcnt lgkmcnt(0)\n"
    "v_mfma_f32_16x16x32_bf16 v[72:75], v[108:111], v[92:95], v[72:75]\n"
    "v_max3_f32 v17, v64, v65, v66\n"
    "v_max3_f32 v18, v68, v69, v70\n"
    "v_max3_f32 v17, v17, v67, v71\n"
    "v_max_f32 v76, v17, v18\n"
    "v_max_f32 v17, v17, v18\n"
    "s_nop 1\n"
    "v_permlane32_swap_b32 v17, v76\n"
    "v_max3_f32 v18, v72, v73, v74\n"
    "v_max3_f32 v17, v17, v76, v75\n"
    "v_max_f32 v76, v17, v18\n"
    "v_max_f32 v17, v17, v18\n"
    "s_nop 1\n"
    "v_permlane16_swap_b32 v17, v76\n"
    "v_max_f32 v17, v17, v76\n"
    "v_mul_f32 v76, s70, v17\n"
    "v_pk_fma_f32 v[64:65], v[64:65], s[70:71], v[76:77] op_sel:[0,0,0] op_sel_hi:[1,0,0] neg_lo:[0,0,1] neg_hi:[0,0,1]\n"
    "v_add_f32 v77, 1.0, v76\n"
    "v_pk_fma_f32 v[66:67], v[66:67], s[70:71], v[76:77] op_sel:[0,0,0] op_sel_hi:[1,0,0] neg_lo:[0,0,1] neg_hi:[0,0,1]\n"
    "v_exp_f32 v64, v64\n"
    "v_exp_f32 v65, v65\n"
    "v_pk_fma_f32 v[68:69], v[68:69], s[70:71], v[76:77] op_sel:[0,0,0] op_sel_hi:[1,0,0] neg_lo:[0,0,1] neg_hi:[0,0,1]\n"
    "v_exp_f32 v66, v66\n"
    "v_exp_f32 v67, v67\n"
    "v_pk_fma_f32 v[70:71], v[70:71], s[70:71], v[76:77] op_sel:[0,0,0] op_sel_hi:[1,0,0] neg_lo:[0,0,1] neg_hi:[0,0,1]\n"
    "v_exp_f32 v68, v68\n"
    "v_exp_f32 v69, v69\n"
    "v_pk_fma_f32 v[72:73], v[72:73], s[70:71], v[76:77] op_sel:[0,0,1] op_sel_hi:[1,0,1] neg_lo:[0,0,1] neg_hi:[0,0,1]\n"
    "v_exp_f32 v70, v70\n"
    "v_exp_f32 v71, v71\n"
    "v_pk_fma_f32 v[74:75], v[74:75], s[70:71], v[76:77] op_sel:[0,0,1] op_sel_hi:[1,0,1] neg_lo:[0,0,1] neg_hi:[0,0,1]\n"
    "v_cvt_pk_bf16_f32 v112, v64, v65\n"
    "v_cvt_pk_bf16_f32 v113, v66, v67\n"
    "v_cvt_pk_bf16_f32 v114, v68, v69\n"
    "v_cvt_pk_bf16_f32 v115, v70, v71\n"
    "v_exp_f32 v72, v72\n"
    "v_exp_f32 v73, v73\n"
    "v_mfma_f32_16x16x32_bf16 v[76:79], v[122:125], v[112:115], 0\n"
    "v_exp_f32 v74, v74\n"
    "v_exp_f32 v75, v75\n"
    "v_mfma_f32_16x16x32_bf16 v[80:83], v[52:55], v[112:115], 0\n"
    "v_cvt_pk_bf16_f32 v118, v72, v73\n"
    "v_cvt_pk_bf16_f32 v119, v74, v75\n"
    "s_waitcnt vmcnt(0)\n"
    "v_max3_u32 v17, v60, v61, v62\n"
    "v_mfma_f32_16x16x32_bf16 v[76:79], v[122:125], v[116:119], v[76:79]\n"
    "v_max_u32 v17, v17, v63\n"
    "v_mfma_f32_16x16x32_bf16 v[80:83], v[60:63], v[118:121], v[80:83]\n"
    "v_cmp_eq_u32 s[52:53], -1, v17\n"
    "v_mfma_f32_16x16x32_bf16 v[84:87], v[56:59], v[112:115], 0\n"
    "s_nop 0\n"
    "v_rcp_f32 v76, v76\n"
    "v_mfma_f32_16x16x32_bf16 v[84:87], v[60:63], v[116:119], v[84:87]\n"
    "v_cmp_u_f32 vcc, v77, v77\n"
    "v_mul_f32 v80, v80, v76\n"
    "v_mul_f32 v81, v81, v76\n"
    "v_mul_f32 v82, v82, v76\n"
    "v_mul_f32 v83, v83, v76\n"
    "v_cvt_pk_bf16_f32 v88, v80, v81\n"
    "v_cvt_pk_bf16_f32 v89, v82, v83\n"
    "v_min_u32 v88, -2, v88\n"
    "v_min_u32 v89, -2, v89\n"
    "s_or_b64 s[54:55], vcc, s[52:53]\n"
    "s_cselect_b32 s46, 0, s42\n"
    "v_pk_mul_f32 v[84:85], v[84:85], v[76:77] op_sel_hi:[1,0]\n"
    "v_pk_mul_f32 v[86:87], v[86:87], v[76:77] op_sel_hi:[1,0]\n"
    "v_cvt_pk_bf16_f32 v90, v84, v85\n"
    "v_cvt_pk_bf16_f32 v91, v86, v87\n"
    "v_min_u32 v90, -2, v90\n"
    "v_min_u32 v91, -2, v91\n"
    "buffer_store_dwordx4 v[88:91], v6, s[44:47], s61 offen sc1\n"
    "s_cbranch_scc1 .LA_fix%=\n"
    ".LA_pub%=:\n"
    "s_cmp_eq_u32 s56, 3\n"
    "s_cbranch_scc0 .LA_pc%=\n"
    "s_memrealtime s[76:77]\n"
    "s_nop 0\n"
    "s_nop 0\n"
    "s_memrealtime s[78:79]\n"
    "s_nop 0\n"
    "s_nop 0\n"
    "s_memrealtime s[80:81]\n"
    "s_nop 0\n"
    "s_nop 0\n"
    "s_memrealtime s[82:83]\n"
    "s_nop 0\n"
    "s_nop 0\n"
    "s_memrealtime s[84:85]\n"
    "s_nop 0\n"
    "s_nop 0\n"
    "s_memrealtime s[86:87]\n"
    "s_nop 0\n"
    "s_nop 0\n"
    "s_memrealtime s[88:89]\n"
    "s_nop 0\n"
    "s_nop 0\n"
    "s_memrealtime s[90:91]\n"
    "s_nop 0\n"
    "s_nop 0\n"
    "s_branch .LA_pd%=\n"
    ".LA_pc%=:\n"
    "s_memrealtime s[76:77]\n"
    "s_waitcnt lgkmcnt(0)\n"
    "s_mov_b32 s78, s76\n"
    "s_mov_b32 s80, s76\n"
    "s_mov_b32 s82, s76\n"
    "s_mov_b32 s84, s76\n"
    "s_mov_b32 s86, s76\n"
    "s_mov_b32 s88, s76\n"
    "s_mov_b32 s90, s76\n"
    ".LA_pd%=:\n"
    "s_xor_b32 s48, s48, 16\n"
    "buffer_load_dwordx4 v[28:31], v12, s[48:51], 0 idxen sc1\n"
    "buffer_load_dword v35, v16, s[40:43], s73 offen sc1\n"
    "s_xor_b32 s32, s32, 16\n"
    "v_writelane_b32 v15, s32, 0\n"
    "s_sleep 2\n"
    "buffer_load_dwordx4 v[52:55], v3, s[40:43], s65 offen offset:0 sc1\n"
    "buffer_load_dwordx4 v[56:59], v3, s[40:43], s65 offen offset:1024 sc1\n"
    "s_lshl_b32 s69, s56, 9\n"
    "v_add_u32 v18, s69, v14\n"
    "ds_read_b64_tr_b16 v[92:93], v18 offset:2048\n"
    "ds_read_b64_tr_b16 v[94:95], v18 offset:2304\n"
    "v_mov_b64 v[96:97], -1\n"
    "v_mov_b64 v[98:99], -1\n"
    "buffer_store_dwordx4 v[96:99], v6, s[40:43], s63 offen sc1\n"
    "s_waitcnt lgkmcnt(0)\n"
    "buffer_store_dwordx4 v[92:95], v4, s[40:43], s67 offen sc1\n"
    "buffer_store_dwordx4 v[60:63], v5, s[40:43], s67 offen sc1\n"
    "s_waitcnt lgkmcnt(0)\n"
    "s_add_u32 s100, s76, s78\n"
    "s_add_u32 s100, s100, s80\n"
    "s_add_u32 s100, s100, s82\n"
    "s_add_u32 s100, s100, s84\n"
    "s_add_u32 s100, s100, s86\n"
    "s_add_u32 s100, s100, s88\n"
    "s_add_u32 s100, s100, s90\n"
    "s_sub_u32 s100, s100, s38\n"
    "s_cmp_eq_u32 s39, 1\n"
    "s_cselect_b32 s100, s62, s100\n"
    "s_sub_u32 s100, s100, s62\n"
    "s_max_i32 s100, s100, -48\n"
    "s_cmp_lt_u32 s60, 8\n"
    "s_cselect_b32 s74, 1, 4\n"
    "s_ashr_i32 s74, s100, s74\n"
    "s_max_i32 s74, s74, s100\n"
    "s_cmp_gt_i32 s100, 60\n"
    "s_cselect_b32 s74, 8, s74\n"
    "s_add_u32 s62, s62, s74\n"
    "s_sub_u32 s71, s64, s61\n"
    "s_add_u32 s34, s34, s71\n"
    "s_add_u32 s35, s35, s71\n"
    "s_add_u32 s36, s36, s71\n"
    "s_add_u32 s37, s37, s71\n"
    "s_mov_b32 s69, s63\n"
    "s_mov_b32 s63, s61\n"
    "s_mov_b32 s61, s64\n"
    "s_mov_b32 s64, s69\n"
    "s_add_u32 s60, s60, 1\n"
    "s_add_u32 s67, s67, 0x20000\n"
    "s_branch .LA_top%=\n"
    ".LA_fix%=:\n"
    "s_cbranch_vccnz .LA_ar1%=\n"
    ".LA_nan%=:\n"
    "buffer_load_dwordx4 v[60:63], v1, s[40:43], s61 offen sc1\n"
    "s_waitcnt vmcnt(0)\n"
    "v_max3_u32 v17, v60, v61, v62\n"
    "v_max_u32 v17, v17, v63\n"
    "v_cmp_eq_u32 vcc, -1, v17\n"
    "s_cbranch_vccnz .LA_vr%=\n"
    "v_mfma_f32_16x16x32_bf16 v[80:83], v[52:55], v[112:115], 0\n"
    "v_mfma_f32_16x16x32_bf16 v[80:83], v[60:63], v[118:121], v[80:83]\n"
    "v_mfma_f32_16x16x32_bf16 v[84:87], v[56:59], v[112:115], 0\n"
    "v_mfma_f32_16x16x32_bf16 v[84:87], v[60:63], v[116:119], v[84:87]\n"
    "s_nop 0\n"
    "v_mul_f32 v80, v80, v76\n"
    "v_mul_f32 v81, v81, v76\n"
    "v_mul_f32 v82, v82, v76\n"
    "v_mul_f32 v83, v83, v76\n"
    "v_cvt_pk_bf16_f32 v88, v80, v81\n"
    "v_cvt_pk_bf16_f32 v89, v82, v83\n"
    "v_min_u32 v88, -2, v88\n"
    "v_min_u32 v89, -2, v89\n"
    "v_pk_mul_f32 v[84:85], v[84:85], v[76:77] op_sel_hi:[1,0]\n"
    "v_pk_mul_f32 v[86:87], v[86:87], v[76:77] op_sel_hi:[1,0]\n"
    "v_cvt_pk_bf16_f32 v90, v84, v85\n"
    "v_cvt_pk_bf16_f32 v91, v86, v87\n"
    "v_min_u32 v90, -2, v90\n"
    "v_min_u32 v91, -2, v91\n"
    "buffer_store_dwordx4 v[88:91], v6, s[40:43], s61 offen sc1\n"
    "s_branch .LA_pub%=\n"
    ".LA_vr%=:\n"
    "s_add_u32 s68, s68, 1\n"
    "s_cmp_lt_u32 s68, 65536\n"
    "s_cbranch_scc1 .LA_nan%=\n"
    "s_branch .LA_tmo%=\n"
    ".LA_f0r%=:\n"
    "s_sleep 1\n"
    "buffer_load_dword v35, v16, s[40:43], s73 offen sc1\n"
    "s_waitcnt vmcnt(0)\n"
    "s_add_u32 s68, s68, 1\n"
    "s_cmp_lt_u32 s68, 65536\n"
    "s_cbranch_scc1 .LA_f0%=\n"
    "s_branch .LA_tmo%=\n"
    ".LA_str%=:\n"
    "buffer_load_dwordx4 v[28:31], v12, s[48:51], 0 idxen sc1\n"
    "s_waitcnt vmcnt(0)\n"
    "s_add_u32 s33, s33, 1\n"
    "s_cmp_lt_u32 s33, 128\n"
    "s_cbranch_scc1 .LA_stc%=\n"
    "s_branch .LA_w0%=\n"
    ".LA_flr%=:\n"
    "s_sleep 1\n"
    "buffer_load_dword v35, v16, s[40:43], s73 offen sc1\n"
    "s_waitcnt vmcnt(0)\n"
    "s_add_u32 s68, s68, 1\n"
    "s_cmp_lt_u32 s68, 65536\n"
    "s_cbranch_scc1 .LA_fl%=\n"
    "s_branch .LA_tmo%=\n"
    ".LA_ar2%=:\n"
    "s_mov_b32 s33, 1\n"
    "s_branch .LA_ar5%=\n"
    ".LA_ar1%=:\n"
    "s_cmp_eq_u32 s66, 1\n"
    "s_cbranch_scc1 .LA_nan%=\n"
    "s_mov_b32 s66, 1\n"
    ".LA_ar0%=:\n"
    "s_mov_b32 s33, 0\n"
    ".LA_ar5%=:\n"
    "v_mov_b32 v18, s60\n"
    "s_sub_u32 s69, s38, 0x100000\n"
    "v_writelane_b32 v18, s69, 0\n"
    "buffer_store_dword v18, v15, s[40:43], 0 offen sc1\n"
    "s_waitcnt lgkmcnt(0)\n"
    "ds_read_b128 v[104:107], v12 offset:0\n"
    "ds_read_b128 v[108:111], v12 offset:1024\n"
    "ds_read_b128 v[112:115], v12 offset:2048\n"
    "ds_read_b128 v[116:119], v12 offset:3072\n"
    "ds_read_b128 v[120:123], v12 offset:4096\n"
    "ds_read_b128 v[124:127], v12 offset:5120\n"
    "v_mov_b64 v[64:65], 0\n"
    "v_mov_b64 v[66:67], 0\n"
    "v_mov_b64 v[68:69], 0\n"
    "v_mov_b64 v[70:71], 0\n"
    "v_mov_b64 v[72:73], 0\n"
    "v_mov_b64 v[74:75], 0\n"
    "v_mov_b64 v[76:77], 0\n"
    "v_mov_b64 v[78:79], 0\n"
    ".LA_ar3%=:\n"
    "buffer_load_dwordx4 v[80:83], v0, s[40:43], s34 offen offset:0 sc1\n"
    "buffer_load_dwordx4 v[84:87], v0, s[40:43], s34 offen offset:1024 sc1\n"
    "buffer_load_dwordx4 v[88:91], v0, s[40:43], s34 offen offset:2048 sc1\n"
    "buffer_load_dwordx4 v[92:95], v0, s[40:43], s34 offen offset:3072 sc1\n"
    "buffer_load_dwordx4 v[96:99], v0, s[40:43], s35 offen offset:0 sc1\n"
    "buffer_load_dwordx4 v[100:103], v0, s[40:43], s35 offen offset:1024 sc1\n"
    "s_waitcnt vmcnt(5)\n"
    "v_max3_u32 v17, v80, v81, v80\n"
    "v_max3_u32 v17, v82, v83, v17\n"
    "v_cmp_eq_u32 vcc, -1, v17\n"
    "s_cbranch_vccnz .LA_ar4%=\n"
    "s_waitcnt vmcnt(4)\n"
    "v_max3_u32 v17, v84, v85, v17\n"
    "v_max3_u32 v17, v86, v87, v17\n"
    "s_waitcnt vmcnt(3)\n"
    "v_max3_u32 v17, v88, v89, v17\n"
    "v_max3_u32 v17, v90, v91, v17\n"
    "s_waitcnt vmcnt(2)\n"
    "v_max3_u32 v17, v92, v93, v17\n"
    "v_max3_u32 v17, v94, v95, v17\n"
    "s_waitcnt vmcnt(1)\n"
    "v_max3_u32 v17, v96, v97, v17\n"
    "v_max3_u32 v17, v98, v99, v17\n"
    "s_waitcnt vmcnt(0)\n"
    "v_max3_u32 v17, v100, v101, v17\n"
    "v_max3_u32 v17, v102, v103, v17\n"
    "v_cmp_eq_u32 vcc, -1, v17\n"
    "s_cbranch_vccnz .LA_ar4%=\n"
    "s_memrealtime s[38:39]\n"
    "s_waitcnt lgkmcnt(0)\n"
    "s_lshl_b32 s38, s38, 3\n"
    "s_sub_u32 s38, s38, 150\n"
    "s_mov_b32 s39, 1\n"
    "s_mov_b32 s58, s62\n"
    "s_branch .LA_ag0%=\n"
    ".LA_ar4%=:\n"
    "s_add_u32 s68, s68, 1\n"
    "s_cmp_lt_u32 s68, 65536\n"
    "s_cbranch_scc1 .LA_ar3%=\n"
    "s_branch .LA_tmo%=\n"
    ".LA_tmo%=:\n"
    "v_mov_b32 v20, 1\n"
    "v_mov_b32 v21, 1572864\n"
    "buffer_store_dword v20, v21, s[40:43], 0 offen sc1\n"
    "s_waitcnt vmcnt(0)\n"
    "s_endpgm\n"
    ".LA_last%=:\n"
    "s_waitcnt lgkmcnt(0)\n"
    "ds_read_b64_tr_b16 v[96:97], v14 offset:2048\n"
    "ds_read_b64_tr_b16 v[98:99], v14 offset:2304\n"
    "ds_read_b64_tr_b16 v[100:101], v14 offset:2560\n"
    "ds_read_b64_tr_b16 v[102:103], v14 offset:2816\n"
    "ds_read_b64_tr_b16 v[104:105], v14 offset:3072\n"
    "ds_read_b64_tr_b16 v[106:107], v14 offset:3328\n"
    "ds_read_b64_tr_b16 v[108:109], v14 offset:3584\n"
    "ds_read_b64_tr_b16 v[110:111], v14 offset:3840\n"
    "s_waitcnt lgkmcnt(0)\n"
    "v_mfma_f32_16x16x32_bf16 v[72:75], v[96:99], v[80:83], 0\n"
    "v_mfma_f32_16x16x32_bf16 v[72:75], v[100:103], v[84:87], v[72:75]\n"
    "v_mfma_f32_16x16x32_bf16 v[72:75], v[104:107], v[88:91], v[72:75]\n"
    "v_mfma_f32_16x16x32_bf16 v[72:75], v[108:111], v[92:95], v[72:75]\n"
    "s_nop 7\n"
    "v_add_f32 v72, v72, v73\n"
    "v_add_f32 v74, v74, v75\n"
    "v_cmp_u_f32 vcc, v72, v74\n"
    "s_cbranch_vccz .LA_lk%=\n"
    "s_cmp_eq_u32 s66, 0\n"
    "s_cbranch_scc1 .LA_ar1%=\n"
    ".LA_lk%=:\n"
    "s_lshl_b32 s69, s56, 9\n"
    "v_add_u32 v18, s69, v14\n"
    "ds_read_b64_tr_b16 v[92:93], v18 offset:2048\n"
    "ds_read_b64_tr_b16 v[94:95], v18 offset:2304\n"
    "s_waitcnt lgkmcnt(0)\n"
    "buffer_store_dwordx4 v[92:95], v4, s[40:43], s67 offen sc1\n"
    ".LA_lr%=:\n"
    "buffer_load_dwordx4 v[60:63], v1, s[40:43], s61 offen sc1\n"
    "s_waitcnt vmcnt(0)\n"
    "v_max3_u32 v17, v60, v61, v62\n"
    "v_max_u32 v17, v17, v63\n"
    "v_cmp_eq_u32 vcc, -1, v17\n"
    "s_cbranch_vccz .LA_lg%=\n"
    "s_add_u32 s68, s68, 1\n"
    "s_cmp_lt_u32 s68, 65536\n"
    "s_cbranch_scc1 .LA_lr%=\n"
    "s_branch .LA_tmo%=\n"
    ".LA_lg%=:\n"
    "buffer_store_dwordx4 v[60:63], v5, s[40:43], s67 offen sc1\n"
    "s_waitcnt vmcnt(0)\n"
    "s_barrier\n"
    "v_mov_b32 v18, 64\n"
    "buffer_store_dword v18, v15, s[40:43], 0 offen sc1\n"
    "s_waitcnt vmcnt(0)\n"
    ".LA_fin%=:\n"
    "s_endpgm\n"
    :
    :
      "{v64}"(vo_mem), "{v65}"(vo_vm), "{v66}"(vo_kf), "{v67}"(vo_vf), "{v68}"(vo_km), "{v69}"(vo_vmp), "{v70}"(vo_o), "{v71}"(idx), "{v72}"(a_m0),
      "{v73}"(a_m1), "{v74}"(a_m2), "{v75}"(a_m3), "{v76}"(a_w), "{v77}"(a_qw), "{v78}"(a_qr), "{v79}"(vo_prog), "{v80}"(vo_flag), "{v81}"(vo_src),
      "{v82}"(a_tr), "{v83}"(vo_dbg), "{v84}"(vo_rdy), "{s[40:43]}"(rxv), "{s[44:47]}"(rw), "{s56}"(w), "{s57}"(d0)
    :
      "memory", "vcc", "scc", "v255", "a255", "s101");

  __builtin_amdgcn_endpgm();
}

DI void role_BR(const Params& p, u32 lb, int b, int w, int lane) {
  const u32 c = lane & 15, g = lane >> 4;
  const u32 rg = b >> 5, cs = b & 31;
  const rsrc_t rx = mkrs(p.wb, WS_END);
  const u32 m_off = physo(((lane & 1) ? L_MEMB : L_MEMA) + cs * 512 + rg * 256) + (lane >> 2) * 16;
  const u32 v_off = physo(L_VM + b * 1024) + lane * 16, vo_vpub = physo(L_VM + b * 1024) + w * 256 + lane * 4;
  if (w < 3) {
    const v4u sv = {SENT, SENT, SENT, SENT};
    if (w) stx4(sv, rx, m_off + ((lane >> 1) & 1) * 8192, w * EX_SLOT); stx4(sv, rx, v_off, w * EX_SLOT); stx4(sv, rx, v_off + 8192, w * EX_SLOT);
  } else {
    const u32 now = rt32() * 8 - 0x800000u;
    __builtin_amdgcn_raw_buffer_store_b32(now, rx, lane < 8 ? OFF_STAMP + (lane >> 2) * 8192 + (32 + cs) * 128 + rg * 4 + (lane & 3) * 8 : OOB, 0, SC1);
  }
  const u32 vo_rdy = threadIdx.x == 0 ? OFF_CTL + 4 * C_F + 8 * (32 + b) : OOB;
  const u32 rho = 0, wr = w;
  const u32 vo_mem = physo(L_MEMB + wr * 4096) + (g >> 1) * 512 + (c >> 3) * 256 + ((g & 1) * 8 + (c & 7)) * 16;
  const u32 vo_o = physo(L_O + rg * 32768 + wr * 8192) + (c >> 3) * 512 + g * 128 + ((c >> 1) & 1) * 64 + (2 * ((c >> 2) & 1) + (c & 1)) * 16;
  const u32 vo_mpub = w != 1 ? physo(((w & 1) ? L_MEMB : L_MEMA) + cs * 512 + rg * 256) + lane * 4 : OOB;
  const u32 vo_st = OFF_STAMP + lane * 128, vo_stw = (w == 3 && lane < 4) ? OFF_STAMP + (32 + cs) * 128 + rg * 4 + lane * 8 : OOB;
  const u32 idx = (c & 2) ? 0xEEEEu : 0x4444u;
  const u32 a_bw = lb + LDS_MEM + w * 2048 + (g >> 1) * 256 + c * 16 + (g & 1) * 8, a_br = lb + LDS_MEM + (w >> 1) * 1024 + ((lane >> 1) & 1) * 512 + (w & 1) * 256 + (lane >> 2) * 16 + (lane & 1) * 8;
  const u32 f_rr = 2 * (g & 1) + ((lane >> 2) & 1), f_cp = 8 * (g >> 1) + 4 * ((lane >> 3) & 1) + (lane & 3);
  const u32 a_rw = lb + LDS_MEM + 8192 + w * 512 + (g & 1) * 256 + ((g >> 1) * 16 + c) * 4, a_rr = lb + LDS_MEM + 8192 + f_rr * 128 + f_cp * 8;
  const u32 vo_srcv = (wr * 256 + (lane >> 3)) * 8192 + b * 128 + (((lane & 7) * 16) ^ ((lane >> 5) * 32));
  const u32 vo_srco = (wr * 1024 + ((lane >> 2) ^ ((lane >> 3) & 2))) * 2048 + cs * 64 + (lane & 3) * 16;
  const u32 a_tv = lb + w * 32768 + (16 * (g >> 1) + 4 * (g & 1) + ((c >> 2) & 1) + 8 * (c >> 3)) * 128 + ((g & 1) + 2 * (c >> 3)) * 32 + (c & 3) * 8;
  const u32 a_to = lb + w * 32768 + (16 * (g & 1) + 8 * (g >> 1) + ((c >> 2) & 1) + 4 * (c >> 3)) * 64 + (g & 1) * 32 + (c >> 3) * 128 + (c & 3) * 8;
  const u32 vo_mem0 = (4 * rg + f_rr) * 2048 + cs * 64 + f_cp * 4;
  const u32 vo_m0d = physo(((w & 1) ? L_MEMB : L_MEMA) + cs * 512 + rg * 256) + (w >> 1) * 8192 + lane * 4;
  const u32 vo_dbg = lane == 0 ? OFF_DBG + ((32 + b) * 4 + w) * 128 : OOB;
  const v4u rxv = mkrv(p.wb, WS_END), rw = mkrv(p.Wv, 8u << 20), rw2 = mkrv(p.Wo, 8u << 20), r0 = mkrv(p.mem0, 16384);
  const u32 d0 = p.db, d1 = p.dr;
  asm volatile(
    "v_mov_b32 v0, v64\n"
    "v_mov_b32 v1, v65\n"
    "v_mov_b32 v2, v66\n"
    "v_mov_b32 v3, v67\n"
    "v_mov_b32 v4, v68\n"
    "v_mov_b32 v6, v69\n"
    "v_mov_b32 v7, v70\n"
    "v_mov_b32 v8, v71\n"
    "v_mov_b32 v9, v72\n"
    "v_mov_b32 v10, v73\n"
    "v_mov_b32 v11, v74\n"
    "v_mov_b32 v12, v75\n"
    "v_mov_b32 v13, v76\n"
    "v_mov_b32 v14, v77\n"
    "v_mov_b32 v15, v78\n"
    "v_mov_b32 v96, v79\n"
    "v_mov_b32 v98, v80\n"
    "v_mov_b32 v99, v81\n"
    "v_mov_b32 v97, v82\n"
    "s_nop 4\n"
    "s_lshl_b32 s72, s56, 15\n"
    "v_xor_b32 v32, 0, v10\n"
    "v_xor_b32 v33, 64, v10\n"
    "v_xor_b32 v34, 0, v11\n"
    "v_xor_b32 v35, 32, v11\n"
    "v_xor_b32 v38, 0, v12\n"
    "v_xor_b32 v39, 32, v12\n"
    "v_xor_b32 v40, 64, v12\n"
    "v_xor_b32 v41, 96, v12\n"
    "v_xor_b32 v42, 0, v13\n"
    "v_xor_b32 v43, 32, v13\n"
    "v_xor_b32 v44, 128, v13\n"
    "v_xor_b32 v45, 160, v13\n"
    "v_mov_b32 v52, 1573120\n"
    "buffer_load_dwordx2 v[50:51], v52, s[40:43], 0 offen sc1\n"
    "buffer_load_dword v54, v14, s[52:55], 0 offen\n"
    "s_add_u32 s69, s75, 0\n"
    "s_and_b32 s69, s69, 3\n"
    "s_lshl_b32 s69, s69, 19\n"
    "s_add_u32 m0, s72, 0\n"
    "s_nop 0\n"
    "buffer_load_dwordx4 v32, s[44:47], s69 offen lds\n"
    "s_add_u32 m0, m0, 1024\n"
    "s_add_u32 s69, s69, 65536\n"
    "buffer_load_dwordx4 v33, s[44:47], s69 offen lds\n"
    "s_add_u32 m0, m0, 1024\n"
    "s_add_u32 s69, s69, 65536\n"
    "buffer_load_dwordx4 v32, s[44:47], s69 offen lds\n"
    "s_add_u32 m0, m0, 1024\n"
    "s_add_u32 s69, s69, 65536\n"
    "buffer_load_dwordx4 v33, s[44:47], s69 offen lds\n"
    "s_add_u32 m0, m0, 1024\n"
    "s_add_u32 s69, s69, 65536\n"
    "buffer_load_dwordx4 v32, s[44:47], s69 offen lds\n"
    "s_add_u32 m0, m0, 1024\n"
    "s_add_u32 s69, s69, 65536\n"
    "buffer_load_dwordx4 v33, s[44:47], s69 offen lds\n"
    "s_add_u32 m0, m0, 1024\n"
    "s_add_u32 s69, s69, 65536\n"
    "buffer_load_dwordx4 v32, s[44:47], s69 offen lds\n"
    "s_add_u32 m0, m0, 1024\n"
    "s_add_u32 s69, s69, 65536\n"
    "buffer_load_dwordx4 v33, s[44:47], s69 offen lds\n"
    "s_add_u32 s69, s75, 1\n"
    "s_and_b32 s69, s69, 3\n"
    "s_lshl_b32 s69, s69, 19\n"
    "s_add_u32 m0, s72, 8192\n"
    "s_nop 0\n"
    "buffer_load_dwordx4 v32, s[44:47], s69 offen lds\n"
    "s_add_u32 m0, m0, 1024\n"
    "s_add_u32 s69, s69, 65536\n"
    "buffer_load_dwordx4 v33, s[44:47], s69 offen lds\n"
    "s_add_u32 m0, m0, 1024\n"
    "s_add_u32 s69, s69, 65536\n"
    "buffer_load_dwordx4 v32, s[44:47], s69 offen lds\n"
    "s_add_u32 m0, m0, 1024\n"
    "s_add_u32 s69, s69, 65536\n"
    "buffer_load_dwordx4 v33, s[44:47], s69 offen lds\n"
    "s_add_u32 m0, m0, 1024\n"
    "s_add_u32 s69, s69, 65536\n"
    "buffer_load_dwordx4 v32, s[44:47], s69 offen lds\n"
    "s_add_u32 m0, m0, 1024\n"
    "s_add_u32 s69, s69, 65536\n"
    "buffer_load_dwordx4 v33, s[44:47], s69 offen lds\n"
    "s_add_u32 m0, m0, 1024\n"
    "s_add_u32 s69, s69, 65536\n"
    "buffer_load_dwordx4 v32, s[44:47], s69 offen lds\n"
    "s_add_u32 m0, m0, 1024\n"
    "s_add_u32 s69, s69, 65536\n"
    "buffer_load_dwordx4 v33, s[44:47], s69 offen lds\n"
    "s_add_u32 s69, s75, 2\n"
    "s_and_b32 s69, s69, 3\n"
    "s_lshl_b32 s69, s69, 19\n"
    "s_add_u32 m0, s72, 16384\n"
    "s_nop 0\n"
    "buffer_load_dwordx4 v32, s[44:47], s69 offen lds\n"
    "s_add_u32 m0, m0, 1024\n"
    "s_add_u32 s69, s69, 65536\n"
    "buffer_load_dwordx4 v33, s[44:47], s69 offen lds\n"
    "s_add_u32 m0, m0, 1024\n"
    "s_add_u32 s69, s69, 65536\n"
    "buffer_load_dwordx4 v32, s[44:47], s69 offen lds\n"
    "s_add_u32 m0, m0, 1024\n"
    "s_add_u32 s69, s69, 65536\n"
    "buffer_load_dwordx4 v33, s[44:47], s69 offen lds\n"
    "s_add_u32 m0, m0, 1024\n"
    "s_add_u32 s69, s69, 65536\n"
    "buffer_load_dwordx4 v32, s[44:47], s69 offen lds\n"
    "s_add_u32 m0, m0, 1024\n"
    "s_add_u32 s69, s69, 65536\n"
    "buffer_load_dwordx4 v33, s[44:47], s69 offen lds\n"
    "s_add_u32 m0, m0, 1024\n"
    "s_add_u32 s69, s69, 65536\n"
    "buffer_load_dwordx4 v32, s[44:47], s69 offen lds\n"
    "s_add_u32 m0, m0, 1024\n"
    "s_add_u32 s69, s69, 65536\n"
    "buffer_load_dwordx4 v33, s[44:47], s69 offen lds\n"
    "s_add_u32 s69, s75, 3\n"
    "s_and_b32 s69, s69, 3\n"
    "s_lshl_b32 s69, s69, 19\n"
    "s_add_u32 m0, s72, 24576\n"
    "s_nop 0\n"
    "buffer_load_dwordx4 v32, s[44:47], s69 offen lds\n"
    "s_add_u32 m0, m0, 1024\n"
    "s_add_u32 s69, s69, 65536\n"
    "buffer_load_dwordx4 v33, s[44:47], s69 offen lds\n"
    "s_add_u32 m0, m0, 1024\n"
    "s_add_u32 s69, s69, 65536\n"
    "buffer_load_dwordx4 v32, s[44:47], s69 offen lds\n"
    "s_add_u32 m0, m0, 1024\n"
    "s_add_u32 s69, s69, 65536\n"
    "buffer_load_dwordx4 v33, s[44:47], s69 offen lds\n"
    "s_add_u32 m0, m0, 1024\n"
    "s_add_u32 s69, s69, 65536\n"
    "buffer_load_dwordx4 v32, s[44:47], s69 offen lds\n"
    "s_add_u32 m0, m0, 1024\n"
    "s_add_u32 s69, s69, 65536\n"
    "buffer_load_dwordx4 v33, s[44:47], s69 offen lds\n"
    "s_add_u32 m0, m0, 1024\n"
    "s_add_u32 s69, s69, 65536\n"
    "buffer_load_dwordx4 v32, s[44:47], s69 offen lds\n"
    "s_add_u32 m0, m0, 1024\n"
    "s_add_u32 s69, s69, 65536\n"
    "buffer_load_dwordx4 v33, s[44:47], s69 offen lds\n"
    "s_waitcnt vmcnt(24)\n"
    "v_min_u32 v54, -2, v54\n"
    "buffer_store_dword v54, v96, s[40:43], 0 offen sc1\n"
    "ds_read_b64_tr_b16 v[128:129], v38 offset:0\n"
    "ds_read_b64_tr_b16 v[130:131], v38 offset:256\n"
    "ds_read_b64_tr_b16 v[132:133], v38 offset:4096\n"
    "ds_read_b64_tr_b16 v[134:135], v38 offset:4352\n"
    "ds_read_b64_tr_b16 v[136:137], v39 offset:0\n"
    "ds_read_b64_tr_b16 v[138:139], v39 offset:256\n"
    "ds_read_b64_tr_b16 v[140:141], v39 offset:4096\n"
    "ds_read_b64_tr_b16 v[142:143], v39 offset:4352\n"
    "s_waitcnt lgkmcnt(0)\n"
    "ds_read_b64_tr_b16 v[144:145], v40 offset:0\n"
    "ds_read_b64_tr_b16 v[146:147], v40 offset:256\n"
    "ds_read_b64_tr_b16 v[148:149], v40 offset:4096\n"
    "ds_read_b64_tr_b16 v[150:151], v40 offset:4352\n"
    "ds_read_b64_tr_b16 v[152:153], v41 offset:0\n"
    "ds_read_b64_tr_b16 v[154:155], v41 offset:256\n"
    "ds_read_b64_tr_b16 v[156:157], v41 offset:4096\n"
    "ds_read_b64_tr_b16 v[158:159], v41 offset:4352\n"
    "s_waitcnt lgkmcnt(0)\n"
    "s_add_u32 s69, s75, 0\n"
    "s_and_b32 s69, s69, 7\n"
    "s_lshl_b32 s69, s69, 18\n"
    "s_add_u32 m0, s72, 0\n"
    "s_nop 0\n"
    "buffer_load_dwordx4 v34, s[48:51], s69 offen lds\n"
    "s_add_u32 m0, m0, 1024\n"
    "s_add_u32 s69, s69, 32768\n"
    "buffer_load_dwordx4 v35, s[48:51], s69 offen lds\n"
    "s_add_u32 m0, m0, 1024\n"
    "s_add_u32 s69, s69, 32768\n"
    "buffer_load_dwordx4 v34, s[48:51], s69 offen lds\n"
    "s_add_u32 m0, m0, 1024\n"
    "s_add_u32 s69, s69, 32768\n"
    "buffer_load_dwordx4 v35, s[48:51], s69 offen lds\n"
    "s_add_u32 m0, m0, 1024\n"
    "s_add_u32 s69, s69, 32768\n"
    "buffer_load_dwordx4 v34, s[48:51], s69 offen lds\n"
    "s_add_u32 m0, m0, 1024\n"
    "s_add_u32 s69, s69, 32768\n"
    "buffer_load_dwordx4 v35, s[48:51], s69 offen lds\n"
    "s_add_u32 m0, m0, 1024\n"
    "s_add_u32 s69, s69, 32768\n"
    "buffer_load_dwordx4 v34, s[48:51], s69 offen lds\n"
    "s_add_u32 m0, m0, 1024\n"
    "s_add_u32 s69, s69, 32768\n"
    "buffer_load_dwordx4 v35, s[48:51], s69 offen lds\n"
    "s_waitcnt vmcnt(24)\n"
    "ds_read_b64_tr_b16 v[160:161], v38 offset:8192\n"
    "ds_read_b64_tr_b16 v[162:163], v38 offset:8448\n"
    "ds_read_b64_tr_b16 v[164:165], v38 offset:12288\n"
    "ds_read_b64_tr_b16 v[166:167], v38 offset:12544\n"
    "ds_read_b64_tr_b16 v[168:169], v39 offset:8192\n"
    "ds_read_b64_tr_b16 v[170:171], v39 offset:8448\n"
    "ds_read_b64_tr_b16 v[172:173], v39 offset:12288\n"
    "ds_read_b64_tr_b16 v[174:175], v39 offset:12544\n"
    "s_waitcnt lgkmcnt(0)\n"
    "ds_read_b64_tr_b16 v[176:177], v40 offset:8192\n"
    "ds_read_b64_tr_b16 v[178:179], v40 offset:8448\n"
    "ds_read_b64_tr_b16 v[180:181], v40 offset:12288\n"
    "ds_read_b64_tr_b16 v[182:183], v40 offset:12544\n"
    "ds_read_b64_tr_b16 v[184:185], v41 offset:8192\n"
    "ds_read_b64_tr_b16 v[186:187], v41 offset:8448\n"
    "ds_read_b64_tr_b16 v[188:189], v41 offset:12288\n"
    "ds_read_b64_tr_b16 v[190:191], v41 offset:12544\n"
    "s_waitcnt lgkmcnt(0)\n"
    "s_add_u32 s69, s75, 1\n"
    "s_and_b32 s69, s69, 7\n"
    "s_lshl_b32 s69, s69, 18\n"
    "s_add_u32 m0, s72, 8192\n"
    "s_nop 0\n"
    "buffer_load_dwordx4 v34, s[48:51], s69 offen lds\n"
    "s_add_u32 m0, m0, 1024\n"
    "s_add_u32 s69, s69, 32768\n"
    "buffer_load_dwordx4 v35, s[48:51], s69 offen lds\n"
    "s_add_u32 m0, m0, 1024\n"
    "s_add_u32 s69, s69, 32768\n"
    "buffer_load_dwordx4 v34, s[48:51], s69 offen lds\n"
    "s_add_u32 m0, m0, 1024\n"
    "s_add_u32 s69, s69, 32768\n"
    "buffer_load_dwordx4 v35, s[48:51], s69 offen lds\n"
    "s_add_u32 m0, m0, 1024\n"
    "s_add_u32 s69, s69, 32768\n"
    "buffer_load_dwordx4 v34, s[48:51], s69 offen lds\n"
    "s_add_u32 m0, m0, 1024\n"
    "s_add_u32 s69, s69, 32768\n"
    "buffer_load_dwordx4 v35, s[48:51], s69 offen lds\n"
    "s_add_u32 m0, m0, 1024\n"
    "s_add_u32 s69, s69, 32768\n"
    "buffer_load_dwordx4 v34, s[48:51], s69 offen lds\n"
    "s_add_u32 m0, m0, 1024\n"
    "s_add_u32 s69, s69, 32768\n"
    "buffer_load_dwordx4 v35, s[48:51], s69 offen lds\n"
    "s_waitcnt vmcnt(24)\n"
    "ds_read_b64_tr_b16 v[192:193], v38 offset:16384\n"
    "ds_read_b64_tr_b16 v[194:195], v38 offset:16640\n"
    "ds_read_b64_tr_b16 v[196:197], v38 offset:20480\n"
    "ds_read_b64_tr_b16 v[198:199], v38 offset:20736\n"
    "ds_read_b64_tr_b16 v[200:201], v39 offset:16384\n"
    "ds_read_b64_tr_b16 v[202:203], v39 offset:16640\n"
    "ds_read_b64_tr_b16 v[204:205], v39 offset:20480\n"
    "ds_read_b64_tr_b16 v[206:207], v39 offset:20736\n"
    "s_waitcnt lgkmcnt(0)\n"
    "ds_read_b64_tr_b16 v[208:209], v40 offset:16384\n"
    "ds_read_b64_tr_b16 v[210:211], v40 offset:16640\n"
    "ds_read_b64_tr_b16 v[212:213], v40 offset:20480\n"
    "ds_read_b64_tr_b16 v[214:215], v40 offset:20736\n"
    "ds_read_b64_tr_b16 v[216:217], v41 offset:16384\n"
    "ds_read_b64_tr_b16 v[218:219], v41 offset:16640\n"
    "ds_read_b64_tr_b16 v[220:221], v41 offset:20480\n"
    "ds_read_b64_tr_b16 v[222:223], v41 offset:20736\n"
    "s_waitcnt lgkmcnt(0)\n"
    "s_add_u32 s69, s75, 2\n"
    "s_and_b32 s69, s69, 7\n"
    "s_lshl_b32 s69, s69, 18\n"
    "s_add_u32 m0, s72, 16384\n"
    "s_nop 0\n"
    "buffer_load_dwordx4 v34, s[48:51], s69 offen lds\n"
    "s_add_u32 m0, m0, 1024\n"
    "s_add_u32 s69, s69, 32768\n"
    "buffer_load_dwordx4 v35, s[48:51], s69 offen lds\n"
    "s_add_u32 m0, m0, 1024\n"
    "s_add_u32 s69, s69, 32768\n"
    "buffer_load_dwordx4 v34, s[48:51], s69 offen lds\n"
    "s_add_u32 m0, m0, 1024\n"
    "s_add_u32 s69, s69, 32768\n"
    "buffer_load_dwordx4 v35, s[48:51], s69 offen lds\n"
    "s_add_u32 m0, m0, 1024\n"
    "s_add_u32 s69, s69, 32768\n"
    "buffer_load_dwordx4 v34, s[48:51], s69 offen lds\n"
    "s_add_u32 m0, m0, 1024\n"
    "s_add_u32 s69, s69, 32768\n"
    "buffer_load_dwordx4 v35, s[48:51], s69 offen lds\n"
    "s_add_u32 m0, m0, 1024\n"
    "s_add_u32 s69, s69, 32768\n"
    "buffer_load_dwordx4 v34, s[48:51], s69 offen lds\n"
    "s_add_u32 m0, m0, 1024\n"
    "s_add_u32 s69, s69, 32768\n"
    "buffer_load_dwordx4 v35, s[48:51], s69 offen lds\n"
    "s_waitcnt vmcnt(24)\n"
    "ds_read_b64_tr_b16 v[224:225], v38 offset:24576\n"
    "ds_read_b64_tr_b16 v[226:227], v38 offset:24832\n"
    "ds_read_b64_tr_b16 v[228:229], v38 offset:28672\n"
    "ds_read_b64_tr_b16 v[230:231], v38 offset:28928\n"
    "ds_read_b64_tr_b16 v[232:233], v39 offset:24576\n"
    "ds_read_b64_tr_b16 v[234:235], v39 offset:24832\n"
    "ds_read_b64_tr_b16 v[236:237], v39 offset:28672\n"
    "ds_read_b64_tr_b16 v[238:239], v39 offset:28928\n"
    "s_waitcnt lgkmcnt(0)\n"
    "ds_read_b64_tr_b16 v[240:241], v40 offset:24576\n"
    "ds_read_b64_tr_b16 v[242:243], v40 offset:24832\n"
    "ds_read_b64_tr_b16 v[244:245], v40 offset:28672\n"
    "ds_read_b64_tr_b16 v[246:247], v40 offset:28928\n"
    "ds_read_b64_tr_b16 v[248:249], v41 offset:24576\n"
    "ds_read_b64_tr_b16 v[250:251], v41 offset:24832\n"
    "ds_read_b64_tr_b16 v[252:253], v41 offset:28672\n"
    "ds_read_b64_tr_b16 v[254:255], v41 offset:28928\n"
    "s_waitcnt lgkmcnt(0)\n"
    "s_add_u32 s69, s75, 3\n"
    "s_and_b32 s69, s69, 7\n"
    "s_lshl_b32 s69, s69, 18\n"
    "s_add_u32 m0, s72, 24576\n"
    "s_nop 0\n"
    "buffer_load_dwordx4 v34, s[48:51], s69 offen lds\n"
    "s_add_u32 m0, m0, 1024\n"
    "s_add_u32 s69, s69, 32768\n"
    "buffer_load_dwordx4 v35, s[48:51], s69 offen lds\n"
    "s_add_u32 m0, m0, 1024\n"
    "s_add_u32 s69, s69, 32768\n"
    "buffer_load_dwordx4 v34, s[48:51], s69 offen lds\n"
    "s_add_u32 m0, m0, 1024\n"
    "s_add_u32 s69, s69, 32768\n"
    "buffer_load_dwordx4 v35, s[48:51], s69 offen lds\n"
    "s_add_u32 m0, m0, 1024\n"
    "s_add_u32 s69, s69, 32768\n"
    "buffer_load_dwordx4 v34, s[48:51], s69 offen lds\n"
    "s_add_u32 m0, m0, 1024\n"
    "s_add_u32 s69, s69, 32768\n"
    "buffer_load_dwordx4 v35, s[48:51], s69 offen lds\n"
    "s_add_u32 m0, m0, 1024\n"
    "s_add_u32 s69, s69, 32768\n"
    "buffer_load_dwordx4 v34, s[48:51], s69 offen lds\n"
    "s_add_u32 m0, m0, 1024\n"
    "s_add_u32 s69, s69, 32768\n"
    "buffer_load_dwordx4 v35, s[48:51], s69 offen lds\n"
    "s_waitcnt vmcnt(24)\n"
    "s_barrier\n"
    "v_readfirstlane_b32 s64, v50\n"
    "v_readfirstlane_b32 s65, v51\n"
    "s_nop 0\n"
    "s_mul_i32 s63, s65, 0x85ebca77\n"
    "s_mul_i32 s64, s64, 0x9e3779b1\n"
    "s_add_u32 s64, s64, s63\n"
    "s_add_u32 s64, s64, 0x7f4a7c15\n"
    "s_lshr_b32 s63, s64, 15\n"
    "s_xor_b32 s64, s64, s63\n"
    "s_mul_i32 s63, s64, 0x27d4eb2f\n"
    "s_mul_i32 s65, s65, 0xc2b2ae3d\n"
    "s_add_u32 s65, s65, s63\n"
    "s_add_u32 s65, s65, 0x165667b1\n"
    "s_lshr_b32 s63, s65, 13\n"
    "s_xor_b32 s65, s65, s63\n"
    "s_andn2_b32 s64, s64, 1\n"
    "v_mov_b32 v50, s64\n"
    "v_mov_b32 v51, s65\n"
    "buffer_store_dwordx2 v[50:51], v97, s[40:43], 0 offen sc1\n"
    "ds_read_b64_tr_b16 a[0:1], v42 offset:0\n"
    "ds_read_b64_tr_b16 a[2:3], v44 offset:0\n"
    "ds_read_b64_tr_b16 a[4:5], v42 offset:2048\n"
    "ds_read_b64_tr_b16 a[6:7], v44 offset:2048\n"
    "ds_read_b64_tr_b16 a[8:9], v43 offset:0\n"
    "ds_read_b64_tr_b16 a[10:11], v45 offset:0\n"
    "ds_read_b64_tr_b16 a[12:13], v43 offset:2048\n"
    "ds_read_b64_tr_b16 a[14:15], v45 offset:2048\n"
    "s_waitcnt lgkmcnt(0)\n"
    "ds_read_b64_tr_b16 a[16:17], v42 offset:4096\n"
    "ds_read_b64_tr_b16 a[18:19], v44 offset:4096\n"
    "ds_read_b64_tr_b16 a[20:21], v42 offset:6144\n"
    "ds_read_b64_tr_b16 a[22:23], v44 offset:6144\n"
    "ds_read_b64_tr_b16 a[24:25], v43 offset:4096\n"
    "ds_read_b64_tr_b16 a[26:27], v45 offset:4096\n"
    "ds_read_b64_tr_b16 a[28:29], v43 offset:6144\n"
    "ds_read_b64_tr_b16 a[30:31], v45 offset:6144\n"
    "s_waitcnt lgkmcnt(0)\n"
    "s_add_u32 s69, s75, 4\n"
    "s_and_b32 s69, s69, 7\n"
    "s_lshl_b32 s69, s69, 18\n"
    "s_add_u32 m0, s72, 0\n"
    "s_nop 0\n"
    "buffer_load_dwordx4 v34, s[48:51], s69 offen lds\n"
    "s_add_u32 m0, m0, 1024\n"
    "s_add_u32 s69, s69, 32768\n"
    "buffer_load_dwordx4 v35, s[48:51], s69 offen lds\n"
    "s_add_u32 m0, m0, 1024\n"
    "s_add_u32 s69, s69, 32768\n"
    "buffer_load_dwordx4 v34, s[48:51], s69 offen lds\n"
    "s_add_u32 m0, m0, 1024\n"
    "s_add_u32 s69, s69, 32768\n"
    "buffer_load_dwordx4 v35, s[48:51], s69 offen lds\n"
    "s_add_u32 m0, m0, 1024\n"
    "s_add_u32 s69, s69, 32768\n"
    "buffer_load_dwordx4 v34, s[48:51], s69 offen lds\n"
    "s_add_u32 m0, m0, 1024\n"
    "s_add_u32 s69, s69, 32768\n"
    "buffer_load_dwordx4 v35, s[48:51], s69 offen lds\n"
    "s_add_u32 m0, m0, 1024\n"
    "s_add_u32 s69, s69, 32768\n"
    "buffer_load_dwordx4 v34, s[48:51], s69 offen lds\n"
    "s_add_u32 m0, m0, 1024\n"
    "s_add_u32 s69, s69, 32768\n"
    "buffer_load_dwordx4 v35, s[48:51], s69 offen lds\n"
    "s_waitcnt vmcnt(24)\n"
    "ds_read_b64_tr_b16 a[32:33], v42 offset:8192\n"
    "ds_read_b64_tr_b16 a[34:35], v44 offset:8192\n"
    "ds_read_b64_tr_b16 a[36:37], v42 offset:10240\n"
    "ds_read_b64_tr_b16 a[38:39], v44 offset:10240\n"
    "ds_read_b64_tr_b16 a[40:41], v43 offset:8192\n"
    "ds_read_b64_tr_b16 a[42:43], v45 offset:8192\n"
    "ds_read_b64_tr_b16 a[44:45], v43 offset:10240\n"
    "ds_read_b64_tr_b16 a[46:47], v45 offset:10240\n"
    "s_waitcnt lgkmcnt(0)\n"
    "ds_read_b64_tr_b16 a[48:49], v42 offset:12288\n"
    "ds_read_b64_tr_b16 a[50:51], v44 offset:12288\n"
    "ds_read_b64_tr_b16 a[52:53], v42 offset:14336\n"
    "ds_read_b64_tr_b16 a[54:55], v44 offset:14336\n"
    "ds_read_b64_tr_b16 a[56:57], v43 offset:12288\n"
    "ds_read_b64_tr_b16 a[58:59], v45 offset:12288\n"
    "ds_read_b64_tr_b16 a[60:61], v43 offset:14336\n"
    "ds_read_b64_tr_b16 a[62:63], v45 offset:14336\n"
    "s_waitcnt lgkmcnt(0)\n"
    "s_add_u32 s69, s75, 5\n"
    "s_and_b32 s69, s69, 7\n"
    "s_lshl_b32 s69, s69, 18\n"
    "s_add_u32 m0, s72, 8192\n"
    "s_nop 0\n"
    "buffer_load_dwordx4 v34, s[48:51], s69 offen lds\n"
    "s_add_u32 m0, m0, 1024\n"
    "s_add_u32 s69, s69, 32768\n"
    "buffer_load_dwordx4 v35, s[48:51], s69 offen lds\n"
    "s_add_u32 m0, m0, 1024\n"
    "s_add_u32 s69, s69, 32768\n"
    "buffer_load_dwordx4 v34, s[48:51], s69 offen lds\n"
    "s_add_u32 m0, m0, 1024\n"
    "s_add_u32 s69, s69, 32768\n"
    "buffer_load_dwordx4 v35, s[48:51], s69 offen lds\n"
    "s_add_u32 m0, m0, 1024\n"
    "s_add_u32 s69, s69, 32768\n"
    "buffer_load_dwordx4 v34, s[48:51], s69 offen lds\n"
    "s_add_u32 m0, m0, 1024\n"
    "s_add_u32 s69, s69, 32768\n"
    "buffer_load_dwordx4 v35, s[48:51], s69 offen lds\n"
    "s_add_u32 m0, m0, 1024\n"
    "s_add_u32 s69, s69, 32768\n"
    "buffer_load_dwordx4 v34, s[48:51], s69 offen lds\n"
    "s_add_u32 m0, m0, 1024\n"
    "s_add_u32 s69, s69, 32768\n"
    "buffer_load_dwordx4 v35, s[48:51], s69 offen lds\n"
    "s_waitcnt vmcnt(24)\n"
    "ds_read_b64_tr_b16 a[64:65], v42 offset:16384\n"
    "ds_read_b64_tr_b16 a[66:67], v44 offset:16384\n"
    "ds_read_b64_tr_b16 a[68:69], v42 offset:18432\n"
    "ds_read_b64_tr_b16 a[70:71], v44 offset:18432\n"
    "ds_read_b64_tr_b16 a[72:73], v43 offset:16384\n"
    "ds_read_b64_tr_b16 a[74:75], v45 offset:16384\n"
    "ds_read_b64_tr_b16 a[76:77], v43 offset:18432\n"
    "ds_read_b64_tr_b16 a[78:79], v45 offset:18432\n"
    "s_waitcnt lgkmcnt(0)\n"
    "ds_read_b64_tr_b16 a[80:81], v42 offset:20480\n"
    "ds_read_b64_tr_b16 a[82:83], v44 offset:20480\n"
    "ds_read_b64_tr_b16 a[84:85], v42 offset:22528\n"
    "ds_read_b64_tr_b16 a[86:87], v44 offset:22528\n"
    "ds_read_b64_tr_b16 a[88:89], v43 offset:20480\n"
    "ds_read_b64_tr_b16 a[90:91], v45 offset:20480\n"
    "ds_read_b64_tr_b16 a[92:93], v43 offset:22528\n"
    "ds_read_b64_tr_b16 a[94:95], v45 offset:22528\n"
    "s_waitcnt lgkmcnt(0)\n"
    "s_add_u32 s69, s75, 6\n"
    "s_and_b32 s69, s69, 7\n"
    "s_lshl_b32 s69, s69, 18\n"
    "s_add_u32 m0, s72, 16384\n"
    "s_nop 0\n"
    "buffer_load_dwordx4 v34, s[48:51], s69 offen lds\n"
    "s_add_u32 m0, m0, 1024\n"
    "s_add_u32 s69, s69, 32768\n"
    "buffer_load_dwordx4 v35, s[48:51], s69 offen lds\n"
    "s_add_u32 m0, m0, 1024\n"
    "s_add_u32 s69, s69, 32768\n"
    "buffer_load_dwordx4 v34, s[48:51], s69 offen lds\n"
    "s_add_u32 m0, m0, 1024\n"
    "s_add_u32 s69, s69, 32768\n"
    "buffer_load_dwordx4 v35, s[48:51], s69 offen lds\n"
    "s_add_u32 m0, m0, 1024\n"
    "s_add_u32 s69, s69, 32768\n"
    "buffer_load_dwordx4 v34, s[48:51], s69 offen lds\n"
    "s_add_u32 m0, m0, 1024\n"
    "s_add_u32 s69, s69, 32768\n"
    "buffer_load_dwordx4 v35, s[48:51], s69 offen lds\n"
    "s_add_u32 m0, m0, 1024\n"
    "s_add_u32 s69, s69, 32768\n"
    "buffer_load_dwordx4 v34, s[48:51], s69 offen lds\n"
    "s_add_u32 m0, m0, 1024\n"
    "s_add_u32 s69, s69, 32768\n"
    "buffer_load_dwordx4 v35, s[48:51], s69 offen lds\n"
    "s_waitcnt vmcnt(24)\n"
    "ds_read_b64_tr_b16 a[96:97], v42 offset:24576\n"
    "ds_read_b64_tr_b16 a[98:99], v44 offset:24576\n"
    "ds_read_b64_tr_b16 a[100:101], v42 offset:26624\n"
    "ds_read_b64_tr_b16 a[102:103], v44 offset:26624\n"
    "ds_read_b64_tr_b16 a[104:105], v43 offset:24576\n"
    "ds_read_b64_tr_b16 a[106:107], v45 offset:24576\n"
    "ds_read_b64_tr_b16 a[108:109], v43 offset:26624\n"
    "ds_read_b64_tr_b16 a[110:111], v45 offset:26624\n"
    "s_waitcnt lgkmcnt(0)\n"
    "ds_read_b64_tr_b16 a[112:113], v42 offset:28672\n"
    "ds_read_b64_tr_b16 a[114:115], v44 offset:28672\n"
    "ds_read_b64_tr_b16 a[116:117], v42 offset:30720\n"
    "ds_read_b64_tr_b16 a[118:119], v44 offset:30720\n"
    "ds_read_b64_tr_b16 a[120:121], v43 offset:28672\n"
    "ds_read_b64_tr_b16 a[122:123], v45 offset:28672\n"
    "ds_read_b64_tr_b16 a[124:125], v43 offset:30720\n"
    "ds_read_b64_tr_b16 a[126:127], v45 offset:30720\n"
    "s_waitcnt lgkmcnt(0)\n"
    "s_add_u32 s69, s75, 7\n"
    "s_and_b32 s69, s69, 7\n"
    "s_lshl_b32 s69, s69, 18\n"
    "s_add_u32 m0, s72, 24576\n"
    "s_nop 0\n"
    "buffer_load_dwordx4 v34, s[48:51], s69 offen lds\n"
    "s_add_u32 m0, m0, 1024\n"
    "s_add_u32 s69, s69, 32768\n"
    "buffer_load_dwordx4 v35, s[48:51], s69 offen lds\n"
    "s_add_u32 m0, m0, 1024\n"
    "s_add_u32 s69, s69, 32768\n"
    "buffer_load_dwordx4 v34, s[48:51], s69 offen lds\n"
    "s_add_u32 m0, m0, 1024\n"
    "s_add_u32 s69, s69, 32768\n"
    "buffer_load_dwordx4 v35, s[48:51], s69 offen lds\n"
    "s_add_u32 m0, m0, 1024\n"
    "s_add_u32 s69, s69, 32768\n"
    "buffer_load_dwordx4 v34, s[48:51], s69 offen lds\n"
    "s_add_u32 m0, m0, 1024\n"
    "s_add_u32 s69, s69, 32768\n"
    "buffer_load_dwordx4 v35, s[48:51], s69 offen lds\n"
    "s_add_u32 m0, m0, 1024\n"
    "s_add_u32 s69, s69, 32768\n"
    "buffer_load_dwordx4 v34, s[48:51], s69 offen lds\n"
    "s_add_u32 m0, m0, 1024\n"
    "s_add_u32 s69, s69, 32768\n"
    "buffer_load_dwordx4 v35, s[48:51], s69 offen lds\n"
    "s_waitcnt vmcnt(24)\n"
    "ds_read_b64_tr_b16 a[128:129], v42 offset:0\n"
    "ds_read_b64_tr_b16 a[130:131], v44 offset:0\n"
    "ds_read_b64_tr_b16 a[132:133], v42 offset:2048\n"
    "ds_read_b64_tr_b16 a[134:135], v44 offset:2048\n"
    "ds_read_b64_tr_b16 a[136:137], v43 offset:0\n"
    "ds_read_b64_tr_b16 a[138:139], v45 offset:0\n"
    "ds_read_b64_tr_b16 a[140:141], v43 offset:2048\n"
    "ds_read_b64_tr_b16 a[142:143], v45 offset:2048\n"
    "s_waitcnt lgkmcnt(0)\n"
    "ds_read_b64_tr_b16 a[144:145], v42 offset:4096\n"
    "ds_read_b64_tr_b16 a[146:147], v44 offset:4096\n"
    "ds_read_b64_tr_b16 a[148:149], v42 offset:6144\n"
    "ds_read_b64_tr_b16 a[150:151], v44 offset:6144\n"
    "ds_read_b64_tr_b16 a[152:153], v43 offset:4096\n"
    "ds_read_b64_tr_b16 a[154:155], v45 offset:4096\n"
    "ds_read_b64_tr_b16 a[156:157], v43 offset:6144\n"
    "ds_read_b64_tr_b16 a[158:159], v45 offset:6144\n"
    "s_waitcnt lgkmcnt(0)\n"
    "s_waitcnt vmcnt(16)\n"
    "ds_read_b64_tr_b16 a[160:161], v42 offset:8192\n"
    "ds_read_b64_tr_b16 a[162:163], v44 offset:8192\n"
    "ds_read_b64_tr_b16 a[164:165], v42 offset:10240\n"
    "ds_read_b64_tr_b16 a[166:167], v44 offset:10240\n"
    "ds_read_b64_tr_b16 a[168:169], v43 offset:8192\n"
    "ds_read_b64_tr_b16 a[170:171], v45 offset:8192\n"
    "ds_read_b64_tr_b16 a[172:173], v43 offset:10240\n"
    "ds_read_b64_tr_b16 a[174:175], v45 offset:10240\n"
    "s_waitcnt lgkmcnt(0)\n"
    "ds_read_b64_tr_b16 a[176:177], v42 offset:12288\n"
    "ds_read_b64_tr_b16 a[178:179], v44 offset:12288\n"
    "ds_read_b64_tr_b16 a[180:181], v42 offset:14336\n"
    "ds_read_b64_tr_b16 a[182:183], v44 offset:14336\n"
    "ds_read_b64_tr_b16 a[184:185], v43 offset:12288\n"
    "ds_read_b64_tr_b16 a[186:187], v45 offset:12288\n"
    "ds_read_b64_tr_b16 a[188:189], v43 offset:14336\n"
    "ds_read_b64_tr_b16 a[190:191], v45 offset:14336\n"
    "s_waitcnt lgkmcnt(0)\n"
    "s_waitcnt vmcnt(8)\n"
    "ds_read_b64_tr_b16 a[192:193], v42 offset:16384\n"
    "ds_read_b64_tr_b16 a[194:195], v44 offset:16384\n"
    "ds_read_b64_tr_b16 a[196:197], v42 offset:18432\n"
    "ds_read_b64_tr_b16 a[198:199], v44 offset:18432\n"
    "ds_read_b64_tr_b16 a[200:201], v43 offset:16384\n"
    "ds_read_b64_tr_b16 a[202:203], v45 offset:16384\n"
    "ds_read_b64_tr_b16 a[204:205], v43 offset:18432\n"
    "ds_read_b64_tr_b16 a[206:207], v45 offset:18432\n"
    "s_waitcnt lgkmcnt(0)\n"
    "ds_read_b64_tr_b16 a[208:209], v42 offset:20480\n"
    "ds_read_b64_tr_b16 a[210:211], v44 offset:20480\n"
    "ds_read_b64_tr_b16 a[212:213], v42 offset:22528\n"
    "ds_read_b64_tr_b16 a[214:215], v44 offset:22528\n"
    "ds_read_b64_tr_b16 a[216:217], v43 offset:20480\n"
    "ds_read_b64_tr_b16 a[218:219], v45 offset:20480\n"
    "ds_read_b64_tr_b16 a[220:221], v43 offset:22528\n"
    "ds_read_b64_tr_b16 a[222:223], v45 offset:22528\n"
    "s_waitcnt lgkmcnt(0)\n"
    "v_mbcnt_lo_u32_b32 v68, -1, 0\n"
    "v_mbcnt_hi_u32_b32 v68, -1, v68\n"
    "v_min_u32 v68, 48, v68\n"
    "v_lshlrev_b32 v68, 4, v68\n"
    "v_add_u32 v68, 1573376, v68\n"
    "buffer_load_dwordx4 v[64:67], v68, s[40:43], 0 offen sc1\n"
    "s_getpc_b64 s[100:101]\n"
    ".LB_tc%=:\n"
    "v_mbcnt_lo_u32_b32 v70, -1, 0\n"
    "v_mbcnt_hi_u32_b32 v70, -1, v70\n"
    "v_lshlrev_b32 v70, 7, v70\n"
    "v_min_u32 v70, .LB_fin%=-.LB_tc%=, v70\n"
    "global_load_dword v70, v70, s[100:101]\n"
    "s_waitcnt vmcnt(0)\n"
    "ds_read_b64_tr_b16 a[224:225], v42 offset:24576\n"
    "ds_read_b64_tr_b16 a[226:227], v44 offset:24576\n"
    "ds_read_b64_tr_b16 a[228:229], v42 offset:26624\n"
    "ds_read_b64_tr_b16 a[230:231], v44 offset:26624\n"
    "ds_read_b64_tr_b16 a[232:233], v43 offset:24576\n"
    "ds_read_b64_tr_b16 a[234:235], v45 offset:24576\n"
    "ds_read_b64_tr_b16 a[236:237], v43 offset:26624\n"
    "ds_read_b64_tr_b16 a[238:239], v45 offset:26624\n"
    "s_waitcnt lgkmcnt(0)\n"
    "ds_read_b64_tr_b16 a[240:241], v42 offset:28672\n"
    "ds_read_b64_tr_b16 a[242:243], v44 offset:28672\n"
    "ds_read_b64_tr_b16 a[244:245], v42 offset:30720\n"
    "ds_read_b64_tr_b16 a[246:247], v44 offset:30720\n"
    "ds_read_b64_tr_b16 a[248:249], v43 offset:28672\n"
    "ds_read_b64_tr_b16 a[250:251], v45 offset:28672\n"
    "ds_read_b64_tr_b16 a[252:253], v43 offset:30720\n"
    "ds_read_b64_tr_b16 a[254:255], v45 offset:30720\n"
    "s_waitcnt lgkmcnt(0)\n"
    "s_add_u32 s69, s75, 0\n"
    "s_and_b32 s69, s69, 3\n"
    "s_lshl_b32 s69, s69, 10\n"
    "v_add_u32 v116, s69, v0\n"
    "s_add_u32 s69, s75, 1\n"
    "s_and_b32 s69, s69, 3\n"
    "s_lshl_b32 s69, s69, 10\n"
    "v_add_u32 v117, s69, v0\n"
    "s_add_u32 s69, s75, 2\n"
    "s_and_b32 s69, s69, 3\n"
    "s_lshl_b32 s69, s69, 10\n"
    "v_add_u32 v118, s69, v0\n"
    "s_add_u32 s69, s75, 3\n"
    "s_and_b32 s69, s69, 3\n"
    "s_lshl_b32 s69, s69, 10\n"
    "v_add_u32 v119, s69, v0\n"
    "s_add_u32 s69, s75, 0\n"
    "s_and_b32 s69, s69, 7\n"
    "s_lshl_b32 s69, s69, 10\n"
    "v_add_u32 v120, s69, v1\n"
    "s_add_u32 s69, s75, 1\n"
    "s_and_b32 s69, s69, 7\n"
    "s_lshl_b32 s69, s69, 10\n"
    "v_add_u32 v121, s69, v1\n"
    "s_add_u32 s69, s75, 2\n"
    "s_and_b32 s69, s69, 7\n"
    "s_lshl_b32 s69, s69, 10\n"
    "v_add_u32 v122, s69, v1\n"
    "s_add_u32 s69, s75, 3\n"
    "s_and_b32 s69, s69, 7\n"
    "s_lshl_b32 s69, s69, 10\n"
    "v_add_u32 v123, s69, v1\n"
    "s_add_u32 s69, s75, 4\n"
    "s_and_b32 s69, s69, 7\n"
    "s_lshl_b32 s69, s69, 10\n"
    "v_add_u32 v124, s69, v1\n"
    "s_add_u32 s69, s75, 5\n"
    "s_and_b32 s69, s69, 7\n"
    "s_lshl_b32 s69, s69, 10\n"
    "v_add_u32 v125, s69, v1\n"
    "s_add_u32 s69, s75, 6\n"
    "s_and_b32 s69, s69, 7\n"
    "s_lshl_b32 s69, s69, 10\n"
    "v_add_u32 v126, s69, v1\n"
    "s_add_u32 s69, s75, 7\n"
    "s_and_b32 s69, s69, 7\n"
    "s_lshl_b32 s69, s69, 10\n"
    "v_add_u32 v127, s69, v1\n"
    "s_mov_b32 s68, 0\n"
    ".LB_rdy%=:\n"
    "v_readlane_b32 s69, v66, 48\n"
    "v_xor_b32 v64, s64, v64\n"
    "v_xor_b32 v65, s65, v65\n"
    "v_xor_b32 v66, s64, v66\n"
    "v_xor_b32 v67, s65, v67\n"
    "v_or_b32 v64, v64, v66\n"
    "v_lshrrev_b32 v64, 1, v64\n"
    "v_or3_b32 v64, v64, v65, v67\n"
    "v_cmp_ne_u32 vcc, 0, v64\n"
    "s_cbranch_vccz .LB_rdyok%=\n"
    "buffer_load_dwordx4 v[64:67], v68, s[40:43], 0 offen sc1\n"
    "s_waitcnt vmcnt(0)\n"
    "s_add_u32 s68, s68, 1\n"
    "s_cmp_lt_u32 s68, 65536\n"
    "s_cbranch_scc1 .LB_rdy%=\n"
    "s_branch .LB_tmo%=\n"
    ".LB_rdyok%=:\n"
    "s_and_b32 s69, s69, 1\n"
    "s_lshl_b32 s61, s69, 13\n"
    "s_add_u32 s64, s61, 327680\n"
    "s_add_u32 s63, s61, 655360\n"
    "s_mov_b32 s60, 0\n"
    "v_add_u32 v98, s61, v98\n"
    "v_add_u32 v99, s61, v99\n"
    "s_memrealtime s[100:101]\n"
    "s_waitcnt lgkmcnt(0)\n"
    "s_lshl_b32 s100, s100, 3\n"
    "s_sub_u32 s59, s100, 0x400000\n"
    "s_mov_b32 s62, 600\n"
    "s_mov_b32 s38, s59\n"
    "v_mov_b64 v[100:101], -1\n"
    "v_mov_b64 v[102:103], -1\n"
    "v_mov_b64 v[64:65], 0\n"
    "v_mov_b64 v[66:67], 0\n"
    "v_mov_b64 v[68:69], 0\n"
    "v_mov_b64 v[70:71], 0\n"
    "v_mov_b64 v[72:73], 0\n"
    "v_mov_b64 v[74:75], 0\n"
    "v_mov_b64 v[76:77], 0\n"
    "v_mov_b64 v[78:79], 0\n"
    "s_mov_b32 s68, 0\n"
    ".LB_top%=:\n"
    "buffer_load_dwordx4 v[16:19], v116, s[40:43], s61 offen sc1\n"
    "s_nop 7\n"
    "buffer_load_dwordx4 v[20:23], v117, s[40:43], s61 offen sc1\n"
    "s_nop 7\n"
    "buffer_load_dwordx4 v[24:27], v118, s[40:43], s61 offen sc1\n"
    "s_nop 7\n"
    "buffer_load_dwordx4 v[28:31], v119, s[40:43], s61 offen sc1\n"
    "s_sleep 1\n"
    "v_mov_b32 v97, s38\n"
    "buffer_store_dword v97, v99, s[40:43], 0 offen sc1\n"
    ".LB_ms0%=:\n"
    "s_waitcnt vmcnt(4)\n"
    "v_smfmac_f32_16x16x64_bf16 v[64:67], v[16:19], v[128:135], v4\n"
    "v_max3_u32 v96, v16, v17, v16\n"
    "v_smfmac_f32_16x16x64_bf16 v[68:71], v[16:19], v[136:143], v4\n"
    "v_max3_u32 v96, v18, v19, v96\n"
    "v_smfmac_f32_16x16x64_bf16 v[72:75], v[16:19], v[144:151], v4\n"
    "v_cmp_eq_u32 vcc, -1, v96\n"
    "v_smfmac_f32_16x16x64_bf16 v[76:79], v[16:19], v[152:159], v4\n"
    "s_cbranch_vccnz .LB_mredo%=\n"
    "s_waitcnt vmcnt(3)\n"
    "v_smfmac_f32_16x16x64_bf16 v[64:67], v[20:23], v[160:167], v4\n"
    "v_max3_u32 v96, v20, v21, v96\n"
    "v_smfmac_f32_16x16x64_bf16 v[68:71], v[20:23], v[168:175], v4\n"
    "v_max3_u32 v96, v22, v23, v96\n"
    "v_smfmac_f32_16x16x64_bf16 v[72:75], v[20:23], v[176:183], v4\n"
    "v_smfmac_f32_16x16x64_bf16 v[76:79], v[20:23], v[184:191], v4\n"
    "s_waitcnt vmcnt(2)\n"
    "v_smfmac_f32_16x16x64_bf16 v[64:67], v[24:27], v[192:199], v4\n"
    "v_max3_u32 v96, v24, v25, v96\n"
    "v_smfmac_f32_16x16x64_bf16 v[68:71], v[24:27], v[200:207], v4\n"
    "v_max3_u32 v96, v26, v27, v96\n"
    "v_smfmac_f32_16x16x64_bf16 v[72:75], v[24:27], v[208:215], v4\n"
    "v_smfmac_f32_16x16x64_bf16 v[76:79], v[24:27], v[216:223], v4\n"
    "s_waitcnt vmcnt(1)\n"
    "v_smfmac_f32_16x16x64_bf16 v[64:67], v[28:31], v[224:231], v4\n"
    "v_max3_u32 v96, v28, v29, v96\n"
    "v_smfmac_f32_16x16x64_bf16 v[68:71], v[28:31], v[232:239], v4\n"
    "v_max3_u32 v96, v30, v31, v96\n"
    "v_smfmac_f32_16x16x64_bf16 v[72:75], v[28:31], v[240:247], v4\n"
    "v_cmp_eq_u32 vcc, -1, v96\n"
    "v_smfmac_f32_16x16x64_bf16 v[76:79], v[28:31], v[248:255], v4\n"
    ".LB_mdone%=:\n"
    "v_add_f32 v64, v64, v66\n"
    "v_add_f32 v65, v65, v67\n"
    "v_add_f32 v68, v68, v70\n"
    "v_add_f32 v69, v69, v71\n"
    "ds_write2st64_b64 v6, v[64:65], v[68:69] offset0:0 offset1:1\n"
    "v_add_f32 v72, v72, v74\n"
    "v_add_f32 v73, v73, v75\n"
    "s_nop 0\n"
    "v_add_f32 v76, v76, v78\n"
    "v_add_f32 v77, v77, v79\n"
    "ds_write2st64_b64 v6, v[72:73], v[76:77] offset0:2 offset1:3\n"
    "s_cbranch_vccnz .LB_mredo%=\n"
    "v_mov_b64 v[80:81], 0\n"
    "v_mov_b64 v[82:83], 0\n"
    "v_mov_b64 v[84:85], 0\n"
    "v_mov_b64 v[86:87], 0\n"
    "v_mov_b64 v[88:89], 0\n"
    "v_mov_b64 v[90:91], 0\n"
    "v_mov_b64 v[92:93], 0\n"
    "v_mov_b64 v[94:95], 0\n"
    "s_waitcnt lgkmcnt(0)\n"
    "s_barrier\n"
    "ds_read_b64 v[108:109], v7 offset:0\n"
    "v_mov_b64 v[64:65], 0\n"
    "ds_read_b64 v[110:111], v7 offset:2048\n"
    "v_mov_b64 v[66:67], 0\n"
    "ds_read_b64 v[112:113], v7 offset:4096\n"
    "v_mov_b64 v[68:69], 0\n"
    "ds_read_b64 v[114:115], v7 offset:6144\n"
    "v_mov_b64 v[70:71], 0\n"
    "v_mov_b64 v[72:73], 0\n"
    "s_nop 0\n"
    "v_mov_b64 v[74:75], 0\n"
    "s_nop 0\n"
    "v_mov_b64 v[76:77], 0\n"
    "s_nop 0\n"
    "v_mov_b64 v[78:79], 0\n"
    "s_nop 0\n"
    "s_waitcnt lgkmcnt(2)\n"
    "v_pk_add_f32 v[108:109], v[108:109], v[110:111]\n"
    "s_waitcnt lgkmcnt(0)\n"
    "v_pk_add_f32 v[112:113], v[112:113], v[114:115]\n"
    "v_pk_add_f32 v[108:109], v[108:109], v[112:113]\n"
    "v_cvt_pk_bf16_f32 v96, v108, v109\n"
    "v_min_u32 v96, -2, v96\n"
    "buffer_store_dword v96, v2, s[40:43], s61 offen sc1\n"
    "s_cmp_eq_u32 s60, 63\n"
    "s_cbranch_scc1 .LB_end%=\n"
    "s_sleep 1\n"
    "buffer_store_dword v100, v2, s[40:43], s63 offen sc1\n"
    "s_cmp_lt_u32 s56, 2\n"
    "s_cbranch_scc1 .LB_w0%=\n"
    "s_cmp_eq_u32 s60, 0\n"
    "s_cbranch_scc1 .LB_w3%=\n"
    "buffer_store_dword v100, v3, s[40:43], s63 offen sc1\n"
    ".LB_w3%=:\n"
    "s_cmp_eq_u32 s56, 3\n"
    "s_cbranch_scc0 .LB_w0%=\n"
    "s_sleep 2\n"
    "buffer_load_dwordx4 v[104:107], v98, s[40:43], 0 offen sc1\n"
    "s_mov_b32 s33, 0\n"
    "s_waitcnt vmcnt(0)\n"
    ".LB_stc%=:\n"
    "v_subrev_u32 v104, s59, v104\n"
    "v_subrev_u32 v105, s59, v105\n"
    "v_subrev_u32 v106, s59, v106\n"
    "v_subrev_u32 v107, s59, v107\n"
    "v_min3_i32 v108, v104, v105, v106\n"
    "v_max3_i32 v109, v104, v105, v106\n"
    "v_min_i32 v108, v108, v107\n"
    "v_max_i32 v109, v109, v107\n"
    "v_cmp_lt_i32 vcc, 0, v108\n"
    "s_nop 0\n"
    "v_max_i32_dpp v109, v109, v109 row_ror:1 row_mask:0xf bank_mask:0xf\n"
    "s_nop 1\n"
    "v_max_i32_dpp v109, v109, v109 row_ror:2 row_mask:0xf bank_mask:0xf\n"
    "s_nop 1\n"
    "v_max_i32_dpp v109, v109, v109 row_ror:4 row_mask:0xf bank_mask:0xf\n"
    "s_nop 1\n"
    "v_max_i32_dpp v109, v109, v109 row_ror:8 row_mask:0xf bank_mask:0xf\n"
    "s_cmp_eq_u32 vcc_lo, -1\n"
    "s_cbranch_scc0 .LB_str%=\n"
    "v_readlane_b32 s69, v109, 0\n"
    "v_readlane_b32 s74, v109, 16\n"
    "v_readlane_b32 s71, v109, 32\n"
    "v_readlane_b32 s100, v109, 48\n"
    "s_max_i32 s69, s69, s74\n"
    "s_max_i32 s71, s71, s100\n"
    "s_cmp_lt_i32 s71, 200\n"
    "s_cselect_b32 s71, s62, s71\n"
    "s_cmp_lt_u32 s60, 2\n"
    "s_cselect_b32 s71, s62, s71\n"
    "s_sub_u32 s71, s71, s62\n"
    "s_max_i32 s71, s71, -48\n"
    "s_cmp_lt_u32 s60, 8\n"
    "s_cselect_b32 s74, 1, 4\n"
    "s_ashr_i32 s74, s71, s74\n"
    "s_max_i32 s74, s74, s71\n"
    "s_cmp_gt_i32 s71, 60\n"
    "s_cselect_b32 s74, 8, s74\n"
    "s_add_u32 s62, s62, s74\n"
    "s_add_u32 s59, s59, s69\n"
    "s_add_u32 s69, s59, s58\n"
    "s_mov_b32 s74, 0\n"
    "s_sub_u32 s71, s69, 160\n"
    "s_mov_b32 s33, 0\n"
    ".LB_wc%=:\n"
    "s_memrealtime s[100:101]\n"
    "s_add_u32 s74, s74, 0x1000000\n"
    "s_cbranch_scc1 .LB_wce%=\n"
    "s_waitcnt lgkmcnt(0)\n"
    "s_lshl_b32 s100, s100, 3\n"
    "s_sub_u32 s100, s100, s71\n"
    "s_cmp_lt_i32 s100, 0\n"
    "s_cbranch_scc1 .LB_wc%=\n"
    "s_cmp_gt_i32 s100, 130\n"
    "s_cbranch_scc1 .LB_wce%=\n"
    "s_memrealtime s[76:77]\n"
    "s_nop 0\n"
    "s_nop 0\n"
    "s_memrealtime s[78:79]\n"
    "s_nop 0\n"
    "s_nop 0\n"
    "s_memrealtime s[80:81]\n"
    "s_nop 0\n"
    "s_nop 0\n"
    "s_memrealtime s[82:83]\n"
    "s_nop 0\n"
    "s_nop 0\n"
    "s_memrealtime s[84:85]\n"
    "s_nop 0\n"
    "s_nop 0\n"
    "s_memrealtime s[86:87]\n"
    "s_nop 0\n"
    "s_nop 0\n"
    "s_memrealtime s[88:89]\n"
    "s_nop 0\n"
    "s_nop 0\n"
    "s_memrealtime s[90:91]\n"
    "s_nop 0\n"
    "s_nop 0\n"
    "s_waitcnt lgkmcnt(0)\n"
    "s_add_u32 s71, s76, s78\n"
    "s_add_u32 s71, s71, s80\n"
    "s_add_u32 s71, s71, s82\n"
    "s_add_u32 s71, s71, s84\n"
    "s_add_u32 s71, s71, s86\n"
    "s_add_u32 s71, s71, s88\n"
    "s_add_u32 s71, s71, s90\n"
    "s_sub_u32 s71, s69, s71\n"
    "s_sub_u32 s71, s71, 70\n"
    "s_cmp_ge_i32 s71, -8\n"
    "s_cselect_b32 s33, 1, 0\n"
    "s_max_i32 s71, s71, 0\n"
    "s_mul_i32 s71, s71, 91\n"
    "s_lshr_b32 s71, s71, 8\n"
    "s_min_u32 s71, s71, 48\n"
    "s_lshl_b32 s71, s71, 2\n"
    "s_sub_u32 s71, 204, s71\n"
    "s_getpc_b64 s[100:101]\n"
    "s_add_u32 s100, s100, s71\n"
    "s_addc_u32 s101, s101, 0\n"
    "s_setpc_b64 s[100:101]\n"
    "s_nop 1\n"
    "s_nop 1\n"
    "s_nop 1\n"
    "s_nop 1\n"
    "s_nop 1\n"
    "s_nop 1\n"
    "s_nop 1\n"
    "s_nop 1\n"
    "s_nop 1\n"
    "s_nop 1\n"
    "s_nop 1\n"
    "s_nop 1\n"
    "s_nop 1\n"
    "s_nop 1\n"
    "s_nop 1\n"
    "s_nop 1\n"
    "s_nop 1\n"
    "s_nop 1\n"
    "s_nop 1\n"
    "s_nop 1\n"
    "s_nop 1\n"
    "s_nop 1\n"
    "s_nop 1\n"
    "s_nop 1\n"
    "s_nop 1\n"
    "s_nop 1\n"
    "s_nop 1\n"
    "s_nop 1\n"
    "s_nop 1\n"
    "s_nop 1\n"
    "s_nop 1\n"
    "s_nop 1\n"
    "s_nop 1\n"
    "s_nop 1\n"
    "s_nop 1\n"
    "s_nop 1\n"
    "s_nop 1\n"
    "s_nop 1\n"
    "s_nop 1\n"
    "s_nop 1\n"
    "s_nop 1\n"
    "s_nop 1\n"
    "s_nop 1\n"
    "s_nop 1\n"
    "s_nop 1\n"
    "s_nop 1\n"
    "s_nop 1\n"
    "s_nop 1\n"
    ".LB_wce%=:\n"
    "s_waitcnt lgkmcnt(0)\n"
    ".LB_w0%=:\n"
    "s_waitcnt vmcnt(0)\n"
    "s_barrier\n"
    "buffer_load_dwordx4 v[32:35], v120, s[40:43], s61 offen sc1\n"
    "s_nop 7\n"
    "buffer_load_dwordx4 v[36:39], v121, s[40:43], s61 offen sc1\n"
    "s_nop 7\n"
    "buffer_load_dwordx4 v[40:43], v122, s[40:43], s61 offen sc1\n"
    "s_nop 7\n"
    "buffer_load_dwordx4 v[44:47], v123, s[40:43], s61 offen sc1\n"
    "s_nop 7\n"
    "buffer_load_dwordx4 v[48:51], v124, s[40:43], s61 offen sc1\n"
    "s_nop 7\n"
    "buffer_load_dwordx4 v[52:55], v125, s[40:43], s61 offen sc1\n"
    "s_nop 7\n"
    "buffer_load_dwordx4 v[56:59], v126, s[40:43], s61 offen sc1\n"
    "s_nop 7\n"
    "buffer_load_dwordx4 v[60:63], v127, s[40:43], s61 offen sc1\n"
    ".LB_os0%=:\n"
    "s_waitcnt vmcnt(7)\n"
    "v_smfmac_f32_16x16x64_bf16 v[80:83], v[32:35], a[0:7], v4\n"
    "v_max3_u32 v96, v32, v33, v32\n"
    "v_smfmac_f32_16x16x64_bf16 v[88:91], v[32:35], a[16:23], v4\n"
    "v_max3_u32 v96, v34, v35, v96\n"
    "v_smfmac_f32_16x16x64_bf16 v[84:87], v[32:35], a[8:15], v4\n"
    "v_cmp_eq_u32 vcc, -1, v96\n"
    "v_smfmac_f32_16x16x64_bf16 v[92:95], v[32:35], a[24:31], v4\n"
    "s_cbranch_vccnz .LB_oredo%=\n"
    "s_waitcnt vmcnt(6)\n"
    "v_smfmac_f32_16x16x64_bf16 v[80:83], v[36:39], a[32:39], v4\n"
    "v_max3_u32 v96, v36, v37, v96\n"
    "v_smfmac_f32_16x16x64_bf16 v[88:91], v[36:39], a[48:55], v4\n"
    "v_max3_u32 v96, v38, v39, v96\n"
    "v_smfmac_f32_16x16x64_bf16 v[84:87], v[36:39], a[40:47], v4\n"
    "v_smfmac_f32_16x16x64_bf16 v[92:95], v[36:39], a[56:63], v4\n"
    "s_waitcnt vmcnt(5)\n"
    "v_smfmac_f32_16x16x64_bf16 v[80:83], v[40:43], a[64:71], v4\n"
    "v_max3_u32 v96, v40, v41, v96\n"
    "v_smfmac_f32_16x16x64_bf16 v[88:91], v[40:43], a[80:87], v4\n"
    "v_max3_u32 v96, v42, v43, v96\n"
    "v_smfmac_f32_16x16x64_bf16 v[84:87], v[40:43], a[72:79], v4\n"
    "v_smfmac_f32_16x16x64_bf16 v[92:95], v[40:43], a[88:95], v4\n"
    "s_waitcnt vmcnt(4)\n"
    "v_smfmac_f32_16x16x64_bf16 v[80:83], v[44:47], a[96:103], v4\n"
    "v_max3_u32 v96, v44, v45, v96\n"
    "v_smfmac_f32_16x16x64_bf16 v[88:91], v[44:47], a[112:119], v4\n"
    "v_max3_u32 v96, v46, v47, v96\n"
    "v_smfmac_f32_16x16x64_bf16 v[84:87], v[44:47], a[104:111], v4\n"
    "v_smfmac_f32_16x16x64_bf16 v[92:95], v[44:47], a[120:127], v4\n"
    "s_waitcnt vmcnt(3)\n"
    "v_smfmac_f32_16x16x64_bf16 v[80:83], v[48:51], a[128:135], v4\n"
    "v_max3_u32 v96, v48, v49, v96\n"
    "v_smfmac_f32_16x16x64_bf16 v[88:91], v[48:51], a[144:151], v4\n"
    "v_max3_u32 v96, v50, v51, v96\n"
    "v_smfmac_f32_16x16x64_bf16 v[84:87], v[48:51], a[136:143], v4\n"
    "v_smfmac_f32_16x16x64_bf16 v[92:95], v[48:51], a[152:159], v4\n"
    "s_waitcnt vmcnt(2)\n"
    "v_smfmac_f32_16x16x64_bf16 v[80:83], v[52:55], a[160:167], v4\n"
    "v_max3_u32 v96, v52, v53, v96\n"
    "v_smfmac_f32_16x16x64_bf16 v[88:91], v[52:55], a[176:183], v4\n"
    "v_max3_u32 v96, v54, v55, v96\n"
    "v_smfmac_f32_16x16x64_bf16 v[84:87], v[52:55], a[168:175], v4\n"
    "v_smfmac_f32_16x16x64_bf16 v[92:95], v[52:55], a[184:191], v4\n"
    "s_waitcnt vmcnt(1)\n"
    "v_smfmac_f32_16x16x64_bf16 v[80:83], v[56:59], a[192:199], v4\n"
    "v_max3_u32 v96, v56, v57, v96\n"
    "v_smfmac_f32_16x16x64_bf16 v[88:91], v[56:59], a[208:215], v4\n"
    "v_max3_u32 v96, v58, v59, v96\n"
    "v_smfmac_f32_16x16x64_bf16 v[84:87], v[56:59], a[200:207], v4\n"
    "v_smfmac_f32_16x16x64_bf16 v[92:95], v[56:59], a[216:223], v4\n"
    "s_waitcnt vmcnt(0)\n"
    "v_smfmac_f32_16x16x64_bf16 v[80:83], v[60:63], a[224:231], v4\n"
    "v_max3_u32 v96, v60, v61, v96\n"
    "v_smfmac_f32_16x16x64_bf16 v[88:91], v[60:63], a[240:247], v4\n"
    "v_max3_u32 v96, v62, v63, v96\n"
    "v_smfmac_f32_16x16x64_bf16 v[84:87], v[60:63], a[232:239], v4\n"
    "v_cmp_eq_u32 vcc, -1, v96\n"
    "v_smfmac_f32_16x16x64_bf16 v[92:95], v[60:63], a[248:255], v4\n"
    ".LB_odone%=:\n"
    "v_add_f32_dpp v80, v80, v82 quad_perm:[0,1,2,3] row_mask:0x3 bank_mask:0xf\n"
    "v_add_f32_dpp v81, v81, v83 quad_perm:[0,1,2,3] row_mask:0x3 bank_mask:0xf\n"
    "s_nop 0\n"
    "v_add_f32_dpp v80, v88, v90 quad_perm:[0,1,2,3] row_mask:0xc bank_mask:0xf\n"
    "v_add_f32_dpp v81, v89, v91 quad_perm:[0,1,2,3] row_mask:0xc bank_mask:0xf\n"
    "v_add_f32_dpp v84, v84, v86 quad_perm:[0,1,2,3] row_mask:0x3 bank_mask:0xf\n"
    "v_add_f32_dpp v85, v85, v87 quad_perm:[0,1,2,3] row_mask:0x3 bank_mask:0xf\n"
    "s_nop 0\n"
    "v_add_f32_dpp v84, v92, v94 quad_perm:[0,1,2,3] row_mask:0xc bank_mask:0xf\n"
    "v_add_f32_dpp v85, v93, v95 quad_perm:[0,1,2,3] row_mask:0xc bank_mask:0xf\n"
    "s_nop 0\n"
    "v_permlane32_swap_b32 v80, v84\n"
    "v_permlane32_swap_b32 v81, v85\n"
    "v_pk_add_f32 v[80:81], v[80:81], v[84:85]\n"
    "ds_write2_b32 v8, v80, v81 offset1:32\n"
    "s_cbranch_vccnz .LB_oredo%=\n"
    "s_add_u32 s69, s64, 32768\n"
    "s_cmp_lt_u32 s56, 1\n"
    "s_cbranch_scc0 .LB_nr%=\n"
    "s_waitcnt lgkmcnt(0)\n"
    "s_barrier\n"
    "ds_read_b64 v[108:109], v9 offset:0\n"
    "ds_read_b64 v[110:111], v9 offset:512\n"
    "ds_read_b64 v[112:113], v9 offset:1024\n"
    "ds_read_b64 v[114:115], v9 offset:1536\n"
    "s_waitcnt lgkmcnt(2)\n"
    "v_pk_add_f32 v[108:109], v[108:109], v[110:111]\n"
    "s_waitcnt lgkmcnt(0)\n"
    "v_pk_add_f32 v[112:113], v[112:113], v[114:115]\n"
    "v_pk_add_f32 v[108:109], v[108:109], v[112:113]\n"
    "v_cvt_pk_bf16_f32 v96, v108, v109\n"
    "v_min_u32 v96, -2, v96\n"
    "buffer_store_dword v96, v3, s[40:43], s64 offen sc1\n"
    "buffer_store_dword v96, v3, s[40:43], s69 offen sc1\n"
    "s_sleep 2\n"
    ".LB_nj%=:\n"
    "s_mov_b32 s68, 0\n"
    "v_xor_b32 v98, 16, v98\n"
    "s_mov_b32 s69, s63\n"
    "s_mov_b32 s63, s61\n"
    "s_mov_b32 s61, s64\n"
    "s_mov_b32 s64, s69\n"
    "s_add_u32 s60, s60, 1\n"
    "s_barrier\n"
    "s_branch .LB_top%=\n"
    ".LB_nr%=:\n"
    "s_add_u32 s69, s59, s62\n"
    "s_add_u32 s69, s69, s57\n"
    "s_cmp_eq_u32 s56, 3\n"
    "s_waitcnt lgkmcnt(0)\n"
    "s_barrier\n"
    "s_cbranch_scc0 .LB_nj%=\n"
    "s_memrealtime s[76:77]\n"
    "s_nop 0\n"
    "s_nop 0\n"
    "s_memrealtime s[78:79]\n"
    "s_nop 0\n"
    "s_nop 0\n"
    "s_memrealtime s[80:81]\n"
    "s_nop 0\n"
    "s_nop 0\n"
    "s_memrealtime s[82:83]\n"
    "s_nop 0\n"
    "s_nop 0\n"
    "s_memrealtime s[84:85]\n"
    "s_nop 0\n"
    "s_nop 0\n"
    "s_memrealtime s[86:87]\n"
    "s_nop 0\n"
    "s_nop 0\n"
    "s_memrealtime s[88:89]\n"
    "s_nop 0\n"
    "s_nop 0\n"
    "s_memrealtime s[90:91]\n"
    "s_nop 0\n"
    "s_nop 0\n"
    "s_waitcnt lgkmcnt(0)\n"
    "s_add_u32 s38, s76, s78\n"
    "s_add_u32 s38, s38, s80\n"
    "s_add_u32 s38, s38, s82\n"
    "s_add_u32 s38, s38, s84\n"
    "s_add_u32 s38, s38, s86\n"
    "s_add_u32 s38, s38, s88\n"
    "s_add_u32 s38, s38, s90\n"
    "s_sub_u32 s71, s69, s38\n"
    "s_sub_u32 s71, s71, 70\n"
    "s_cmp_ge_i32 s71, -8\n"
    "s_cselect_b32 s33, 1, 0\n"
    "s_max_i32 s71, s71, 0\n"
    "s_mul_i32 s71, s71, 91\n"
    "s_lshr_b32 s71, s71, 8\n"
    "s_min_u32 s71, s71, 48\n"
    "s_lshl_b32 s71, s71, 2\n"
    "s_sub_u32 s71, 204, s71\n"
    "s_getpc_b64 s[100:101]\n"
    "s_add_u32 s100, s100, s71\n"
    "s_addc_u32 s101, s101, 0\n"
    "s_setpc_b64 s[100:101]\n"
    "s_nop 1\n"
    "s_nop 1\n"
    "s_nop 1\n"
    "s_nop 1\n"
    "s_nop 1\n"
    "s_nop 1\n"
    "s_nop 1\n"
    "s_nop 1\n"
    "s_nop 1\n"
    "s_nop 1\n"
    "s_nop 1\n"
    "s_nop 1\n"
    "s_nop 1\n"
    "s_nop 1\n"
    "s_nop 1\n"
    "s_nop 1\n"
    "s_nop 1\n"
    "s_nop 1\n"
    "s_nop 1\n"
    "s_nop 1\n"
    "s_nop 1\n"
    "s_nop 1\n"
    "s_nop 1\n"
    "s_nop 1\n"
    "s_nop 1\n"
    "s_nop 1\n"
    "s_nop 1\n"
    "s_nop 1\n"
    "s_nop 1\n"
    "s_nop 1\n"
    "s_nop 1\n"
    "s_nop 1\n"
    "s_nop 1\n"
    "s_nop 1\n"
    "s_nop 1\n"
    "s_nop 1\n"
    "s_nop 1\n"
    "s_nop 1\n"
    "s_nop 1\n"
    "s_nop 1\n"
    "s_nop 1\n"
    "s_nop 1\n"
    "s_nop 1\n"
    "s_nop 1\n"
    "s_nop 1\n"
    "s_nop 1\n"
    "s_nop 1\n"
    "s_nop 1\n"
    "s_branch .LB_nj%=\n"
    ".LB_mredo%=:\n"
    "buffer_load_dwordx4 v[16:19], v116, s[40:43], s61 offen sc1\n"
    "buffer_load_dwordx4 v[20:23], v117, s[40:43], s61 offen sc1\n"
    "buffer_load_dwordx4 v[24:27], v118, s[40:43], s61 offen sc1\n"
    "buffer_load_dwordx4 v[28:31], v119, s[40:43], s61 offen sc1\n"
    "s_nop 7\n"
    "v_mov_b64 v[64:65], 0\n"
    "v_mov_b64 v[66:67], 0\n"
    "v_mov_b64 v[68:69], 0\n"
    "v_mov_b64 v[70:71], 0\n"
    "v_mov_b64 v[72:73], 0\n"
    "v_mov_b64 v[74:75], 0\n"
    "v_mov_b64 v[76:77], 0\n"
    "v_mov_b64 v[78:79], 0\n"
    ".LB_mk0%=:\n"
    "s_waitcnt vmcnt(3)\n"
    "v_max3_u32 v96, v16, v17, v18\n"
    "v_max_u32 v96, v96, v19\n"
    "v_cmp_eq_u32 vcc, -1, v96\n"
    "s_cbranch_vccnz .LB_mp0%=\n"
    ".LB_mg0%=:\n"
    "v_smfmac_f32_16x16x64_bf16 v[64:67], v[16:19], v[128:135], v4\n"
    "v_smfmac_f32_16x16x64_bf16 v[68:71], v[16:19], v[136:143], v4\n"
    "s_waitcnt vmcnt(2)\n"
    "v_max3_u32 v96, v20, v21, v22\n"
    "v_smfmac_f32_16x16x64_bf16 v[72:75], v[16:19], v[144:151], v4\n"
    "v_max_u32 v96, v96, v23\n"
    "v_cmp_eq_u32 vcc, -1, v96\n"
    "v_smfmac_f32_16x16x64_bf16 v[76:79], v[16:19], v[152:159], v4\n"
    "s_cbranch_vccnz .LB_mp1%=\n"
    ".LB_mg1%=:\n"
    "v_smfmac_f32_16x16x64_bf16 v[64:67], v[20:23], v[160:167], v4\n"
    "v_smfmac_f32_16x16x64_bf16 v[68:71], v[20:23], v[168:175], v4\n"
    "s_waitcnt vmcnt(1)\n"
    "v_max3_u32 v96, v24, v25, v26\n"
    "v_smfmac_f32_16x16x64_bf16 v[72:75], v[20:23], v[176:183], v4\n"
    "v_max_u32 v96, v96, v27\n"
    "v_cmp_eq_u32 vcc, -1, v96\n"
    "v_smfmac_f32_16x16x64_bf16 v[76:79], v[20:23], v[184:191], v4\n"
    "s_cbranch_vccnz .LB_mp2%=\n"
    ".LB_mg2%=:\n"
    "v_smfmac_f32_16x16x64_bf16 v[64:67], v[24:27], v[192:199], v4\n"
    "v_smfmac_f32_16x16x64_bf16 v[68:71], v[24:27], v[200:207], v4\n"
    "s_waitcnt vmcnt(0)\n"
    "v_max3_u32 v96, v28, v29, v30\n"
    "v_smfmac_f32_16x16x64_bf16 v[72:75], v[24:27], v[208:215], v4\n"
    "v_max_u32 v96, v96, v31\n"
    "v_cmp_eq_u32 vcc, -1, v96\n"
    "v_smfmac_f32_16x16x64_bf16 v[76:79], v[24:27], v[216:223], v4\n"
    "s_cbranch_vccnz .LB_mp3%=\n"
    ".LB_mg3%=:\n"
    "v_smfmac_f32_16x16x64_bf16 v[64:67], v[28:31], v[224:231], v4\n"
    "v_smfmac_f32_16x16x64_bf16 v[68:71], v[28:31], v[232:239], v4\n"
    "v_smfmac_f32_16x16x64_bf16 v[72:75], v[28:31], v[240:247], v4\n"
    "v_smfmac_f32_16x16x64_bf16 v[76:79], v[28:31], v[248:255], v4\n"
    ".LB_mfin%=:\n"
    "s_nop 7\n"
    "s_branch .LB_mdone%=\n"
    ".LB_mp0%=:\n"
    "buffer_load_dwordx4 v[16:19], v116, s[40:43], s61 offen sc1\n"
    "buffer_load_dwordx4 v[20:23], v117, s[40:43], s61 offen sc1\n"
    "buffer_load_dwordx4 v[24:27], v118, s[40:43], s61 offen sc1\n"
    "buffer_load_dwordx4 v[28:31], v119, s[40:43], s61 offen sc1\n"
    "s_waitcnt vmcnt(3)\n"
    "v_max3_u32 v96, v16, v17, v18\n"
    "v_max_u32 v96, v96, v19\n"
    "v_cmp_eq_u32 vcc, -1, v96\n"
    "s_cbranch_vccz .LB_mg0%=\n"
    "s_add_u32 s68, s68, 1\n"
    "s_cmp_lt_u32 s68, 65536\n"
    "s_cbranch_scc1 .LB_mp0%=\n"
    "s_branch .LB_tmo%=\n"
    ".LB_mp1%=:\n"
    "buffer_load_dwordx4 v[20:23], v117, s[40:43], s61 offen sc1\n"
    "buffer_load_dwordx4 v[24:27], v118, s[40:43], s61 offen sc1\n"
    "buffer_load_dwordx4 v[28:31], v119, s[40:43], s61 offen sc1\n"
    "s_waitcnt vmcnt(2)\n"
    "v_max3_u32 v96, v20, v21, v22\n"
    "v_max_u32 v96, v96, v23\n"
    "v_cmp_eq_u32 vcc, -1, v96\n"
    "s_cbranch_vccz .LB_mg1%=\n"
    "s_add_u32 s68, s68, 1\n"
    "s_cmp_lt_u32 s68, 65536\n"
    "s_cbranch_scc1 .LB_mp1%=\n"
    "s_branch .LB_tmo%=\n"
    ".LB_mp2%=:\n"
    "buffer_load_dwordx4 v[24:27], v118, s[40:43], s61 offen sc1\n"
    "buffer_load_dwordx4 v[28:31], v119, s[40:43], s61 offen sc1\n"
    "s_waitcnt vmcnt(1)\n"
    "v_max3_u32 v96, v24, v25, v26\n"
    "v_max_u32 v96, v96, v27\n"
    "v_cmp_eq_u32 vcc, -1, v96\n"
    "s_cbranch_vccz .LB_mg2%=\n"
    "s_add_u32 s68, s68, 1\n"
    "s_cmp_lt_u32 s68, 65536\n"
    "s_cbranch_scc1 .LB_mp2%=\n"
    "s_branch .LB_tmo%=\n"
    ".LB_mp3%=:\n"
    "buffer_load_dwordx4 v[28:31], v119, s[40:43], s61 offen sc1\n"
    "s_waitcnt vmcnt(0)\n"
    "v_max3_u32 v96, v28, v29, v30\n"
    "v_max_u32 v96, v96, v31\n"
    "v_cmp_eq_u32 vcc, -1, v96\n"
    "s_cbranch_vccz .LB_mg3%=\n"
    "s_add_u32 s68, s68, 1\n"
    "s_cmp_lt_u32 s68, 65536\n"
    "s_cbranch_scc1 .LB_mp3%=\n"
    "s_branch .LB_tmo%=\n"
    ".LB_str%=:\n"
    "buffer_load_dwordx4 v[104:107], v98, s[40:43], 0 offen sc1\n"
    "s_waitcnt vmcnt(0)\n"
    "s_add_u32 s33, s33, 1\n"
    "s_cmp_lt_u32 s33, 128\n"
    "s_cbranch_scc1 .LB_stc%=\n"
    "s_branch .LB_w0%=\n"
    ".LB_oredo%=:\n"
    "buffer_load_dwordx4 v[32:35], v120, s[40:43], s61 offen sc1\n"
    "buffer_load_dwordx4 v[36:39], v121, s[40:43], s61 offen sc1\n"
    "buffer_load_dwordx4 v[40:43], v122, s[40:43], s61 offen sc1\n"
    "buffer_load_dwordx4 v[44:47], v123, s[40:43], s61 offen sc1\n"
    "buffer_load_dwordx4 v[48:51], v124, s[40:43], s61 offen sc1\n"
    "buffer_load_dwordx4 v[52:55], v125, s[40:43], s61 offen sc1\n"
    "buffer_load_dwordx4 v[56:59], v126, s[40:43], s61 offen sc1\n"
    "buffer_load_dwordx4 v[60:63], v127, s[40:43], s61 offen sc1\n"
    "s_nop 7\n"
    "v_mov_b64 v[80:81], 0\n"
    "v_mov_b64 v[82:83], 0\n"
    "v_mov_b64 v[84:85], 0\n"
    "v_mov_b64 v[86:87], 0\n"
    "v_mov_b64 v[88:89], 0\n"
    "v_mov_b64 v[90:91], 0\n"
    "v_mov_b64 v[92:93], 0\n"
    "v_mov_b64 v[94:95], 0\n"
    ".LB_ok0%=:\n"
    "s_waitcnt vmcnt(7)\n"
    "v_max3_u32 v96, v32, v33, v34\n"
    "v_max_u32 v96, v96, v35\n"
    "v_cmp_eq_u32 vcc, -1, v96\n"
    "s_cbranch_vccnz .LB_op0%=\n"
    ".LB_og0%=:\n"
    "v_smfmac_f32_16x16x64_bf16 v[80:83], v[32:35], a[0:7], v4\n"
    "v_smfmac_f32_16x16x64_bf16 v[88:91], v[32:35], a[16:23], v4\n"
    "s_waitcnt vmcnt(6)\n"
    "v_max3_u32 v96, v36, v37, v38\n"
    "v_smfmac_f32_16x16x64_bf16 v[84:87], v[32:35], a[8:15], v4\n"
    "v_max_u32 v96, v96, v39\n"
    "v_cmp_eq_u32 vcc, -1, v96\n"
    "v_smfmac_f32_16x16x64_bf16 v[92:95], v[32:35], a[24:31], v4\n"
    "s_cbranch_vccnz .LB_op1%=\n"
    ".LB_og1%=:\n"
    "v_smfmac_f32_16x16x64_bf16 v[80:83], v[36:39], a[32:39], v4\n"
    "v_smfmac_f32_16x16x64_bf16 v[88:91], v[36:39], a[48:55], v4\n"
    "s_waitcnt vmcnt(5)\n"
    "v_max3_u32 v96, v40, v41, v42\n"
    "v_smfmac_f32_16x16x64_bf16 v[84:87], v[36:39], a[40:47], v4\n"
    "v_max_u32 v96, v96, v43\n"
    "v_cmp_eq_u32 vcc, -1, v96\n"
    "v_smfmac_f32_16x16x64_bf16 v[92:95], v[36:39], a[56:63], v4\n"
    "s_cbranch_vccnz .LB_op2%=\n"
    ".LB_og2%=:\n"
    "v_smfmac_f32_16x16x64_bf16 v[80:83], v[40:43], a[64:71], v4\n"
    "v_smfmac_f32_16x16x64_bf16 v[88:91], v[40:43], a[80:87], v4\n"
    "s_waitcnt vmcnt(4)\n"
    "v_max3_u32 v96, v44, v45, v46\n"
    "v_smfmac_f32_16x16x64_bf16 v[84:87], v[40:43], a[72:79], v4\n"
    "v_max_u32 v96, v96, v47\n"
    "v_cmp_eq_u32 vcc, -1, v96\n"
    "v_smfmac_f32_16x16x64_bf16 v[92:95], v[40:43], a[88:95], v4\n"
    "s_cbranch_vccnz .LB_op3%=\n"
    ".LB_og3%=:\n"
    "v_smfmac_f32_16x16x64_bf16 v[80:83], v[44:47], a[96:103], v4\n"
    "v_smfmac_f32_16x16x64_bf16 v[88:91], v[44:47], a[112:119], v4\n"
    "s_waitcnt vmcnt(3)\n"
    "v_max3_u32 v96, v48, v49, v50\n"
    "v_smfmac_f32_16x16x64_bf16 v[84:87], v[44:47], a[104:111], v4\n"
    "v_max_u32 v96, v96, v51\n"
    "v_cmp_eq_u32 vcc, -1, v96\n"
    "v_smfmac_f32_16x16x64_bf16 v[92:95], v[44:47], a[120:127], v4\n"
    "s_cbranch_vccnz .LB_op4%=\n"
    ".LB_og4%=:\n"
    "v_smfmac_f32_16x16x64_bf16 v[80:83], v[48:51], a[128:135], v4\n"
    "v_smfmac_f32_16x16x64_bf16 v[88:91], v[48:51], a[144:151], v4\n"
    "s_waitcnt vmcnt(2)\n"
    "v_max3_u32 v96, v52, v53, v54\n"
    "v_smfmac_f32_16x16x64_bf16 v[84:87], v[48:51], a[136:143], v4\n"
    "v_max_u32 v96, v96, v55\n"
    "v_cmp_eq_u32 vcc, -1, v96\n"
    "v_smfmac_f32_16x16x64_bf16 v[92:95], v[48:51], a[152:159], v4\n"
    "s_cbranch_vccnz .LB_op5%=\n"
    ".LB_og5%=:\n"
    "v_smfmac_f32_16x16x64_bf16 v[80:83], v[52:55], a[160:167], v4\n"
    "v_smfmac_f32_16x16x64_bf16 v[88:91], v[52:55], a[176:183], v4\n"
    "s_waitcnt vmcnt(1)\n"
    "v_max3_u32 v96, v56, v57, v58\n"
    "v_smfmac_f32_16x16x64_bf16 v[84:87], v[52:55], a[168:175], v4\n"
    "v_max_u32 v96, v96, v59\n"
    "v_cmp_eq_u32 vcc, -1, v96\n"
    "v_smfmac_f32_16x16x64_bf16 v[92:95], v[52:55], a[184:191], v4\n"
    "s_cbranch_vccnz .LB_op6%=\n"
    ".LB_og6%=:\n"
    "v_smfmac_f32_16x16x64_bf16 v[80:83], v[56:59], a[192:199], v4\n"
    "v_smfmac_f32_16x16x64_bf16 v[88:91], v[56:59], a[208:215], v4\n"
    "s_waitcnt vmcnt(0)\n"
    "v_max3_u32 v96, v60, v61, v62\n"
    "v_smfmac_f32_16x16x64_bf16 v[84:87], v[56:59], a[200:207], v4\n"
    "v_max_u32 v96, v96, v63\n"
    "v_cmp_eq_u32 vcc, -1, v96\n"
    "v_smfmac_f32_16x16x64_bf16 v[92:95], v[56:59], a[216:223], v4\n"
    "s_cbranch_vccnz .LB_op7%=\n"
    ".LB_og7%=:\n"
    "v_smfmac_f32_16x16x64_bf16 v[80:83], v[60:63], a[224:231], v4\n"
    "v_smfmac_f32_16x16x64_bf16 v[88:91], v[60:63], a[240:247], v4\n"
    "v_smfmac_f32_16x16x64_bf16 v[84:87], v[60:63], a[232:239], v4\n"
    "v_smfmac_f32_16x16x64_bf16 v[92:95], v[60:63], a[248:255], v4\n"
    ".LB_ofin%=:\n"
    "s_nop 7\n"
    "s_branch .LB_odone%=\n"
    ".LB_op0%=:\n"
    "buffer_load_dwordx4 v[32:35], v120, s[40:43], s61 offen sc1\n"
    "buffer_load_dwordx4 v[36:39], v121, s[40:43], s61 offen sc1\n"
    "buffer_load_dwordx4 v[40:43], v122, s[40:43], s61 offen sc1\n"
    "buffer_load_dwordx4 v[44:47], v123, s[40:43], s61 offen sc1\n"
    "buffer_load_dwordx4 v[48:51], v124, s[40:43], s61 offen sc1\n"
    "buffer_load_dwordx4 v[52:55], v125, s[40:43], s61 offen sc1\n"
    "buffer_load_dwordx4 v[56:59], v126, s[40:43], s61 offen sc1\n"
    "buffer_load_dwordx4 v[60:63], v127, s[40:43], s61 offen sc1\n"
    "s_waitcnt vmcnt(7)\n"
    "v_max3_u32 v96, v32, v33, v34\n"
    "v_max_u32 v96, v96, v35\n"
    "v_cmp_eq_u32 vcc, -1, v96\n"
    "s_cbranch_vccz .LB_og0%=\n"
    "s_add_u32 s68, s68, 1\n"
    "s_cmp_lt_u32 s68, 65536\n"
    "s_cbranch_scc1 .LB_op0%=\n"
    "s_branch .LB_tmo%=\n"
    ".LB_op1%=:\n"
    "buffer_load_dwordx4 v[36:39], v121, s[40:43], s61 offen sc1\n"
    "buffer_load_dwordx4 v[40:43], v122, s[40:43], s61 offen sc1\n"
    "buffer_load_dwordx4 v[44:47], v123, s[40:43], s61 offen sc1\n"
    "buffer_load_dwordx4 v[48:51], v124, s[40:43], s61 offen sc1\n"
    "buffer_load_dwordx4 v[52:55], v125, s[40:43], s61 offen sc1\n"
    "buffer_load_dwordx4 v[56:59], v126, s[40:43], s61 offen sc1\n"
    "buffer_load_dwordx4 v[60:63], v127, s[40:43], s61 offen sc1\n"
    "s_waitcnt vmcnt(6)\n"
    "v_max3_u32 v96, v36, v37, v38\n"
    "v_max_u32 v96, v96, v39\n"
    "v_cmp_eq_u32 vcc, -1, v96\n"
    "s_cbranch_vccz .LB_og1%=\n"
    "s_add_u32 s68, s68, 1\n"
    "s_cmp_lt_u32 s68, 65536\n"
    "s_cbranch_scc1 .LB_op1%=\n"
    "s_branch .LB_tmo%=\n"
    ".LB_op2%=:\n"
    "buffer_load_dwordx4 v[40:43], v122, s[40:43], s61 offen sc1\n"
    "buffer_load_dwordx4 v[44:47], v123, s[40:43], s61 offen sc1\n"
    "buffer_load_dwordx4 v[48:51], v124, s[40:43], s61 offen sc1\n"
    "buffer_load_dwordx4 v[52:55], v125, s[40:43], s61 offen sc1\n"
    "buffer_load_dwordx4 v[56:59], v126, s[40:43], s61 offen sc1\n"
    "buffer_load_dwordx4 v[60:63], v127, s[40:43], s61 offen sc1\n"
    "s_waitcnt vmcnt(5)\n"
    "v_max3_u32 v96, v40, v41, v42\n"
    "v_max_u32 v96, v96, v43\n"
    "v_cmp_eq_u32 vcc, -1, v96\n"
    "s_cbranch_vccz .LB_og2%=\n"
    "s_add_u32 s68, s68, 1\n"
    "s_cmp_lt_u32 s68, 65536\n"
    "s_cbranch_scc1 .LB_op2%=\n"
    "s_branch .LB_tmo%=\n"
    ".LB_op3%=:\n"
    "buffer_load_dwordx4 v[44:47], v123, s[40:43], s61 offen sc1\n"
    "buffer_load_dwordx4 v[48:51], v124, s[40:43], s61 offen sc1\n"
    "buffer_load_dwordx4 v[52:55], v125, s[40:43], s61 offen sc1\n"
    "buffer_load_dwordx4 v[56:59], v126, s[40:43], s61 offen sc1\n"
    "buffer_load_dwordx4 v[60:63], v127, s[40:43], s61 offen sc1\n"
    "s_waitcnt vmcnt(4)\n"
    "v_max3_u32 v96, v44, v45, v46\n"
    "v_max_u32 v96, v96, v47\n"
    "v_cmp_eq_u32 vcc, -1, v96\n"
    "s_cbranch_vccz .LB_og3%=\n"
    "s_add_u32 s68, s68, 1\n"
    "s_cmp_lt_u32 s68, 65536\n"
    "s_cbranch_scc1 .LB_op3%=\n"
    "s_branch .LB_tmo%=\n"
    ".LB_op4%=:\n"
    "buffer_load_dwordx4 v[48:51], v124, s[40:43], s61 offen sc1\n"
    "buffer_load_dwordx4 v[52:55], v125, s[40:43], s61 offen sc1\n"
    "buffer_load_dwordx4 v[56:59], v126, s[40:43], s61 offen sc1\n"
    "buffer_load_dwordx4 v[60:63], v127, s[40:43], s61 offen sc1\n"
    "s_waitcnt vmcnt(3)\n"
    "v_max3_u32 v96, v48, v49, v50\n"
    "v_max_u32 v96, v96, v51\n"
    "v_cmp_eq_u32 vcc, -1, v96\n"
    "s_cbranch_vccz .LB_og4%=\n"
    "s_add_u32 s68, s68, 1\n"
    "s_cmp_lt_u32 s68, 65536\n"
    "s_cbranch_scc1 .LB_op4%=\n"
    "s_branch .LB_tmo%=\n"
    ".LB_op5%=:\n"
    "buffer_load_dwordx4 v[52:55], v125, s[40:43], s61 offen sc1\n"
    "buffer_load_dwordx4 v[56:59], v126, s[40:43], s61 offen sc1\n"
    "buffer_load_dwordx4 v[60:63], v127, s[40:43], s61 offen sc1\n"
    "s_waitcnt vmcnt(2)\n"
    "v_max3_u32 v96, v52, v53, v54\n"
    "v_max_u32 v96, v96, v55\n"
    "v_cmp_eq_u32 vcc, -1, v96\n"
    "s_cbranch_vccz .LB_og5%=\n"
    "s_add_u32 s68, s68, 1\n"
    "s_cmp_lt_u32 s68, 65536\n"
    "s_cbranch_scc1 .LB_op5%=\n"
    "s_branch .LB_tmo%=\n"
    ".LB_op6%=:\n"
    "buffer_load_dwordx4 v[56:59], v126, s[40:43], s61 offen sc1\n"
    "buffer_load_dwordx4 v[60:63], v127, s[40:43], s61 offen sc1\n"
    "s_waitcnt vmcnt(1)\n"
    "v_max3_u32 v96, v56, v57, v58\n"
    "v_max_u32 v96, v96, v59\n"
    "v_cmp_eq_u32 vcc, -1, v96\n"
    "s_cbranch_vccz .LB_og6%=\n"
    "s_add_u32 s68, s68, 1\n"
    "s_cmp_lt_u32 s68, 65536\n"
    "s_cbranch_scc1 .LB_op6%=\n"
    "s_branch .LB_tmo%=\n"
    ".LB_op7%=:\n"
    "buffer_load_dwordx4 v[60:63], v127, s[40:43], s61 offen sc1\n"
    "s_waitcnt vmcnt(0)\n"
    "v_max3_u32 v96, v60, v61, v62\n"
    "v_max_u32 v96, v96, v63\n"
    "v_cmp_eq_u32 vcc, -1, v96\n"
    "s_cbranch_vccz .LB_og7%=\n"
    "s_add_u32 s68, s68, 1\n"
    "s_cmp_lt_u32 s68, 65536\n"
    "s_cbranch_scc1 .LB_op7%=\n"
    "s_branch .LB_tmo%=\n"
    ".LB_tmo%=:\n"
    "v_mov_b32 v20, 1\n"
    "v_mov_b32 v21, 1572864\n"
    "buffer_store_dword v20, v21, s[40:43], 0 offen sc1\n"
    "s_waitcnt vmcnt(0)\n"
    "s_endpgm\n"
    ".LB_end%=:\n"
    "s_waitcnt vmcnt(0)\n"
    ".LB_fin%=:\n"
    "s_endpgm\n"
    :
    :
      "{v64}"(vo_mem), "{v65}"(vo_o), "{v66}"(vo_vpub), "{v67}"(vo_mpub), "{v68}"(idx), "{v69}"(a_bw), "{v70}"(a_br), "{v71}"(a_rw), "{v72}"(a_rr),
      "{v73}"(vo_srcv), "{v74}"(vo_srco), "{v75}"(a_tv), "{v76}"(a_to), "{v77}"(vo_mem0), "{v78}"(vo_dbg), "{v79}"(vo_m0d), "{v80}"(vo_st),
      "{v81}"(vo_stw), "{v82}"(vo_rdy), "{s[40:43]}"(rxv), "{s[44:47]}"(rw), "{s[48:51]}"(rw2), "{s[52:55]}"(r0), "{s56}"(w), "{s57}"(d0), "{s58}"(d1),
      "{s75}"(rho)
    :
      "memory", "vcc", "scc", "v255", "a255", "s101");

  __builtin_amdgcn_endpgm();
}
constexpr int NSGRP = 2, WPACE = 0, SSPLIT = 44, NMINI = 2, OUT_AUX = 16;
#define CAS __attribute__((address_space(4)))
typedef float f16v __attribute__((ext_vector_type(16)));
DI v4u ldx4(rsrc_t r, u32 vo, u32 so) { return __builtin_amdgcn_raw_buffer_load_b128(r, vo, so, SC1); }
DI u32 pk(float a, float b) { f2 x = {a, b}; return __builtin_bit_cast(u32, __builtin_convertvector(x, b2)); }
DI f4 mf32(v4u a, v4u b, f4 c) { return __builtin_amdgcn_mfma_f32_16x16x32_bf16(__builtin_bit_cast(b8, a), __builtin_bit_cast(b8, b), c, 0, 0, 0); }
DI float fb(u32 x) { return __builtin_bit_cast(float, x); }
DI u32 bf(float x) { return __builtin_bit_cast(u32, x); }
DI float bflo(u32 x) { return fb(x << 16); }
DI float bfhi(u32 x) { return fb(x & 0xffff0000u); }
DI float rbf(float x) { return bflo(pk(x, x)); }
DI v4u pk8(const f16v& a, const f16v& b, int m) { return (v4u){pk(a[4 * m], a[4 * m + 1]), pk(a[4 * m + 2], a[4 * m + 3]), pk(b[4 * m], b[4 * m + 1]), pk(b[4 * m + 2], b[4 * m + 3])}; }
DI void wait_lanes(gu32* ctl, u32 word, u32 want) {
  u32 got, spins; const u32 vo = word * 4;
  asm volatile(
    "s_mov_b32 %1, 0\n"
    "s_nop 4\n"
    ".LW%=:\n"
    "global_load_dword %0, %2, %3 sc1\n"
    "s_waitcnt vmcnt(0)\n"
    "v_cmp_gt_u32 vcc, %4, %0\n"
    "s_cbranch_vccz .LWe%=\n"
    "s_sleep 4\n"
    "s_add_u32 %1, %1, 1\n"
    "s_cmp_lt_u32 %1, 0x40000\n"
    "s_cbranch_scc1 .LW%=\n"
    "v_mov_b32 %0, 0\n"
    "global_store_dword %0, %2, %3 sc1\n"
    "s_waitcnt vmcnt(0)\n"
    "s_endpgm\n"
    ".LWe%=:\n"
    : "=&v"(got), "=&s"(spins) : "v"(vo), "s"(ctl), "s"(want) : "memory", "vcc", "scc");
}
DI v2u make_tag(u32 lo, u32 hi) {
  lo = lo * 0x9e3779b1u + hi * 0x85ebca77u + 0x7f4a7c15u; lo ^= lo >> 15;
  hi = hi * 0xc2b2ae3du + lo * 0x27d4eb2fu + 0x165667b1u; hi ^= hi >> 13;
  return (v2u){lo & ~1u, hi};
}
DI v2u load_tag(rsrc_t rx) {
  const v2u e = __builtin_amdgcn_raw_buffer_load_b64(rx, OFF_CTL + 4 * C_EP, 0, SC1);
  return make_tag(__builtin_amdgcn_readfirstlane(e.x), __builtin_amdgcn_readfirstlane(e.y));
}
DI void wait_init(gu32* ctl, int lane, bool count) {
  const u32 vo = (lane & 1) ? 4 * (C_F + 192) : 4 * C_EP;
  asm volatile(
    "s_mov_b32 s66, 0\n"
    "s_nop 4\n"
    ".LI%=:\n"
    "global_load_dwordx2 v[10:11], %0, %1 sc1\n"
    "s_waitcnt vmcnt(0)\n"
    "v_readlane_b32 s60, v10, 0\n"
    "v_readlane_b32 s61, v11, 0\n"
    "v_readlane_b32 s62, v10, 1\n"
    "v_readlane_b32 s63, v11, 1\n"
    "s_mul_i32 s64, s61, 0x85ebca77\n"
    "s_mul_i32 s60, s60, 0x9e3779b1\n"
    "s_add_u32 s60, s60, s64\n"
    "s_add_u32 s60, s60, 0x7f4a7c15\n"
    "s_lshr_b32 s64, s60, 15\n"
    "s_xor_b32 s60, s60, s64\n"
    "s_mul_i32 s64, s60, 0x27d4eb2f\n"
    "s_mul_i32 s61, s61, 0xc2b2ae3d\n"
    "s_add_u32 s61, s61, s64\n"
    "s_add_u32 s61, s61, 0x165667b1\n"
    "s_lshr_b32 s64, s61, 13\n"
    "s_xor_b32 s61, s61, s64\n"
    "s_andn2_b32 s60, s60, 1\n"
    "s_cmp_eq_u64 s[60:61], s[62:63]\n"
    "s_cbranch_scc1 .LIe%=\n"
    "s_sleep 2\n"
    "s_add_u32 s66, s66, 1\n"
    "s_cmp_lt_u32 s66, 0x40000\n"
    "s_cbranch_scc1 .LI%=\n"
    "v_mov_b32 v10, 0\n"
    "global_store_dword v10, %0, %1 sc1\n"
    "s_waitcnt vmcnt(0)\n"
    "s_endpgm\n"
    ".LIe%=:\n"
    :: "v"(vo), "s"(ctl) : "memory", "scc", "v10", "v11", "s60", "s61", "s62", "s63", "s64", "s66");
  if (count && lane == 0) __hip_atomic_fetch_add(ctl + C_SEEN, 1u, RLX_AGENT);
}
DI void clear_control(const CAS Params& p) {
  const rsrc_t rx = mkrs(p.wb, WS_END);
  const v2u tag = load_tag(rx);
#pragma unroll 1
  for (u32 o = 4 * C_SEEN + threadIdx.x * 16; o < 4 * C_END; o += 4096) stx4((v4u){0u, 0u, 0u, 0u}, rx, OFF_CTL + o, 0);
  WAITV(0);
  BARRIER();
  if (threadIdx.x == 0) __builtin_amdgcn_raw_buffer_store_b64(tag, rx, OFF_CTL + 4 * (C_F + 192), 0, SC1);
}
DI void probe_placement(const CAS Params& p, int w) {
  const rsrc_t rx = mkrs(p.wb, WS_END);
  const u32 o = 3 * EX_SLOT + w * 16384;
  __builtin_amdgcn_raw_buffer_store_b32(0u, rx, o, 0, SC1); __builtin_amdgcn_raw_buffer_store_b32(0u, rx, o + 8192, 0, SC1);
  const v2u tag = load_tag(rx);
  u32 best0 = 1u << 30, best1 = 1u << 30;
  if (w == 0) {
    for (int it = 0; it < 10; ++it) {
      const u32 t0 = ct();
      __builtin_amdgcn_raw_buffer_store_b32(0u, rx, 3 * EX_SLOT + (it & 7) * 8192, 0, SC1); WAITV(0);
      const u32 d = ct() - t0;
      if (it & 1) best1 = min(best1, d); else best0 = min(best0, d);
    }
    if (threadIdx.x == 0) {
      __builtin_amdgcn_raw_buffer_store_b64((v2u){tag.x | (best1 < best0 ? 1u : 0u), tag.y}, rx, OFF_CTL + 4 * (C_F + 194), 0, SC1);
    }
  }
  BARRIER();
}
DI void warm_slow_columns(const CAS Params& p, int i, int w, int lane) {
  const u32 h = 1 + 4 * (i >> 2), G = i & 3, col = h * 256 + (lane & 1) * 128, r = lane >> 1;
  const rsrc_t ra = mkrs((w & 1) ? p.Wk : p.Wq, 8u << 20), rv = mkrs(p.Wv, 8u << 20), ro = mkrs(p.Wo, 8u << 20);
  u32 t = __builtin_amdgcn_raw_buffer_load_b32(ro, ((i * 4 + w) * 32 + r) * 2048 + 384 + (lane & 1) * 1024, 0, 0);
#pragma unroll
  for (int ph = 1; ph < 4; ++ph) {
    t ^= __builtin_amdgcn_raw_buffer_load_b32(ra, ((4 * G + ((ph + 1) & 3)) * 64 + (w >> 1) * 32 + r) * 8192 + col, 0, 0);
    if (ph < 3) t ^= __builtin_amdgcn_raw_buffer_load_b32(rv, ((ph - 1) * 512 + (G * 4 + w) * 32 + r) * 8192 + col, 0, 0);
  }
  if (G) t ^= __builtin_amdgcn_raw_buffer_load_b32(ra, ((4 * (i >> 3) + ((G + 1) & 3)) * 64 + (w >> 1) * 32 + ((i >> 2) & 1) * 16 + (r & 15)) * 8192 + 7936 + (lane & 1) * 128, 0, 0);
  asm volatile("" ::"v"(t));
}
DI void warm_inputs(const CAS Params& p, int i, int w, int lane) {
  const u32 vo = (i * 4 + w) * 8192 + lane * 128;
  u32 t = 0;
#pragma unroll
  for (int k = 0; k < 16; ++k) {
    const uint16_t* b = k < 4 ? p.xB : k < 6 ? p.Bre : k < 8 ? p.Bim : k < 12 ? p.xA : k < 14 ? p.Cre : p.Cim;
    const u32 mb = (k & 4) ? k & 1 : k & 3;
    t ^= __builtin_amdgcn_raw_buffer_load_b32(mkrs(b, (k & 4) ? 2u << 20 : 4u << 20), vo, mb << 20, 0);
    if (WPACE && (k & 1)) { asm volatile("" ::"v"(t)); __builtin_amdgcn_s_sleep(WPACE); }
  }
  asm volatile("" ::"v"(t));
}
struct GemmArgs { v4u vox, vow0, vow1, aw; v2u ax; v4u rx, rw0, rw1; v2u alt; u32 mx, mw, nt, sw, wstep; v2u tm; };
#define GST(ty)
#define GEMM_UNPACK v4u vox = g.vox, vow0 = g.vow0, vow1 = g.vow1, aw = g.aw, rx = g.rx, rw0 = g.rw0, rw1 = g.rw1; v2u ax = g.ax, alt = g.alt; \
  const u32 mx = g.mx, mw = g.mw, nt = g.nt, sw = g.sw, wstep = g.wstep;
DI void gemm_kn_tr(GemmArgs& g, f16v (&acc)[8]) { GEMM_UNPACK
  asm volatile(
    "s_nop 4\n"
    "s_mov_b32 s60, 0\n"
    "s_mov_b32 s61, 0\n"
    "s_mov_b32 s67, 0\n"
    "s_mov_b32 s66, 0\n"
    "s_mov_b32 s63, s54\n"
    "s_mov_b32 s64, s55\n"
    "s_add_u32 m0, s63, 0\n"
    "s_nop 0\n"
    "buffer_load_dwordx4 v14, s[40:43], s60 offen lds\n"
    "s_add_u32 m0, s63, 1024\n"
    "s_nop 0\n"
    "buffer_load_dwordx4 v15, s[40:43], s60 offen lds\n"
    "s_add_u32 m0, s63, 2048\n"
    "s_nop 0\n"
    "buffer_load_dwordx4 v16, s[40:43], s60 offen lds\n"
    "s_add_u32 m0, s63, 3072\n"
    "s_nop 0\n"
    "buffer_load_dwordx4 v17, s[40:43], s60 offen lds\n"
    "s_add_u32 m0, s64, 0\n"
    "s_nop 0\n"
    "buffer_load_dwordx4 v18, s[44:47], s61 offen lds\n"
    "s_add_u32 m0, s64, 8192\n"
    "s_nop 0\n"
    "buffer_load_dwordx4 v19, s[44:47], s61 offen lds\n"
    "s_add_u32 m0, s64, 16384\n"
    "s_nop 0\n"
    "buffer_load_dwordx4 v20, s[44:47], s61 offen lds\n"
    "s_add_u32 m0, s64, 24576\n"
    "s_nop 0\n"
    "buffer_load_dwordx4 v21, s[44:47], s61 offen lds\n"
    "s_add_u32 m0, s64, 4096\n"
    "s_nop 0\n"
    "buffer_load_dwordx4 v22, s[48:51], s61 offen lds\n"
    "s_add_u32 m0, s64, 12288\n"
    "s_nop 0\n"
    "buffer_load_dwordx4 v23, s[48:51], s61 offen lds\n"
    "s_add_u32 m0, s64, 20480\n"
    "s_nop 0\n"
    "buffer_load_dwordx4 v24, s[48:51], s61 offen lds\n"
    "s_add_u32 m0, s64, 28672\n"
    "s_nop 0\n"
    "buffer_load_dwordx4 v25, s[48:51], s61 offen lds\n"
    "s_add_u32 s60, s60, 128\n"
    "s_add_u32 s67, s67, 1\n"
    "s_cmp_ge_u32 s67, s56\n"
    "s_cselect_b32 s42, 0, s42\n"
    "s_add_u32 s66, s66, 49152\n"
    "s_cmp_eq_u32 s66, 147456\n"
    "s_cselect_b32 s66, 0, s66\n"
    "s_add_u32 s63, s66, s54\n"
    "s_add_u32 s61, s61, s58\n"
    "s_add_u32 s64, s66, s55\n"
    "s_cmp_eq_u32 s67, s57\n"
    "s_cselect_b32 s61, 0, s61\n"
    "s_cselect_b64 s[44:45], s[52:53], s[44:45]\n"
    "s_cselect_b64 s[48:49], s[52:53], s[48:49]\n"
    "s_cmp_ge_u32 s67, s56\n"
    "s_cselect_b32 s46, 0, s46\n"
    "s_cselect_b32 s50, 0, s50\n"
    "s_add_u32 m0, s63, 0\n"
    "s_nop 0\n"
    "buffer_load_dwordx4 v14, s[40:43], s60 offen lds\n"
    "s_add_u32 m0, s63, 1024\n"
    "s_nop 0\n"
    "buffer_load_dwordx4 v15, s[40:43], s60 offen lds\n"
    "s_add_u32 m0, s63, 2048\n"
    "s_nop 0\n"
    "buffer_load_dwordx4 v16, s[40:43], s60 offen lds\n"
    "s_add_u32 m0, s63, 3072\n"
    "s_nop 0\n"
    "buffer_load_dwordx4 v17, s[40:43], s60 offen lds\n"
    "s_add_u32 m0, s64, 0\n"
    "s_nop 0\n"
    "buffer_load_dwordx4 v18, s[44:47], s61 offen lds\n"
    "s_add_u32 m0, s64, 8192\n"
    "s_nop 0\n"
    "buffer_load_dwordx4 v19, s[44:47], s61 offen lds\n"
    "s_add_u32 m0, s64, 16384\n"
    "s_nop 0\n"
    "buffer_load_dwordx4 v20, s[44:47], s61 offen lds\n"
    "s_add_u32 m0, s64, 24576\n"
    "s_nop 0\n"
    "buffer_load_dwordx4 v21, s[44:47], s61 offen lds\n"
    "s_add_u32 m0, s64, 4096\n"
    "s_nop 0\n"
    "buffer_load_dwordx4 v22, s[48:51], s61 offen lds\n"
    "s_add_u32 m0, s64, 12288\n"
    "s_nop 0\n"
    "buffer_load_dwordx4 v23, s[48:51], s61 offen lds\n"
    "s_add_u32 m0, s64, 20480\n"
    "s_nop 0\n"
    "buffer_load_dwordx4 v24, s[48:51], s61 offen lds\n"
    "s_add_u32 m0, s64, 28672\n"
    "s_nop 0\n"
    "buffer_load_dwordx4 v25, s[48:51], s61 offen lds\n"
    "s_add_u32 s60, s60, 128\n"
    "s_add_u32 s67, s67, 1\n"
    "s_cmp_ge_u32 s67, s56\n"
    "s_cselect_b32 s42, 0, s42\n"
    "s_add_u32 s66, s66, 49152\n"
    "s_cmp_eq_u32 s66, 147456\n"
    "s_cselect_b32 s66, 0, s66\n"
    "s_add_u32 s63, s66, s54\n"
    "s_add_u32 s61, s61, s58\n"
    "s_add_u32 s64, s66, s55\n"
    "s_cmp_eq_u32 s67, s57\n"
    "s_cselect_b32 s61, 0, s61\n"
    "s_cselect_b64 s[44:45], s[52:53], s[44:45]\n"
    "s_cselect_b64 s[48:49], s[52:53], s[48:49]\n"
    "s_cmp_ge_u32 s67, s56\n"
    "s_cselect_b32 s46, 0, s46\n"
    "s_cselect_b32 s50, 0, s50\n"
    "s_add_u32 m0, s63, 0\n"
    "s_nop 0\n"
    "buffer_load_dwordx4 v14, s[40:43], s60 offen lds\n"
    "s_add_u32 m0, s63, 1024\n"
    "s_nop 0\n"
    "buffer_load_dwordx4 v15, s[40:43], s60 offen lds\n"
    "s_add_u32 m0, s63, 2048\n"
    "s_nop 0\n"
    "buffer_load_dwordx4 v16, s[40:43], s60 offen lds\n"
    "s_add_u32 m0, s63, 3072\n"
    "s_nop 0\n"
    "buffer_load_dwordx4 v17, s[40:43], s60 offen lds\n"
    "s_add_u32 m0, s64, 0\n"
    "s_nop 0\n"
    "buffer_load_dwordx4 v18, s[44:47], s61 offen lds\n"
    "s_add_u32 m0, s64, 8192\n"
    "s_nop 0\n"
    "buffer_load_dwordx4 v19, s[44:47], s61 offen lds\n"
    "v_mov_b64 v[128:129], 0\n"
    "v_mov_b64 v[130:131], 0\n"
    "v_mov_b64 v[132:133], 0\n"
    "v_mov_b64 v[134:135], 0\n"
    "v_mov_b64 v[136:137], 0\n"
    "v_mov_b64 v[138:139], 0\n"
    "v_mov_b64 v[140:141], 0\n"
    "v_mov_b64 v[142:143], 0\n"
    "v_mov_b64 v[144:145], 0\n"
    "v_mov_b64 v[146:147], 0\n"
    "v_mov_b64 v[148:149], 0\n"
    "v_mov_b64 v[150:151], 0\n"
    "v_mov_b64 v[152:153], 0\n"
    "v_mov_b64 v[154:155], 0\n"
    "v_mov_b64 v[156:157], 0\n"
    "v_mov_b64 v[158:159], 0\n"
    "v_mov_b64 v[160:161], 0\n"
    "v_mov_b64 v[162:163], 0\n"
    "v_mov_b64 v[164:165], 0\n"
    "v_mov_b64 v[166:167], 0\n"
    "v_mov_b64 v[168:169], 0\n"
    "v_mov_b64 v[170:171], 0\n"
    "v_mov_b64 v[172:173], 0\n"
    "v_mov_b64 v[174:175], 0\n"
    "v_mov_b64 v[176:177], 0\n"
    "v_mov_b64 v[178:179], 0\n"
    "v_mov_b64 v[180:181], 0\n"
    "v_mov_b64 v[182:183], 0\n"
    "v_mov_b64 v[184:185], 0\n"
    "v_mov_b64 v[186:187], 0\n"
    "v_mov_b64 v[188:189], 0\n"
    "v_mov_b64 v[190:191], 0\n"
    "v_mov_b64 v[192:193], 0\n"
    "v_mov_b64 v[194:195], 0\n"
    "v_mov_b64 v[196:197], 0\n"
    "v_mov_b64 v[198:199], 0\n"
    "v_mov_b64 v[200:201], 0\n"
    "v_mov_b64 v[202:203], 0\n"
    "v_mov_b64 v[204:205], 0\n"
    "v_mov_b64 v[206:207], 0\n"
    "v_mov_b64 v[208:209], 0\n"
    "v_mov_b64 v[210:211], 0\n"
    "v_mov_b64 v[212:213], 0\n"
    "v_mov_b64 v[214:215], 0\n"
    "v_mov_b64 v[216:217], 0\n"
    "v_mov_b64 v[218:219], 0\n"
    "v_mov_b64 v[220:221], 0\n"
    "v_mov_b64 v[222:223], 0\n"
    "v_mov_b64 v[224:225], 0\n"
    "v_mov_b64 v[226:227], 0\n"
    "v_mov_b64 v[228:229], 0\n"
    "v_mov_b64 v[230:231], 0\n"
    "v_mov_b64 v[232:233], 0\n"
    "v_mov_b64 v[234:235], 0\n"
    "v_mov_b64 v[236:237], 0\n"
    "v_mov_b64 v[238:239], 0\n"
    "v_mov_b64 v[240:241], 0\n"
    "v_mov_b64 v[242:243], 0\n"
    "v_mov_b64 v[244:245], 0\n"
    "v_mov_b64 v[246:247], 0\n"
    "v_mov_b64 v[248:249], 0\n"
    "v_mov_b64 v[250:251], 0\n"
    "v_mov_b64 v[252:253], 0\n"
    "v_mov_b64 v[254:255], 0\n"
    "s_mov_b32 s68, 4294868992\n"
    "s_sub_u32 s62, s56, 0\n"
    "s_mov_b32 s65, 0\n"
    "s_waitcnt vmcnt(18)\n"
    "s_barrier\n"
    "ds_read_b128 v[32:35], v26 offset:0\n"
    "ds_read_b128 v[36:39], v26 offset:2048\n"
    "ds_read_b128 v[40:43], v26 offset:4096\n"
    "ds_read_b128 v[44:47], v26 offset:6144\n"
    "ds_read_b64_tr_b16 v[64:65], v28 offset:0\n"
    "ds_read_b64_tr_b16 v[66:67], v28 offset:512\n"
    "ds_read_b64_tr_b16 v[68:69], v29 offset:0\n"
    "ds_read_b64_tr_b16 v[70:71], v29 offset:512\n"
    "ds_read_b64_tr_b16 v[72:73], v30 offset:0\n"
    "ds_read_b64_tr_b16 v[74:75], v30 offset:512\n"
    "ds_read_b64_tr_b16 v[76:77], v31 offset:0\n"
    "ds_read_b64_tr_b16 v[78:79], v31 offset:512\n"
    "ds_read_b64_tr_b16 v[80:81], v28 offset:8192\n"
    "ds_read_b64_tr_b16 v[82:83], v28 offset:8704\n"
    "ds_read_b64_tr_b16 v[84:85], v29 offset:8192\n"
    "ds_read_b64_tr_b16 v[86:87], v29 offset:8704\n"
    "ds_read_b64_tr_b16 v[88:89], v30 offset:8192\n"
    "ds_read_b64_tr_b16 v[90:91], v30 offset:8704\n"
    "ds_read_b64_tr_b16 v[92:93], v31 offset:8192\n"
    "ds_read_b64_tr_b16 v[94:95], v31 offset:8704\n"
    "s_waitcnt lgkmcnt(0)\n"
    ".LGKN_TR_loop%=:\n"
    "v_mfma_f32_16x16x32_bf16 v[128:131], v[64:67], v[32:35], v[128:131]\n"
    "ds_read_b128 v[48:51], v27 offset:0\n"
    "s_add_u32 m0, s64, 16384\n"
    "v_mfma_f32_16x16x32_bf16 v[132:135], v[64:67], v[36:39], v[132:135]\n"
    "ds_read_b128 v[52:55], v27 offset:2048\n"
    "buffer_load_dwordx4 v20, s[44:47], s61 offen lds\n"
    "v_mfma_f32_16x16x32_bf16 v[136:139], v[64:67], v[40:43], v[136:139]\n"
    "ds_read_b128 v[56:59], v27 offset:4096\n"
    "s_add_u32 s60, s60, 128\n"
    "v_mfma_f32_16x16x32_bf16 v[140:143], v[64:67], v[44:47], v[140:143]\n"
    "ds_read_b128 v[60:63], v27 offset:6144\n"
    "s_add_u32 s67, s67, 1\n"
    "v_mfma_f32_16x16x32_bf16 v[144:147], v[68:71], v[32:35], v[144:147]\n"
    "ds_read_b64_tr_b16 v[96:97], v28 offset:4096\n"
    "s_add_u32 s65, s65, 1\n"
    "v_mfma_f32_16x16x32_bf16 v[148:151], v[68:71], v[36:39], v[148:151]\n"
    "ds_read_b64_tr_b16 v[98:99], v28 offset:4608\n"
    "s_add_u32 m0, s64, 24576\n"
    "v_mfma_f32_16x16x32_bf16 v[152:155], v[68:71], v[40:43], v[152:155]\n"
    "ds_read_b64_tr_b16 v[100:101], v29 offset:4096\n"
    "buffer_load_dwordx4 v21, s[44:47], s61 offen lds\n"
    "v_mfma_f32_16x16x32_bf16 v[156:159], v[68:71], v[44:47], v[156:159]\n"
    "ds_read_b64_tr_b16 v[102:103], v29 offset:4608\n"
    "s_cmp_eq_u32 s65, 3\n"
    "v_mfma_f32_16x16x32_bf16 v[160:163], v[72:75], v[32:35], v[160:163]\n"
    "ds_read_b64_tr_b16 v[104:105], v30 offset:4096\n"
    "s_cselect_b32 s65, 0, s65\n"
    "v_mfma_f32_16x16x32_bf16 v[164:167], v[72:75], v[36:39], v[164:167]\n"
    "ds_read_b64_tr_b16 v[106:107], v30 offset:4608\n"
    "s_cselect_b32 s59, s68, 49152\n"
    "v_mfma_f32_16x16x32_bf16 v[168:171], v[72:75], v[40:43], v[168:171]\n"
    "ds_read_b64_tr_b16 v[108:109], v31 offset:4096\n"
    "s_add_u32 m0, s64, 4096\n"
    "v_mfma_f32_16x16x32_bf16 v[172:175], v[72:75], v[44:47], v[172:175]\n"
    "ds_read_b64_tr_b16 v[110:111], v31 offset:4608\n"
    "buffer_load_dwordx4 v22, s[48:51], s61 offen lds\n"
    "v_mfma_f32_16x16x32_bf16 v[176:179], v[76:79], v[32:35], v[176:179]\n"
    "ds_read_b64_tr_b16 v[112:113], v28 offset:12288\n"
    "s_cmp_ge_u32 s67, s56\n"
    "v_mfma_f32_16x16x32_bf16 v[180:183], v[76:79], v[36:39], v[180:183]\n"
    "ds_read_b64_tr_b16 v[114:115], v28 offset:12800\n"
    "s_cselect_b32 s42, 0, s42\n"
    "v_mfma_f32_16x16x32_bf16 v[184:187], v[76:79], v[40:43], v[184:187]\n"
    "ds_read_b64_tr_b16 v[116:117], v29 offset:12288\n"
    "s_add_u32 s66, s66, 49152\n"
    "v_mfma_f32_16x16x32_bf16 v[188:191], v[76:79], v[44:47], v[188:191]\n"
    "ds_read_b64_tr_b16 v[118:119], v29 offset:12800\n"
    "s_add_u32 m0, s64, 12288\n"
    "v_mfma_f32_16x16x32_bf16 v[192:195], v[80:83], v[32:35], v[192:195]\n"
    "ds_read_b64_tr_b16 v[120:121], v30 offset:12288\n"
    "buffer_load_dwordx4 v23, s[48:51], s61 offen lds\n"
    "v_mfma_f32_16x16x32_bf16 v[196:199], v[80:83], v[36:39], v[196:199]\n"
    "ds_read_b64_tr_b16 v[122:123], v30 offset:12800\n"
    "s_cmp_eq_u32 s66, 147456\n"
    "v_mfma_f32_16x16x32_bf16 v[200:203], v[80:83], v[40:43], v[200:203]\n"
    "ds_read_b64_tr_b16 v[124:125], v31 offset:12288\n"
    "s_cselect_b32 s66, 0, s66\n"
    "v_mfma_f32_16x16x32_bf16 v[204:207], v[80:83], v[44:47], v[204:207]\n"
    "ds_read_b64_tr_b16 v[126:127], v31 offset:12800\n"
    "s_add_u32 s63, s66, s54\n"
    "v_mfma_f32_16x16x32_bf16 v[208:211], v[84:87], v[32:35], v[208:211]\n"
    "v_add_u32 v26, s59, v26\n"
    "s_add_u32 m0, s64, 20480\n"
    "v_mfma_f32_16x16x32_bf16 v[212:215], v[84:87], v[36:39], v[212:215]\n"
    "v_add_u32 v27, s59, v27\n"
    "buffer_load_dwordx4 v24, s[48:51], s61 offen lds\n"
    "v_mfma_f32_16x16x32_bf16 v[216:219], v[84:87], v[40:43], v[216:219]\n"
    "v_add_u32 v28, s59, v28\n"
    "v_mfma_f32_16x16x32_bf16 v[220:223], v[84:87], v[44:47], v[220:223]\n"
    "v_add_u32 v29, s59, v29\n"
    "v_mfma_f32_16x16x32_bf16 v[224:227], v[88:91], v[32:35], v[224:227]\n"
    "v_add_u32 v30, s59, v30\n"
    "v_mfma_f32_16x16x32_bf16 v[228:231], v[88:91], v[36:39], v[228:231]\n"
    "v_add_u32 v31, s59, v31\n"
    "s_add_u32 m0, s64, 28672\n"
    "v_mfma_f32_16x16x32_bf16 v[232:235], v[88:91], v[40:43], v[232:235]\n"
    "buffer_load_dwordx4 v25, s[48:51], s61 offen lds\n"
    "v_mfma_f32_16x16x32_bf16 v[236:239], v[88:91], v[44:47], v[236:239]\n"
    "v_mfma_f32_16x16x32_bf16 v[240:243], v[92:95], v[32:35], v[240:243]\n"
    "v_mfma_f32_16x16x32_bf16 v[244:247], v[92:95], v[36:39], v[244:247]\n"
    "v_mfma_f32_16x16x32_bf16 v[248:251], v[92:95], v[40:43], v[248:251]\n"
    "v_mfma_f32_16x16x32_bf16 v[252:255], v[92:95], v[44:47], v[252:255]\n"
    "s_waitcnt vmcnt(12) lgkmcnt(0)\n"
    "s_barrier\n"
    "v_mfma_f32_16x16x32_bf16 v[128:131], v[96:99], v[48:51], v[128:131]\n"
    "ds_read_b128 v[32:35], v26 offset:0\n"
    "s_add_u32 m0, s63, 0\n"
    "v_mfma_f32_16x16x32_bf16 v[132:135], v[96:99], v[52:55], v[132:135]\n"
    "ds_read_b128 v[36:39], v26 offset:2048\n"
    "buffer_load_dwordx4 v14, s[40:43], s60 offen lds\n"
    "v_mfma_f32_16x16x32_bf16 v[136:139], v[96:99], v[56:59], v[136:139]\n"
    "ds_read_b128 v[40:43], v26 offset:4096\n"
    "s_add_u32 s61, s61, s58\n"
    "v_mfma_f32_16x16x32_bf16 v[140:143], v[96:99], v[60:63], v[140:143]\n"
    "ds_read_b128 v[44:47], v26 offset:6144\n"
    "s_add_u32 s64, s66, s55\n"
    "v_mfma_f32_16x16x32_bf16 v[144:147], v[100:103], v[48:51], v[144:147]\n"
    "ds_read_b64_tr_b16 v[64:65], v28 offset:0\n"
    "v_mfma_f32_16x16x32_bf16 v[148:151], v[100:103], v[52:55], v[148:151]\n"
    "ds_read_b64_tr_b16 v[66:67], v28 offset:512\n"
    "s_add_u32 m0, s63, 1024\n"
    "v_mfma_f32_16x16x32_bf16 v[152:155], v[100:103], v[56:59], v[152:155]\n"
    "ds_read_b64_tr_b16 v[68:69], v29 offset:0\n"
    "buffer_load_dwordx4 v15, s[40:43], s60 offen lds\n"
    "v_mfma_f32_16x16x32_bf16 v[156:159], v[100:103], v[60:63], v[156:159]\n"
    "ds_read_b64_tr_b16 v[70:71], v29 offset:512\n"
    "s_cmp_eq_u32 s67, s57\n"
    "v_mfma_f32_16x16x32_bf16 v[160:163], v[104:107], v[48:51], v[160:163]\n"
    "ds_read_b64_tr_b16 v[72:73], v30 offset:0\n"
    "s_cselect_b32 s61, 0, s61\n"
    "v_mfma_f32_16x16x32_bf16 v[164:167], v[104:107], v[52:55], v[164:167]\n"
    "ds_read_b64_tr_b16 v[74:75], v30 offset:512\n"
    "s_cselect_b64 s[44:45], s[52:53], s[44:45]\n"
    "s_cselect_b64 s[48:49], s[52:53], s[48:49]\n"
    "v_mfma_f32_16x16x32_bf16 v[168:171], v[104:107], v[56:59], v[168:171]\n"
    "ds_read_b64_tr_b16 v[76:77], v31 offset:0\n"
    "s_add_u32 m0, s63, 2048\n"
    "v_mfma_f32_16x16x32_bf16 v[172:175], v[104:107], v[60:63], v[172:175]\n"
    "ds_read_b64_tr_b16 v[78:79], v31 offset:512\n"
    "buffer_load_dwordx4 v16, s[40:43], s60 offen lds\n"
    "v_mfma_f32_16x16x32_bf16 v[176:179], v[108:111], v[48:51], v[176:179]\n"
    "ds_read_b64_tr_b16 v[80:81], v28 offset:8192\n"
    "s_cmp_ge_u32 s67, s56\n"
    "v_mfma_f32_16x16x32_bf16 v[180:183], v[108:111], v[52:55], v[180:183]\n"
    "ds_read_b64_tr_b16 v[82:83], v28 offset:8704\n"
    "s_cselect_b32 s46, 0, s46\n"
    "v_mfma_f32_16x16x32_bf16 v[184:187], v[108:111], v[56:59], v[184:187]\n"
    "ds_read_b64_tr_b16 v[84:85], v29 offset:8192\n"
    "s_cselect_b32 s50, 0, s50\n"
    "v_mfma_f32_16x16x32_bf16 v[188:191], v[108:111], v[60:63], v[188:191]\n"
    "ds_read_b64_tr_b16 v[86:87], v29 offset:8704\n"
    "s_add_u32 m0, s63, 3072\n"
    "v_mfma_f32_16x16x32_bf16 v[192:195], v[112:115], v[48:51], v[192:195]\n"
    "ds_read_b64_tr_b16 v[88:89], v30 offset:8192\n"
    "buffer_load_dwordx4 v17, s[40:43], s60 offen lds\n"
    "v_mfma_f32_16x16x32_bf16 v[196:199], v[112:115], v[52:55], v[196:199]\n"
    "ds_read_b64_tr_b16 v[90:91], v30 offset:8704\n"
    "v_mfma_f32_16x16x32_bf16 v[200:203], v[112:115], v[56:59], v[200:203]\n"
    "ds_read_b64_tr_b16 v[92:93], v31 offset:8192\n"
    "v_mfma_f32_16x16x32_bf16 v[204:207], v[112:115], v[60:63], v[204:207]\n"
    "ds_read_b64_tr_b16 v[94:95], v31 offset:8704\n"
    "v_mfma_f32_16x16x32_bf16 v[208:211], v[116:119], v[48:51], v[208:211]\n"
    "s_add_u32 m0, s64, 0\n"
    "v_mfma_f32_16x16x32_bf16 v[212:215], v[116:119], v[52:55], v[212:215]\n"
    "buffer_load_dwordx4 v18, s[44:47], s61 offen lds\n"
    "v_mfma_f32_16x16x32_bf16 v[216:219], v[116:119], v[56:59], v[216:219]\n"
    "v_mfma_f32_16x16x32_bf16 v[220:223], v[116:119], v[60:63], v[220:223]\n"
    "v_mfma_f32_16x16x32_bf16 v[224:227], v[120:123], v[48:51], v[224:227]\n"
    "v_mfma_f32_16x16x32_bf16 v[228:231], v[120:123], v[52:55], v[228:231]\n"
    "s_add_u32 m0, s64, 8192\n"
    "v_mfma_f32_16x16x32_bf16 v[232:235], v[120:123], v[56:59], v[232:235]\n"
    "buffer_load_dwordx4 v19, s[44:47], s61 offen lds\n"
    "v_mfma_f32_16x16x32_bf16 v[236:239], v[120:123], v[60:63], v[236:239]\n"
    "s_sub_u32 s62, s62, 1\n"
    "v_mfma_f32_16x16x32_bf16 v[240:243], v[124:127], v[48:51], v[240:243]\n"
    "s_cmp_eq_u32 s62, 0\n"
    "v_mfma_f32_16x16x32_bf16 v[244:247], v[124:127], v[52:55], v[244:247]\n"
    "v_mfma_f32_16x16x32_bf16 v[248:251], v[124:127], v[56:59], v[248:251]\n"
    "v_mfma_f32_16x16x32_bf16 v[252:255], v[124:127], v[60:63], v[252:255]\n"
    "s_waitcnt lgkmcnt(0)\n"
    "s_cbranch_scc0 .LGKN_TR_loop%=\n"
    "s_waitcnt vmcnt(0)\n"
    "s_barrier\n"
    "s_nop 7\n"
    :
      "={v[128:143]}"(acc[0]), "={v[144:159]}"(acc[1]), "={v[160:175]}"(acc[2]), "={v[176:191]}"(acc[3]), "={v[192:207]}"(acc[4]),
      "={v[208:223]}"(acc[5]), "={v[224:239]}"(acc[6]), "={v[240:255]}"(acc[7]), "+{v[26:27]}"(ax), "+{v[28:31]}"(aw), "+{s[40:43]}"(rx),
      "+{s[44:47]}"(rw0), "+{s[48:51]}"(rw1)
    :
      "{v[14:17]}"(vox), "{v[18:21]}"(vow0), "{v[22:25]}"(vow1), "{s[52:53]}"(alt), "{s54}"(mx), "{s55}"(mw), "{s56}"(nt), "{s57}"(sw), "{s58}"(wstep)
    :
      "memory", "scc", "v32", "v33", "v34", "v35", "v36", "v37", "v38", "v39", "v40", "v41", "v42", "v43", "v44", "v45", "v46", "v47", "v48", "v49",
      "v50", "v51", "v52", "v53", "v54", "v55", "v56", "v57", "v58", "v59", "v60", "v61", "v62", "v63", "v64", "v65", "v66", "v67", "v68", "v69", "v70",
      "v71", "v72", "v73", "v74", "v75", "v76", "v77", "v78", "v79", "v80", "v81", "v82", "v83", "v84", "v85", "v86", "v87", "v88", "v89", "v90", "v91",
      "v92", "v93", "v94", "v95", "v96", "v97", "v98", "v99", "v100", "v101", "v102", "v103", "v104", "v105", "v106", "v107", "v108", "v109", "v110",
      "v111", "v112", "v113", "v114", "v115", "v116", "v117", "v118", "v119", "v120", "v121", "v122", "v123", "v124", "v125", "v126", "v127", "s59",
      "s60", "s61", "s62", "s63", "s64", "s65", "s66", "s67", "s68");
}
DI void gemm_kn_nt(GemmArgs& g, f16v (&acc)[8]) { GEMM_UNPACK
  asm volatile(
    "s_nop 4\n"
    "s_mov_b32 s60, 0\n"
    "s_mov_b32 s61, 0\n"
    "s_mov_b32 s67, 0\n"
    "s_mov_b32 s66, 0\n"
    "s_mov_b32 s63, s54\n"
    "s_mov_b32 s64, s55\n"
    "s_add_u32 m0, s63, 0\n"
    "s_nop 0\n"
    "buffer_load_dwordx4 v14, s[40:43], s60 offen lds\n"
    "s_add_u32 m0, s63, 1024\n"
    "s_nop 0\n"
    "buffer_load_dwordx4 v15, s[40:43], s60 offen lds\n"
    "s_add_u32 m0, s63, 2048\n"
    "s_nop 0\n"
    "buffer_load_dwordx4 v16, s[40:43], s60 offen lds\n"
    "s_add_u32 m0, s63, 3072\n"
    "s_nop 0\n"
    "buffer_load_dwordx4 v17, s[40:43], s60 offen lds\n"
    "s_add_u32 m0, s64, 0\n"
    "s_nop 0\n"
    "buffer_load_dwordx4 v18, s[44:47], s61 offen lds\n"
    "s_add_u32 m0, s64, 8192\n"
    "s_nop 0\n"
    "buffer_load_dwordx4 v19, s[44:47], s61 offen lds\n"
    "s_add_u32 m0, s64, 16384\n"
    "s_nop 0\n"
    "buffer_load_dwordx4 v20, s[44:47], s61 offen lds\n"
    "s_add_u32 m0, s64, 24576\n"
    "s_nop 0\n"
    "buffer_load_dwordx4 v21, s[44:47], s61 offen lds\n"
    "s_add_u32 m0, s64, 4096\n"
    "s_nop 0\n"
    "buffer_load_dwordx4 v22, s[48:51], s61 offen lds\n"
    "s_add_u32 m0, s64, 12288\n"
    "s_nop 0\n"
    "buffer_load_dwordx4 v23, s[48:51], s61 offen lds\n"
    "s_add_u32 m0, s64, 20480\n"
    "s_nop 0\n"
    "buffer_load_dwordx4 v24, s[48:51], s61 offen lds\n"
    "s_add_u32 m0, s64, 28672\n"
    "s_nop 0\n"
    "buffer_load_dwordx4 v25, s[48:51], s61 offen lds\n"
    "s_add_u32 s60, s60, 128\n"
    "s_add_u32 s67, s67, 1\n"
    "s_cmp_ge_u32 s67, s56\n"
    "s_cselect_b32 s42, 0, s42\n"
    "s_add_u32 s66, s66, 49152\n"
    "s_cmp_eq_u32 s66, 147456\n"
    "s_cselect_b32 s66, 0, s66\n"
    "s_add_u32 s63, s66, s54\n"
    "s_add_u32 s61, s61, s58\n"
    "s_add_u32 s64, s66, s55\n"
    "s_cmp_eq_u32 s67, s57\n"
    "s_cselect_b32 s61, 0, s61\n"
    "s_cselect_b64 s[44:45], s[52:53], s[44:45]\n"
    "s_cselect_b64 s[48:49], s[52:53], s[48:49]\n"
    "s_cmp_ge_u32 s67, s56\n"
    "s_cselect_b32 s46, 0, s46\n"
    "s_cselect_b32 s50, 0, s50\n"
    "s_add_u32 m0, s63, 0\n"
    "s_nop 0\n"
    "buffer_load_dwordx4 v14, s[40:43], s60 offen lds\n"
    "s_add_u32 m0, s63, 1024\n"
    "s_nop 0\n"
    "buffer_load_dwordx4 v15, s[40:43], s60 offen lds\n"
    "s_add_u32 m0, s63, 2048\n"
    "s_nop 0\n"
    "buffer_load_dwordx4 v16, s[40:43], s60 offen lds\n"
    "s_add_u32 m0, s63, 3072\n"
    "s_nop 0\n"
    "buffer_load_dwordx4 v17, s[40:43], s60 offen lds\n"
    "s_add_u32 m0, s64, 0\n"
    "s_nop 0\n"
    "buffer_load_dwordx4 v18, s[44:47], s61 offen lds\n"
    "s_add_u32 m0, s64, 8192\n"
    "s_nop 0\n"
    "buffer_load_dwordx4 v19, s[44:47], s61 offen lds\n"
    "s_add_u32 m0, s64, 16384\n"
    "s_nop 0\n"
    "buffer_load_dwordx4 v20, s[44:47], s61 offen lds\n"
    "s_add_u32 m0, s64, 24576\n"
    "s_nop 0\n"
    "buffer_load_dwordx4 v21, s[44:47], s61 offen lds\n"
    "s_add_u32 m0, s64, 4096\n"
    "s_nop 0\n"
    "buffer_load_dwordx4 v22, s[48:51], s61 offen lds\n"
    "s_add_u32 m0, s64, 12288\n"
    "s_nop 0\n"
    "buffer_load_dwordx4 v23, s[48:51], s61 offen lds\n"
    "s_add_u32 m0, s64, 20480\n"
    "s_nop 0\n"
    "buffer_load_dwordx4 v24, s[48:51], s61 offen lds\n"
    "s_add_u32 m0, s64, 28672\n"
    "s_nop 0\n"
    "buffer_load_dwordx4 v25, s[48:51], s61 offen lds\n"
    "s_add_u32 s60, s60, 128\n"
    "s_add_u32 s67, s67, 1\n"
    "s_cmp_ge_u32 s67, s56\n"
    "s_cselect_b32 s42, 0, s42\n"
    "s_add_u32 s66, s66, 49152\n"
    "s_cmp_eq_u32 s66, 147456\n"
    "s_cselect_b32 s66, 0, s66\n"
    "s_add_u32 s63, s66, s54\n"
    "s_add_u32 s61, s61, s58\n"
    "s_add_u32 s64, s66, s55\n"
    "s_cmp_eq_u32 s67, s57\n"
    "s_cselect_b32 s61, 0, s61\n"
    "s_cselect_b64 s[44:45], s[52:53], s[44:45]\n"
    "s_cselect_b64 s[48:49], s[52:53], s[48:49]\n"
    "s_cmp_ge_u32 s67, s56\n"
    "s_cselect_b32 s46, 0, s46\n"
    "s_cselect_b32 s50, 0, s50\n"
    "s_add_u32 m0, s63, 0\n"
    "s_nop 0\n"
    "buffer_load_dwordx4 v14, s[40:43], s60 offen lds\n"
    "s_add_u32 m0, s63, 1024\n"
    "s_nop 0\n"
    "buffer_load_dwordx4 v15, s[40:43], s60 offen lds\n"
    "s_add_u32 m0, s63, 2048\n"
    "s_nop 0\n"
    "buffer_load_dwordx4 v16, s[40:43], s60 offen lds\n"
    "s_add_u32 m0, s63, 3072\n"
    "s_nop 0\n"
    "buffer_load_dwordx4 v17, s[40:43], s60 offen lds\n"
    "s_add_u32 m0, s64, 0\n"
    "s_nop 0\n"
    "buffer_load_dwordx4 v18, s[44:47], s61 offen lds\n"
    "s_add_u32 m0, s64, 8192\n"
    "s_nop 0\n"
    "buffer_load_dwordx4 v19, s[44:47], s61 offen lds\n"
    "v_mov_b64 v[128:129], 0\n"
    "v_mov_b64 v[130:131], 0\n"
    "v_mov_b64 v[132:133], 0\n"
    "v_mov_b64 v[134:135], 0\n"
    "v_mov_b64 v[136:137], 0\n"
    "v_mov_b64 v[138:139], 0\n"
    "v_mov_b64 v[140:141], 0\n"
    "v_mov_b64 v[142:143], 0\n"
    "v_mov_b64 v[144:145], 0\n"
    "v_mov_b64 v[146:147], 0\n"
    "v_mov_b64 v[148:149], 0\n"
    "v_mov_b64 v[150:151], 0\n"
    "v_mov_b64 v[152:153], 0\n"
    "v_mov_b64 v[154:155], 0\n"
    "v_mov_b64 v[156:157], 0\n"
    "v_mov_b64 v[158:159], 0\n"
    "v_mov_b64 v[160:161], 0\n"
    "v_mov_b64 v[162:163], 0\n"
    "v_mov_b64 v[164:165], 0\n"
    "v_mov_b64 v[166:167], 0\n"
    "v_mov_b64 v[168:169], 0\n"
    "v_mov_b64 v[170:171], 0\n"
    "v_mov_b64 v[172:173], 0\n"
    "v_mov_b64 v[174:175], 0\n"
    "v_mov_b64 v[176:177], 0\n"
    "v_mov_b64 v[178:179], 0\n"
    "v_mov_b64 v[180:181], 0\n"
    "v_mov_b64 v[182:183], 0\n"
    "v_mov_b64 v[184:185], 0\n"
    "v_mov_b64 v[186:187], 0\n"
    "v_mov_b64 v[188:189], 0\n"
    "v_mov_b64 v[190:191], 0\n"
    "v_mov_b64 v[192:193], 0\n"
    "v_mov_b64 v[194:195], 0\n"
    "v_mov_b64 v[196:197], 0\n"
    "v_mov_b64 v[198:199], 0\n"
    "v_mov_b64 v[200:201], 0\n"
    "v_mov_b64 v[202:203], 0\n"
    "v_mov_b64 v[204:205], 0\n"
    "v_mov_b64 v[206:207], 0\n"
    "v_mov_b64 v[208:209], 0\n"
    "v_mov_b64 v[210:211], 0\n"
    "v_mov_b64 v[212:213], 0\n"
    "v_mov_b64 v[214:215], 0\n"
    "v_mov_b64 v[216:217], 0\n"
    "v_mov_b64 v[218:219], 0\n"
    "v_mov_b64 v[220:221], 0\n"
    "v_mov_b64 v[222:223], 0\n"
    "v_mov_b64 v[224:225], 0\n"
    "v_mov_b64 v[226:227], 0\n"
    "v_mov_b64 v[228:229], 0\n"
    "v_mov_b64 v[230:231], 0\n"
    "v_mov_b64 v[232:233], 0\n"
    "v_mov_b64 v[234:235], 0\n"
    "v_mov_b64 v[236:237], 0\n"
    "v_mov_b64 v[238:239], 0\n"
    "v_mov_b64 v[240:241], 0\n"
    "v_mov_b64 v[242:243], 0\n"
    "v_mov_b64 v[244:245], 0\n"
    "v_mov_b64 v[246:247], 0\n"
    "v_mov_b64 v[248:249], 0\n"
    "v_mov_b64 v[250:251], 0\n"
    "v_mov_b64 v[252:253], 0\n"
    "v_mov_b64 v[254:255], 0\n"
    "s_mov_b32 s68, 4294868992\n"
    "s_sub_u32 s62, s56, 0\n"
    "s_mov_b32 s65, 0\n"
    "s_waitcnt vmcnt(18)\n"
    "s_barrier\n"
    "ds_read_b128 v[32:35], v26 offset:0\n"
    "ds_read_b128 v[36:39], v26 offset:2048\n"
    "ds_read_b128 v[40:43], v26 offset:4096\n"
    "ds_read_b128 v[44:47], v26 offset:6144\n"
    "ds_read_b64_tr_b16 v[64:65], v28 offset:0\n"
    "ds_read_b64_tr_b16 v[66:67], v28 offset:512\n"
    "ds_read_b64_tr_b16 v[68:69], v29 offset:0\n"
    "ds_read_b64_tr_b16 v[70:71], v29 offset:512\n"
    "ds_read_b64_tr_b16 v[72:73], v30 offset:0\n"
    "ds_read_b64_tr_b16 v[74:75], v30 offset:512\n"
    "ds_read_b64_tr_b16 v[76:77], v31 offset:0\n"
    "ds_read_b64_tr_b16 v[78:79], v31 offset:512\n"
    "ds_read_b64_tr_b16 v[80:81], v28 offset:8192\n"
    "ds_read_b64_tr_b16 v[82:83], v28 offset:8704\n"
    "ds_read_b64_tr_b16 v[84:85], v29 offset:8192\n"
    "ds_read_b64_tr_b16 v[86:87], v29 offset:8704\n"
    "ds_read_b64_tr_b16 v[88:89], v30 offset:8192\n"
    "ds_read_b64_tr_b16 v[90:91], v30 offset:8704\n"
    "ds_read_b64_tr_b16 v[92:93], v31 offset:8192\n"
    "ds_read_b64_tr_b16 v[94:95], v31 offset:8704\n"
    "s_waitcnt lgkmcnt(0)\n"
    ".LGKN_NT_loop%=:\n"
    "v_mfma_f32_16x16x32_bf16 v[128:131], v[32:35], v[64:67], v[128:131]\n"
    "ds_read_b128 v[48:51], v27 offset:0\n"
    "s_add_u32 m0, s64, 16384\n"
    "v_mfma_f32_16x16x32_bf16 v[132:135], v[36:39], v[64:67], v[132:135]\n"
    "ds_read_b128 v[52:55], v27 offset:2048\n"
    "buffer_load_dwordx4 v20, s[44:47], s61 offen lds\n"
    "v_mfma_f32_16x16x32_bf16 v[136:139], v[40:43], v[64:67], v[136:139]\n"
    "ds_read_b128 v[56:59], v27 offset:4096\n"
    "s_add_u32 s60, s60, 128\n"
    "v_mfma_f32_16x16x32_bf16 v[140:143], v[44:47], v[64:67], v[140:143]\n"
    "ds_read_b128 v[60:63], v27 offset:6144\n"
    "s_add_u32 s67, s67, 1\n"
    "v_mfma_f32_16x16x32_bf16 v[144:147], v[32:35], v[68:71], v[144:147]\n"
    "ds_read_b64_tr_b16 v[96:97], v28 offset:4096\n"
    "s_add_u32 s65, s65, 1\n"
    "v_mfma_f32_16x16x32_bf16 v[148:151], v[36:39], v[68:71], v[148:151]\n"
    "ds_read_b64_tr_b16 v[98:99], v28 offset:4608\n"
    "s_add_u32 m0, s64, 24576\n"
    "v_mfma_f32_16x16x32_bf16 v[152:155], v[40:43], v[68:71], v[152:155]\n"
    "ds_read_b64_tr_b16 v[100:101], v29 offset:4096\n"
    "buffer_load_dwordx4 v21, s[44:47], s61 offen lds\n"
    "v_mfma_f32_16x16x32_bf16 v[156:159], v[44:47], v[68:71], v[156:159]\n"
    "ds_read_b64_tr_b16 v[102:103], v29 offset:4608\n"
    "s_cmp_eq_u32 s65, 3\n"
    "v_mfma_f32_16x16x32_bf16 v[160:163], v[32:35], v[72:75], v[160:163]\n"
    "ds_read_b64_tr_b16 v[104:105], v30 offset:4096\n"
    "s_cselect_b32 s65, 0, s65\n"
    "v_mfma_f32_16x16x32_bf16 v[164:167], v[36:39], v[72:75], v[164:167]\n"
    "ds_read_b64_tr_b16 v[106:107], v30 offset:4608\n"
    "s_cselect_b32 s59, s68, 49152\n"
    "v_mfma_f32_16x16x32_bf16 v[168:171], v[40:43], v[72:75], v[168:171]\n"
    "ds_read_b64_tr_b16 v[108:109], v31 offset:4096\n"
    "s_add_u32 m0, s64, 4096\n"
    "v_mfma_f32_16x16x32_bf16 v[172:175], v[44:47], v[72:75], v[172:175]\n"
    "ds_read_b64_tr_b16 v[110:111], v31 offset:4608\n"
    "buffer_load_dwordx4 v22, s[48:51], s61 offen lds\n"
    "v_mfma_f32_16x16x32_bf16 v[176:179], v[32:35], v[76:79], v[176:179]\n"
    "ds_read_b64_tr_b16 v[112:113], v28 offset:12288\n"
    "s_cmp_ge_u32 s67, s56\n"
    "v_mfma_f32_16x16x32_bf16 v[180:183], v[36:39], v[76:79], v[180:183]\n"
    "ds_read_b64_tr_b16 v[114:115], v28 offset:12800\n"
    "s_cselect_b32 s42, 0, s42\n"
    "v_mfma_f32_16x16x32_bf16 v[184:187], v[40:43], v[76:79], v[184:187]\n"
    "ds_read_b64_tr_b16 v[116:117], v29 offset:12288\n"
    "s_add_u32 s66, s66, 49152\n"
    "v_mfma_f32_16x16x32_bf16 v[188:191], v[44:47], v[76:79], v[188:191]\n"
    "ds_read_b64_tr_b16 v[118:119], v29 offset:12800\n"
    "s_add_u32 m0, s64, 12288\n"
    "v_mfma_f32_16x16x32_bf16 v[192:195], v[32:35], v[80:83], v[192:195]\n"
    "ds_read_b64_tr_b16 v[120:121], v30 offset:12288\n"
    "buffer_load_dwordx4 v23, s[48:51], s61 offen lds\n"
    "v_mfma_f32_16x16x32_bf16 v[196:199], v[36:39], v[80:83], v[196:199]\n"
    "ds_read_b64_tr_b16 v[122:123], v30 offset:12800\n"
    "s_cmp_eq_u32 s66, 147456\n"
    "v_mfma_f32_16x16x32_bf16 v[200:203], v[40:43], v[80:83], v[200:203]\n"
    "ds_read_b64_tr_b16 v[124:125], v31 offset:12288\n"
    "s_cselect_b32 s66, 0, s66\n"
    "v_mfma_f32_16x16x32_bf16 v[204:207], v[44:47], v[80:83], v[204:207]\n"
    "ds_read_b64_tr_b16 v[126:127], v31 offset:12800\n"
    "s_add_u32 s63, s66, s54\n"
    "v_mfma_f32_16x16x32_bf16 v[208:211], v[32:35], v[84:87], v[208:211]\n"
    "v_add_u32 v26, s59, v26\n"
    "s_add_u32 m0, s64, 20480\n"
    "v_mfma_f32_16x16x32_bf16 v[212:215], v[36:39], v[84:87], v[212:215]\n"
    "v_add_u32 v27, s59, v27\n"
    "buffer_load_dwordx4 v24, s[48:51], s61 offen lds\n"
    "v_mfma_f32_16x16x32_bf16 v[216:219], v[40:43], v[84:87], v[216:219]\n"
    "v_add_u32 v28, s59, v28\n"
    "v_mfma_f32_16x16x32_bf16 v[220:223], v[44:47], v[84:87], v[220:223]\n"
    "v_add_u32 v29, s59, v29\n"
    "v_mfma_f32_16x16x32_bf16 v[224:227], v[32:35], v[88:91], v[224:227]\n"
    "v_add_u32 v30, s59, v30\n"
    "v_mfma_f32_16x16x32_bf16 v[228:231], v[36:39], v[88:91], v[228:231]\n"
    "v_add_u32 v31, s59, v31\n"
    "s_add_u32 m0, s64, 28672\n"
    "v_mfma_f32_16x16x32_bf16 v[232:235], v[40:43], v[88:91], v[232:235]\n"
    "buffer_load_dwordx4 v25, s[48:51], s61 offen lds\n"
    "v_mfma_f32_16x16x32_bf16 v[236:239], v[44:47], v[88:91], v[236:239]\n"
    "v_mfma_f32_16x16x32_bf16 v[240:243], v[32:35], v[92:95], v[240:243]\n"
    "v_mfma_f32_16x16x32_bf16 v[244:247], v[36:39], v[92:95], v[244:247]\n"
    "v_mfma_f32_16x16x32_bf16 v[248:251], v[40:43], v[92:95], v[248:251]\n"
    "v_mfma_f32_16x16x32_bf16 v[252:255], v[44:47], v[92:95], v[252:255]\n"
    "s_waitcnt vmcnt(12) lgkmcnt(0)\n"
    "s_barrier\n"
    "v_mfma_f32_16x16x32_bf16 v[128:131], v[48:51], v[96:99], v[128:131]\n"
    "ds_read_b128 v[32:35], v26 offset:0\n"
    "s_add_u32 m0, s63, 0\n"
    "v_mfma_f32_16x16x32_bf16 v[132:135], v[52:55], v[96:99], v[132:135]\n"
    "ds_read_b128 v[36:39], v26 offset:2048\n"
    "buffer_load_dwordx4 v14, s[40:43], s60 offen lds\n"
    "v_mfma_f32_16x16x32_bf16 v[136:139], v[56:59], v[96:99], v[136:139]\n"
    "ds_read_b128 v[40:43], v26 offset:4096\n"
    "s_add_u32 s61, s61, s58\n"
    "v_mfma_f32_16x16x32_bf16 v[140:143], v[60:63], v[96:99], v[140:143]\n"
    "ds_read_b128 v[44:47], v26 offset:6144\n"
    "s_add_u32 s64, s66, s55\n"
    "v_mfma_f32_16x16x32_bf16 v[144:147], v[48:51], v[100:103], v[144:147]\n"
    "ds_read_b64_tr_b16 v[64:65], v28 offset:0\n"
    "v_mfma_f32_16x16x32_bf16 v[148:151], v[52:55], v[100:103], v[148:151]\n"
    "ds_read_b64_tr_b16 v[66:67], v28 offset:512\n"
    "s_add_u32 m0, s63, 1024\n"
    "v_mfma_f32_16x16x32_bf16 v[152:155], v[56:59], v[100:103], v[152:155]\n"
    "ds_read_b64_tr_b16 v[68:69], v29 offset:0\n"
    "buffer_load_dwordx4 v15, s[40:43], s60 offen lds\n"
    "v_mfma_f32_16x16x32_bf16 v[156:159], v[60:63], v[100:103], v[156:159]\n"
    "ds_read_b64_tr_b16 v[70:71], v29 offset:512\n"
    "s_cmp_eq_u32 s67, s57\n"
    "v_mfma_f32_16x16x32_bf16 v[160:163], v[48:51], v[104:107], v[160:163]\n"
    "ds_read_b64_tr_b16 v[72:73], v30 offset:0\n"
    "s_cselect_b32 s61, 0, s61\n"
    "v_mfma_f32_16x16x32_bf16 v[164:167], v[52:55], v[104:107], v[164:167]\n"
    "ds_read_b64_tr_b16 v[74:75], v30 offset:512\n"
    "s_cselect_b64 s[44:45], s[52:53], s[44:45]\n"
    "s_cselect_b64 s[48:49], s[52:53], s[48:49]\n"
    "v_mfma_f32_16x16x32_bf16 v[168:171], v[56:59], v[104:107], v[168:171]\n"
    "ds_read_b64_tr_b16 v[76:77], v31 offset:0\n"
    "s_add_u32 m0, s63, 2048\n"
    "v_mfma_f32_16x16x32_bf16 v[172:175], v[60:63], v[104:107], v[172:175]\n"
    "ds_read_b64_tr_b16 v[78:79], v31 offset:512\n"
    "buffer_load_dwordx4 v16, s[40:43], s60 offen lds\n"
    "v_mfma_f32_16x16x32_bf16 v[176:179], v[48:51], v[108:111], v[176:179]\n"
    "ds_read_b64_tr_b16 v[80:81], v28 offset:8192\n"
    "s_cmp_ge_u32 s67, s56\n"
    "v_mfma_f32_16x16x32_bf16 v[180:183], v[52:55], v[108:111], v[180:183]\n"
    "ds_read_b64_tr_b16 v[82:83], v28 offset:8704\n"
    "s_cselect_b32 s46, 0, s46\n"
    "v_mfma_f32_16x16x32_bf16 v[184:187], v[56:59], v[108:111], v[184:187]\n"
    "ds_read_b64_tr_b16 v[84:85], v29 offset:8192\n"
    "s_cselect_b32 s50, 0, s50\n"
    "v_mfma_f32_16x16x32_bf16 v[188:191], v[60:63], v[108:111], v[188:191]\n"
    "ds_read_b64_tr_b16 v[86:87], v29 offset:8704\n"
    "s_add_u32 m0, s63, 3072\n"
    "v_mfma_f32_16x16x32_bf16 v[192:195], v[48:51], v[112:115], v[192:195]\n"
    "ds_read_b64_tr_b16 v[88:89], v30 offset:8192\n"
    "buffer_load_dwordx4 v17, s[40:43], s60 offen lds\n"
    "v_mfma_f32_16x16x32_bf16 v[196:199], v[52:55], v[112:115], v[196:199]\n"
    "ds_read_b64_tr_b16 v[90:91], v30 offset:8704\n"
    "v_mfma_f32_16x16x32_bf16 v[200:203], v[56:59], v[112:115], v[200:203]\n"
    "ds_read_b64_tr_b16 v[92:93], v31 offset:8192\n"
    "v_mfma_f32_16x16x32_bf16 v[204:207], v[60:63], v[112:115], v[204:207]\n"
    "ds_read_b64_tr_b16 v[94:95], v31 offset:8704\n"
    "v_mfma_f32_16x16x32_bf16 v[208:211], v[48:51], v[116:119], v[208:211]\n"
    "s_add_u32 m0, s64, 0\n"
    "v_mfma_f32_16x16x32_bf16 v[212:215], v[52:55], v[116:119], v[212:215]\n"
    "buffer_load_dwordx4 v18, s[44:47], s61 offen lds\n"
    "v_mfma_f32_16x16x32_bf16 v[216:219], v[56:59], v[116:119], v[216:219]\n"
    "v_mfma_f32_16x16x32_bf16 v[220:223], v[60:63], v[116:119], v[220:223]\n"
    "v_mfma_f32_16x16x32_bf16 v[224:227], v[48:51], v[120:123], v[224:227]\n"
    "v_mfma_f32_16x16x32_bf16 v[228:231], v[52:55], v[120:123], v[228:231]\n"
    "s_add_u32 m0, s64, 8192\n"
    "v_mfma_f32_16x16x32_bf16 v[232:235], v[56:59], v[120:123], v[232:235]\n"
    "buffer_load_dwordx4 v19, s[44:47], s61 offen lds\n"
    "v_mfma_f32_16x16x32_bf16 v[236:239], v[60:63], v[120:123], v[236:239]\n"
    "s_sub_u32 s62, s62, 1\n"
    "v_mfma_f32_16x16x32_bf16 v[240:243], v[48:51], v[124:127], v[240:243]\n"
    "s_cmp_eq_u32 s62, 0\n"
    "v_mfma_f32_16x16x32_bf16 v[244:247], v[52:55], v[124:127], v[244:247]\n"
    "v_mfma_f32_16x16x32_bf16 v[248:251], v[56:59], v[124:127], v[248:251]\n"
    "v_mfma_f32_16x16x32_bf16 v[252:255], v[60:63], v[124:127], v[252:255]\n"
    "s_waitcnt lgkmcnt(0)\n"
    "s_cbranch_scc0 .LGKN_NT_loop%=\n"
    "s_waitcnt vmcnt(0)\n"
    "s_barrier\n"
    "s_nop 7\n"
    :
      "={v[128:143]}"(acc[0]), "={v[144:159]}"(acc[1]), "={v[160:175]}"(acc[2]), "={v[176:191]}"(acc[3]), "={v[192:207]}"(acc[4]),
      "={v[208:223]}"(acc[5]), "={v[224:239]}"(acc[6]), "={v[240:255]}"(acc[7]), "+{v[26:27]}"(ax), "+{v[28:31]}"(aw), "+{s[40:43]}"(rx),
      "+{s[44:47]}"(rw0), "+{s[48:51]}"(rw1)
    :
      "{v[14:17]}"(vox), "{v[18:21]}"(vow0), "{v[22:25]}"(vow1), "{s[52:53]}"(alt), "{s54}"(mx), "{s55}"(mw), "{s56}"(nt), "{s57}"(sw), "{s58}"(wstep)
    :
      "memory", "scc", "v32", "v33", "v34", "v35", "v36", "v37", "v38", "v39", "v40", "v41", "v42", "v43", "v44", "v45", "v46", "v47", "v48", "v49",
      "v50", "v51", "v52", "v53", "v54", "v55", "v56", "v57", "v58", "v59", "v60", "v61", "v62", "v63", "v64", "v65", "v66", "v67", "v68", "v69", "v70",
      "v71", "v72", "v73", "v74", "v75", "v76", "v77", "v78", "v79", "v80", "v81", "v82", "v83", "v84", "v85", "v86", "v87", "v88", "v89", "v90", "v91",
      "v92", "v93", "v94", "v95", "v96", "v97", "v98", "v99", "v100", "v101", "v102", "v103", "v104", "v105", "v106", "v107", "v108", "v109", "v110",
      "v111", "v112", "v113", "v114", "v115", "v116", "v117", "v118", "v119", "v120", "v121", "v122", "v123", "v124", "v125", "v126", "v127", "s59",
      "s60", "s61", "s62", "s63", "s64", "s65", "s66", "s67", "s68");
}
DI void gemm_nk_nt(GemmArgs& g, f16v (&acc)[8]) { GEMM_UNPACK
  asm volatile(
    "s_nop 4\n"
    "s_mov_b32 s60, 0\n"
    "s_mov_b32 s61, 0\n"
    "s_mov_b32 s67, 0\n"
    "s_mov_b32 s66, 0\n"
    "s_mov_b32 s63, s54\n"
    "s_mov_b32 s64, s55\n"
    "s_add_u32 m0, s63, 0\n"
    "s_nop 0\n"
    "buffer_load_dwordx4 v14, s[40:43], s60 offen lds\n"
    "s_add_u32 m0, s63, 1024\n"
    "s_nop 0\n"
    "buffer_load_dwordx4 v15, s[40:43], s60 offen lds\n"
    "s_add_u32 m0, s63, 2048\n"
    "s_nop 0\n"
    "buffer_load_dwordx4 v16, s[40:43], s60 offen lds\n"
    "s_add_u32 m0, s63, 3072\n"
    "s_nop 0\n"
    "buffer_load_dwordx4 v17, s[40:43], s60 offen lds\n"
    "s_add_u32 m0, s64, 0\n"
    "s_nop 0\n"
    "buffer_load_dwordx4 v18, s[44:47], s61 offen lds\n"
    "s_add_u32 m0, s64, 1024\n"
    "s_nop 0\n"
    "buffer_load_dwordx4 v19, s[44:47], s61 offen lds\n"
    "s_add_u32 m0, s64, 2048\n"
    "s_nop 0\n"
    "buffer_load_dwordx4 v20, s[44:47], s61 offen lds\n"
    "s_add_u32 m0, s64, 3072\n"
    "s_nop 0\n"
    "buffer_load_dwordx4 v21, s[44:47], s61 offen lds\n"
    "s_add_u32 m0, s64, 8192\n"
    "s_nop 0\n"
    "buffer_load_dwordx4 v22, s[48:51], s61 offen lds\n"
    "s_add_u32 m0, s64, 9216\n"
    "s_nop 0\n"
    "buffer_load_dwordx4 v23, s[48:51], s61 offen lds\n"
    "s_add_u32 m0, s64, 10240\n"
    "s_nop 0\n"
    "buffer_load_dwordx4 v24, s[48:51], s61 offen lds\n"
    "s_add_u32 m0, s64, 11264\n"
    "s_nop 0\n"
    "buffer_load_dwordx4 v25, s[48:51], s61 offen lds\n"
    "s_add_u32 s60, s60, 128\n"
    "s_add_u32 s67, s67, 1\n"
    "s_cmp_ge_u32 s67, s56\n"
    "s_cselect_b32 s42, 0, s42\n"
    "s_add_u32 s66, s66, 49152\n"
    "s_cmp_eq_u32 s66, 147456\n"
    "s_cselect_b32 s66, 0, s66\n"
    "s_add_u32 s63, s66, s54\n"
    "s_add_u32 s61, s61, s58\n"
    "s_add_u32 s64, s66, s55\n"
    "s_cmp_eq_u32 s67, s57\n"
    "s_cselect_b32 s61, 0, s61\n"
    "s_cselect_b64 s[44:45], s[52:53], s[44:45]\n"
    "s_cselect_b64 s[48:49], s[52:53], s[48:49]\n"
    "s_cmp_ge_u32 s67, s56\n"
    "s_cselect_b32 s46, 0, s46\n"
    "s_cselect_b32 s50, 0, s50\n"
    "s_add_u32 m0, s63, 0\n"
    "s_nop 0\n"
    "buffer_load_dwordx4 v14, s[40:43], s60 offen lds\n"
    "s_add_u32 m0, s63, 1024\n"
    "s_nop 0\n"
    "buffer_load_dwordx4 v15, s[40:43], s60 offen lds\n"
    "s_add_u32 m0, s63, 2048\n"
    "s_nop 0\n"
    "buffer_load_dwordx4 v16, s[40:43], s60 offen lds\n"
    "s_add_u32 m0, s63, 3072\n"
    "s_nop 0\n"
    "buffer_load_dwordx4 v17, s[40:43], s60 offen lds\n"
    "s_add_u32 m0, s64, 0\n"
    "s_nop 0\n"
    "buffer_load_dwordx4 v18, s[44:47], s61 offen lds\n"
    "s_add_u32 m0, s64, 1024\n"
    "s_nop 0\n"
    "buffer_load_dwordx4 v19, s[44:47], s61 offen lds\n"
    "s_add_u32 m0, s64, 2048\n"
    "s_nop 0\n"
    "buffer_load_dwordx4 v20, s[44:47], s61 offen lds\n"
    "s_add_u32 m0, s64, 3072\n"
    "s_nop 0\n"
    "buffer_load_dwordx4 v21, s[44:47], s61 offen lds\n"
    "s_add_u32 m0, s64, 8192\n"
    "s_nop 0\n"
    "buffer_load_dwordx4 v22, s[48:51], s61 offen lds\n"
    "s_add_u32 m0, s64, 9216\n"
    "s_nop 0\n"
    "buffer_load_dwordx4 v23, s[48:51], s61 offen lds\n"
    "s_add_u32 m0, s64, 10240\n"
    "s_nop 0\n"
    "buffer_load_dwordx4 v24, s[48:51], s61 offen lds\n"
    "s_add_u32 m0, s64, 11264\n"
    "s_nop 0\n"
    "buffer_load_dwordx4 v25, s[48:51], s61 offen lds\n"
    "s_add_u32 s60, s60, 128\n"
    "s_add_u32 s67, s67, 1\n"
    "s_cmp_ge_u32 s67, s56\n"
    "s_cselect_b32 s42, 0, s42\n"
    "s_add_u32 s66, s66, 49152\n"
    "s_cmp_eq_u32 s66, 147456\n"
    "s_cselect_b32 s66, 0, s66\n"
    "s_add_u32 s63, s66, s54\n"
    "s_add_u32 s61, s61, s58\n"
    "s_add_u32 s64, s66, s55\n"
    "s_cmp_eq_u32 s67, s57\n"
    "s_cselect_b32 s61, 0, s61\n"
    "s_cselect_b64 s[44:45], s[52:53], s[44:45]\n"
    "s_cselect_b64 s[48:49], s[52:53], s[48:49]\n"
    "s_cmp_ge_u32 s67, s56\n"
    "s_cselect_b32 s46, 0, s46\n"
    "s_cselect_b32 s50, 0, s50\n"
    "s_add_u32 m0, s63, 0\n"
    "s_nop 0\n"
    "buffer_load_dwordx4 v14, s[40:43], s60 offen lds\n"
    "s_add_u32 m0, s63, 1024\n"
    "s_nop 0\n"
    "buffer_load_dwordx4 v15, s[40:43], s60 offen lds\n"
    "s_add_u32 m0, s63, 2048\n"
    "s_nop 0\n"
    "buffer_load_dwordx4 v16, s[40:43], s60 offen lds\n"
    "s_add_u32 m0, s63, 3072\n"
    "s_nop 0\n"
    "buffer_load_dwordx4 v17, s[40:43], s60 offen lds\n"
    "s_add_u32 m0, s64, 0\n"
    "s_nop 0\n"
    "buffer_load_dwordx4 v18, s[44:47], s61 offen lds\n"
    "s_add_u32 m0, s64, 1024\n"
    "s_nop 0\n"
    "buffer_load_dwordx4 v19, s[44:47], s61 offen lds\n"
    "v_mov_b64 v[128:129], 0\n"
    "v_mov_b64 v[130:131], 0\n"
    "v_mov_b64 v[132:133], 0\n"
    "v_mov_b64 v[134:135], 0\n"
    "v_mov_b64 v[136:137], 0\n"
    "v_mov_b64 v[138:139], 0\n"
    "v_mov_b64 v[140:141], 0\n"
    "v_mov_b64 v[142:143], 0\n"
    "v_mov_b64 v[144:145], 0\n"
    "v_mov_b64 v[146:147], 0\n"
    "v_mov_b64 v[148:149], 0\n"
    "v_mov_b64 v[150:151], 0\n"
    "v_mov_b64 v[152:153], 0\n"
    "v_mov_b64 v[154:155], 0\n"
    "v_mov_b64 v[156:157], 0\n"
    "v_mov_b64 v[158:159], 0\n"
    "v_mov_b64 v[160:161], 0\n"
    "v_mov_b64 v[162:163], 0\n"
    "v_mov_b64 v[164:165], 0\n"
    "v_mov_b64 v[166:167], 0\n"
    "v_mov_b64 v[168:169], 0\n"
    "v_mov_b64 v[170:171], 0\n"
    "v_mov_b64 v[172:173], 0\n"
    "v_mov_b64 v[174:175], 0\n"
    "v_mov_b64 v[176:177], 0\n"
    "v_mov_b64 v[178:179], 0\n"
    "v_mov_b64 v[180:181], 0\n"
    "v_mov_b64 v[182:183], 0\n"
    "v_mov_b64 v[184:185], 0\n"
    "v_mov_b64 v[186:187], 0\n"
    "v_mov_b64 v[188:189], 0\n"
    "v_mov_b64 v[190:191], 0\n"
    "v_mov_b64 v[192:193], 0\n"
    "v_mov_b64 v[194:195], 0\n"
    "v_mov_b64 v[196:197], 0\n"
    "v_mov_b64 v[198:199], 0\n"
    "v_mov_b64 v[200:201], 0\n"
    "v_mov_b64 v[202:203], 0\n"
    "v_mov_b64 v[204:205], 0\n"
    "v_mov_b64 v[206:207], 0\n"
    "v_mov_b64 v[208:209], 0\n"
    "v_mov_b64 v[210:211], 0\n"
    "v_mov_b64 v[212:213], 0\n"
    "v_mov_b64 v[214:215], 0\n"
    "v_mov_b64 v[216:217], 0\n"
    "v_mov_b64 v[218:219], 0\n"
    "v_mov_b64 v[220:221], 0\n"
    "v_mov_b64 v[222:223], 0\n"
    "v_mov_b64 v[224:225], 0\n"
    "v_mov_b64 v[226:227], 0\n"
    "v_mov_b64 v[228:229], 0\n"
    "v_mov_b64 v[230:231], 0\n"
    "v_mov_b64 v[232:233], 0\n"
    "v_mov_b64 v[234:235], 0\n"
    "v_mov_b64 v[236:237], 0\n"
    "v_mov_b64 v[238:239], 0\n"
    "v_mov_b64 v[240:241], 0\n"
    "v_mov_b64 v[242:243], 0\n"
    "v_mov_b64 v[244:245], 0\n"
    "v_mov_b64 v[246:247], 0\n"
    "v_mov_b64 v[248:249], 0\n"
    "v_mov_b64 v[250:251], 0\n"
    "v_mov_b64 v[252:253], 0\n"
    "v_mov_b64 v[254:255], 0\n"
    "s_mov_b32 s68, 4294868992\n"
    "s_sub_u32 s62, s56, 0\n"
    "s_mov_b32 s65, 0\n"
    "s_waitcnt vmcnt(18)\n"
    "s_barrier\n"
    "ds_read_b128 v[32:35], v26 offset:0\n"
    "ds_read_b128 v[36:39], v26 offset:2048\n"
    "ds_read_b128 v[40:43], v26 offset:4096\n"
    "ds_read_b128 v[44:47], v26 offset:6144\n"
    "ds_read_b128 v[64:67], v28 offset:0\n"
    "ds_read_b128 v[68:71], v28 offset:2048\n"
    "ds_read_b128 v[72:75], v28 offset:4096\n"
    "ds_read_b128 v[76:79], v28 offset:6144\n"
    "ds_read_b128 v[80:83], v28 offset:8192\n"
    "ds_read_b128 v[84:87], v28 offset:10240\n"
    "ds_read_b128 v[88:91], v28 offset:12288\n"
    "ds_read_b128 v[92:95], v28 offset:14336\n"
    "s_waitcnt lgkmcnt(0)\n"
    ".LGNK_NT_loop%=:\n"
    "v_mfma_f32_16x16x32_bf16 v[128:131], v[32:35], v[64:67], v[128:131]\n"
    "ds_read_b128 v[48:51], v27 offset:0\n"
    "s_add_u32 m0, s64, 2048\n"
    "v_mfma_f32_16x16x32_bf16 v[132:135], v[36:39], v[64:67], v[132:135]\n"
    "ds_read_b128 v[52:55], v27 offset:2048\n"
    "buffer_load_dwordx4 v20, s[44:47], s61 offen lds\n"
    "v_mfma_f32_16x16x32_bf16 v[136:139], v[40:43], v[64:67], v[136:139]\n"
    "ds_read_b128 v[56:59], v27 offset:4096\n"
    "s_add_u32 s60, s60, 128\n"
    "v_mfma_f32_16x16x32_bf16 v[140:143], v[44:47], v[64:67], v[140:143]\n"
    "ds_read_b128 v[60:63], v27 offset:6144\n"
    "s_add_u32 s67, s67, 1\n"
    "v_mfma_f32_16x16x32_bf16 v[144:147], v[32:35], v[68:71], v[144:147]\n"
    "ds_read_b128 v[96:99], v29 offset:0\n"
    "s_add_u32 s65, s65, 1\n"
    "v_mfma_f32_16x16x32_bf16 v[148:151], v[36:39], v[68:71], v[148:151]\n"
    "ds_read_b128 v[100:103], v29 offset:2048\n"
    "s_add_u32 m0, s64, 3072\n"
    "v_mfma_f32_16x16x32_bf16 v[152:155], v[40:43], v[68:71], v[152:155]\n"
    "ds_read_b128 v[104:107], v29 offset:4096\n"
    "buffer_load_dwordx4 v21, s[44:47], s61 offen lds\n"
    "v_mfma_f32_16x16x32_bf16 v[156:159], v[44:47], v[68:71], v[156:159]\n"
    "ds_read_b128 v[108:111], v29 offset:6144\n"
    "s_cmp_eq_u32 s65, 3\n"
    "v_mfma_f32_16x16x32_bf16 v[160:163], v[32:35], v[72:75], v[160:163]\n"
    "ds_read_b128 v[112:115], v29 offset:8192\n"
    "s_cselect_b32 s65, 0, s65\n"
    "v_mfma_f32_16x16x32_bf16 v[164:167], v[36:39], v[72:75], v[164:167]\n"
    "ds_read_b128 v[116:119], v29 offset:10240\n"
    "s_cselect_b32 s59, s68, 49152\n"
    "v_mfma_f32_16x16x32_bf16 v[168:171], v[40:43], v[72:75], v[168:171]\n"
    "ds_read_b128 v[120:123], v29 offset:12288\n"
    "s_add_u32 m0, s64, 8192\n"
    "v_mfma_f32_16x16x32_bf16 v[172:175], v[44:47], v[72:75], v[172:175]\n"
    "ds_read_b128 v[124:127], v29 offset:14336\n"
    "buffer_load_dwordx4 v22, s[48:51], s61 offen lds\n"
    "v_mfma_f32_16x16x32_bf16 v[176:179], v[32:35], v[76:79], v[176:179]\n"
    "v_add_u32 v26, s59, v26\n"
    "s_cmp_ge_u32 s67, s56\n"
    "v_mfma_f32_16x16x32_bf16 v[180:183], v[36:39], v[76:79], v[180:183]\n"
    "v_add_u32 v27, s59, v27\n"
    "s_cselect_b32 s42, 0, s42\n"
    "v_mfma_f32_16x16x32_bf16 v[184:187], v[40:43], v[76:79], v[184:187]\n"
    "v_add_u32 v28, s59, v28\n"
    "s_add_u32 s66, s66, 49152\n"
    "v_mfma_f32_16x16x32_bf16 v[188:191], v[44:47], v[76:79], v[188:191]\n"
    "v_add_u32 v29, s59, v29\n"
    "s_add_u32 m0, s64, 9216\n"
    "v_mfma_f32_16x16x32_bf16 v[192:195], v[32:35], v[80:83], v[192:195]\n"
    "buffer_load_dwordx4 v23, s[48:51], s61 offen lds\n"
    "v_mfma_f32_16x16x32_bf16 v[196:199], v[36:39], v[80:83], v[196:199]\n"
    "s_cmp_eq_u32 s66, 147456\n"
    "v_mfma_f32_16x16x32_bf16 v[200:203], v[40:43], v[80:83], v[200:203]\n"
    "s_cselect_b32 s66, 0, s66\n"
    "v_mfma_f32_16x16x32_bf16 v[204:207], v[44:47], v[80:83], v[204:207]\n"
    "s_add_u32 s63, s66, s54\n"
    "v_mfma_f32_16x16x32_bf16 v[208:211], v[32:35], v[84:87], v[208:211]\n"
    "s_add_u32 m0, s64, 10240\n"
    "v_mfma_f32_16x16x32_bf16 v[212:215], v[36:39], v[84:87], v[212:215]\n"
    "buffer_load_dwordx4 v24, s[48:51], s61 offen lds\n"
    "v_mfma_f32_16x16x32_bf16 v[216:219], v[40:43], v[84:87], v[216:219]\n"
    "v_mfma_f32_16x16x32_bf16 v[220:223], v[44:47], v[84:87], v[220:223]\n"
    "v_mfma_f32_16x16x32_bf16 v[224:227], v[32:35], v[88:91], v[224:227]\n"
    "v_mfma_f32_16x16x32_bf16 v[228:231], v[36:39], v[88:91], v[228:231]\n"
    "s_add_u32 m0, s64, 11264\n"
    "v_mfma_f32_16x16x32_bf16 v[232:235], v[40:43], v[88:91], v[232:235]\n"
    "buffer_load_dwordx4 v25, s[48:51], s61 offen lds\n"
    "v_mfma_f32_16x16x32_bf16 v[236:239], v[44:47], v[88:91], v[236:239]\n"
    "v_mfma_f32_16x16x32_bf16 v[240:243], v[32:35], v[92:95], v[240:243]\n"
    "v_mfma_f32_16x16x32_bf16 v[244:247], v[36:39], v[92:95], v[244:247]\n"
    "v_mfma_f32_16x16x32_bf16 v[248:251], v[40:43], v[92:95], v[248:251]\n"
    "v_mfma_f32_16x16x32_bf16 v[252:255], v[44:47], v[92:95], v[252:255]\n"
    "s_waitcnt vmcnt(12) lgkmcnt(0)\n"
    "s_barrier\n"
    "v_mfma_f32_16x16x32_bf16 v[128:131], v[48:51], v[96:99], v[128:131]\n"
    "ds_read_b128 v[32:35], v26 offset:0\n"
    "s_add_u32 m0, s63, 0\n"
    "v_mfma_f32_16x16x32_bf16 v[132:135], v[52:55], v[96:99], v[132:135]\n"
    "ds_read_b128 v[36:39], v26 offset:2048\n"
    "buffer_load_dwordx4 v14, s[40:43], s60 offen lds\n"
    "v_mfma_f32_16x16x32_bf16 v[136:139], v[56:59], v[96:99], v[136:139]\n"
    "ds_read_b128 v[40:43], v26 offset:4096\n"
    "s_add_u32 s61, s61, s58\n"
    "v_mfma_f32_16x16x32_bf16 v[140:143], v[60:63], v[96:99], v[140:143]\n"
    "ds_read_b128 v[44:47], v26 offset:6144\n"
    "s_add_u32 s64, s66, s55\n"
    "v_mfma_f32_16x16x32_bf16 v[144:147], v[48:51], v[100:103], v[144:147]\n"
    "ds_read_b128 v[64:67], v28 offset:0\n"
    "v_mfma_f32_16x16x32_bf16 v[148:151], v[52:55], v[100:103], v[148:151]\n"
    "ds_read_b128 v[68:71], v28 offset:2048\n"
    "s_add_u32 m0, s63, 1024\n"
    "v_mfma_f32_16x16x32_bf16 v[152:155], v[56:59], v[100:103], v[152:155]\n"
    "ds_read_b128 v[72:75], v28 offset:4096\n"
    "buffer_load_dwordx4 v15, s[40:43], s60 offen lds\n"
    "v_mfma_f32_16x16x32_bf16 v[156:159], v[60:63], v[100:103], v[156:159]\n"
    "ds_read_b128 v[76:79], v28 offset:6144\n"
    "s_cmp_eq_u32 s67, s57\n"
    "v_mfma_f32_16x16x32_bf16 v[160:163], v[48:51], v[104:107], v[160:163]\n"
    "ds_read_b128 v[80:83], v28 offset:8192\n"
    "s_cselect_b32 s61, 0, s61\n"
    "v_mfma_f32_16x16x32_bf16 v[164:167], v[52:55], v[104:107], v[164:167]\n"
    "ds_read_b128 v[84:87], v28 offset:10240\n"
    "s_cselect_b64 s[44:45], s[52:53], s[44:45]\n"
    "s_cselect_b64 s[48:49], s[52:53], s[48:49]\n"
    "v_mfma_f32_16x16x32_bf16 v[168:171], v[56:59], v[104:107], v[168:171]\n"
    "ds_read_b128 v[88:91], v28 offset:12288\n"
    "s_add_u32 m0, s63, 2048\n"
    "v_mfma_f32_16x16x32_bf16 v[172:175], v[60:63], v[104:107], v[172:175]\n"
    "ds_read_b128 v[92:95], v28 offset:14336\n"
    "buffer_load_dwordx4 v16, s[40:43], s60 offen lds\n"
    "v_mfma_f32_16x16x32_bf16 v[176:179], v[48:51], v[108:111], v[176:179]\n"
    "s_cmp_ge_u32 s67, s56\n"
    "v_mfma_f32_16x16x32_bf16 v[180:183], v[52:55], v[108:111], v[180:183]\n"
    "s_cselect_b32 s46, 0, s46\n"
    "v_mfma_f32_16x16x32_bf16 v[184:187], v[56:59], v[108:111], v[184:187]\n"
    "s_cselect_b32 s50, 0, s50\n"
    "v_mfma_f32_16x16x32_bf16 v[188:191], v[60:63], v[108:111], v[188:191]\n"
    "s_add_u32 m0, s63, 3072\n"
    "v_mfma_f32_16x16x32_bf16 v[192:195], v[48:51], v[112:115], v[192:195]\n"
    "buffer_load_dwordx4 v17, s[40:43], s60 offen lds\n"
    "v_mfma_f32_16x16x32_bf16 v[196:199], v[52:55], v[112:115], v[196:199]\n"
    "v_mfma_f32_16x16x32_bf16 v[200:203], v[56:59], v[112:115], v[200:203]\n"
    "v_mfma_f32_16x16x32_bf16 v[204:207], v[60:63], v[112:115], v[204:207]\n"
    "v_mfma_f32_16x16x32_bf16 v[208:211], v[48:51], v[116:119], v[208:211]\n"
    "s_add_u32 m0, s64, 0\n"
    "v_mfma_f32_16x16x32_bf16 v[212:215], v[52:55], v[116:119], v[212:215]\n"
    "buffer_load_dwordx4 v18, s[44:47], s61 offen lds\n"
    "v_mfma_f32_16x16x32_bf16 v[216:219], v[56:59], v[116:119], v[216:219]\n"
    "v_mfma_f32_16x16x32_bf16 v[220:223], v[60:63], v[116:119], v[220:223]\n"
    "v_mfma_f32_16x16x32_bf16 v[224:227], v[48:51], v[120:123], v[224:227]\n"
    "v_mfma_f32_16x16x32_bf16 v[228:231], v[52:55], v[120:123], v[228:231]\n"
    "s_add_u32 m0, s64, 1024\n"
    "v_mfma_f32_16x16x32_bf16 v[232:235], v[56:59], v[120:123], v[232:235]\n"
    "buffer_load_dwordx4 v19, s[44:47], s61 offen lds\n"
    "v_mfma_f32_16x16x32_bf16 v[236:239], v[60:63], v[120:123], v[236:239]\n"
    "s_sub_u32 s62, s62, 1\n"
    "v_mfma_f32_16x16x32_bf16 v[240:243], v[48:51], v[124:127], v[240:243]\n"
    "s_cmp_eq_u32 s62, 0\n"
    "v_mfma_f32_16x16x32_bf16 v[244:247], v[52:55], v[124:127], v[244:247]\n"
    "v_mfma_f32_16x16x32_bf16 v[248:251], v[56:59], v[124:127], v[248:251]\n"
    "v_mfma_f32_16x16x32_bf16 v[252:255], v[60:63], v[124:127], v[252:255]\n"
    "s_waitcnt lgkmcnt(0)\n"
    "s_cbranch_scc0 .LGNK_NT_loop%=\n"
    "s_waitcnt vmcnt(0)\n"
    "s_barrier\n"
    "s_nop 7\n"
    :
      "={v[128:143]}"(acc[0]), "={v[144:159]}"(acc[1]), "={v[160:175]}"(acc[2]), "={v[176:191]}"(acc[3]), "={v[192:207]}"(acc[4]),
      "={v[208:223]}"(acc[5]), "={v[224:239]}"(acc[6]), "={v[240:255]}"(acc[7]), "+{v[26:27]}"(ax), "+{v[28:31]}"(aw), "+{s[40:43]}"(rx),
      "+{s[44:47]}"(rw0), "+{s[48:51]}"(rw1)
    :
      "{v[14:17]}"(vox), "{v[18:21]}"(vow0), "{v[22:25]}"(vow1), "{s[52:53]}"(alt), "{s54}"(mx), "{s55}"(mw), "{s56}"(nt), "{s57}"(sw), "{s58}"(wstep)
    :
      "memory", "scc", "v32", "v33", "v34", "v35", "v36", "v37", "v38", "v39", "v40", "v41", "v42", "v43", "v44", "v45", "v46", "v47", "v48", "v49",
      "v50", "v51", "v52", "v53", "v54", "v55", "v56", "v57", "v58", "v59", "v60", "v61", "v62", "v63", "v64", "v65", "v66", "v67", "v68", "v69", "v70",
      "v71", "v72", "v73", "v74", "v75", "v76", "v77", "v78", "v79", "v80", "v81", "v82", "v83", "v84", "v85", "v86", "v87", "v88", "v89", "v90", "v91",
      "v92", "v93", "v94", "v95", "v96", "v97", "v98", "v99", "v100", "v101", "v102", "v103", "v104", "v105", "v106", "v107", "v108", "v109", "v110",
      "v111", "v112", "v113", "v114", "v115", "v116", "v117", "v118", "v119", "v120", "v121", "v122", "v123", "v124", "v125", "v126", "v127", "s59",
      "s60", "s61", "s62", "s63", "s64", "s65", "s66", "s67", "s68");
}

DI void mini_tr(GemmArgs& g, f16v (&acc)[4]) { GEMM_UNPACK
  asm volatile(
    "s_nop 4\n"
    "s_mov_b32 s60, 0\n"
    "s_mov_b32 s61, 0\n"
    "s_mov_b32 s67, 0\n"
    "s_mov_b32 s66, 0\n"
    "s_mov_b32 s63, s54\n"
    "s_mov_b32 s64, s55\n"
    "s_mov_b32 s68, 131071\n"
    ".LMT_pro%=:\n"
    "s_add_u32 m0, s63, 0\n"
    "s_nop 0\n"
    "buffer_load_dwordx4 v14, s[40:43], s60 offen lds\n"
    "s_add_u32 m0, s63, 1024\n"
    "s_nop 0\n"
    "buffer_load_dwordx4 v15, s[40:43], s60 offen lds\n"
    "s_add_u32 m0, s63, 2048\n"
    "s_nop 0\n"
    "buffer_load_dwordx4 v16, s[40:43], s60 offen lds\n"
    "s_add_u32 m0, s63, 3072\n"
    "s_nop 0\n"
    "buffer_load_dwordx4 v17, s[40:43], s60 offen lds\n"
    "s_add_u32 m0, s64, 0\n"
    "s_nop 0\n"
    "buffer_load_dwordx4 v18, s[44:47], s61 offen lds\n"
    "s_add_u32 m0, s64, 8192\n"
    "s_nop 0\n"
    "buffer_load_dwordx4 v19, s[44:47], s61 offen lds\n"
    "s_add_u32 m0, s64, 4096\n"
    "s_nop 0\n"
    "buffer_load_dwordx4 v22, s[44:47], s61 offen lds\n"
    "s_add_u32 m0, s64, 12288\n"
    "s_nop 0\n"
    "buffer_load_dwordx4 v23, s[44:47], s61 offen lds\n"
    "s_add_u32 s60, s60, 128\n"
    "s_add_u32 s61, s61, s58\n"
    "s_add_u32 s67, s67, 1\n"
    "s_cmp_ge_u32 s67, 16\n"
    "s_cselect_b32 s42, 0, s42\n"
    "s_cselect_b32 s46, 0, s46\n"
    "s_add_u32 s66, s66, 32768\n"
    "s_and_b32 s66, s66, 131071\n"
    "s_add_u32 s63, s66, s54\n"
    "s_add_u32 s64, s66, s55\n"
    "s_cmp_lt_u32 s67, 3\n"
    "s_cbranch_scc1 .LMT_pro%=\n"
    "v_mov_b64 v[128:129], 0\n"
    "v_mov_b64 v[130:131], 0\n"
    "v_mov_b64 v[132:133], 0\n"
    "v_mov_b64 v[134:135], 0\n"
    "v_mov_b64 v[136:137], 0\n"
    "v_mov_b64 v[138:139], 0\n"
    "v_mov_b64 v[140:141], 0\n"
    "v_mov_b64 v[142:143], 0\n"
    "v_mov_b64 v[144:145], 0\n"
    "v_mov_b64 v[146:147], 0\n"
    "v_mov_b64 v[148:149], 0\n"
    "v_mov_b64 v[150:151], 0\n"
    "v_mov_b64 v[152:153], 0\n"
    "v_mov_b64 v[154:155], 0\n"
    "v_mov_b64 v[156:157], 0\n"
    "v_mov_b64 v[158:159], 0\n"
    "v_mov_b64 v[160:161], 0\n"
    "v_mov_b64 v[162:163], 0\n"
    "v_mov_b64 v[164:165], 0\n"
    "v_mov_b64 v[166:167], 0\n"
    "v_mov_b64 v[168:169], 0\n"
    "v_mov_b64 v[170:171], 0\n"
    "v_mov_b64 v[172:173], 0\n"
    "v_mov_b64 v[174:175], 0\n"
    "v_mov_b64 v[176:177], 0\n"
    "v_mov_b64 v[178:179], 0\n"
    "v_mov_b64 v[180:181], 0\n"
    "v_mov_b64 v[182:183], 0\n"
    "v_mov_b64 v[184:185], 0\n"
    "v_mov_b64 v[186:187], 0\n"
    "v_mov_b64 v[188:189], 0\n"
    "v_mov_b64 v[190:191], 0\n"
    ".LMT_loop%=:\n"
    "s_waitcnt vmcnt(16)\n"
    "s_barrier\n"
    "ds_read_b128 v[32:35], v26 offset:0\n"
    "ds_read_b128 v[36:39], v26 offset:2048\n"
    "ds_read_b64_tr_b16 v[64:65], v28 offset:0\n"
    "ds_read_b64_tr_b16 v[66:67], v28 offset:512\n"
    "ds_read_b64_tr_b16 v[68:69], v29 offset:0\n"
    "ds_read_b64_tr_b16 v[70:71], v29 offset:512\n"
    "ds_read_b64_tr_b16 v[72:73], v30 offset:0\n"
    "ds_read_b64_tr_b16 v[74:75], v30 offset:512\n"
    "ds_read_b64_tr_b16 v[76:77], v31 offset:0\n"
    "ds_read_b64_tr_b16 v[78:79], v31 offset:512\n"
    "ds_read_b64_tr_b16 v[80:81], v28 offset:8192\n"
    "ds_read_b64_tr_b16 v[82:83], v28 offset:8704\n"
    "ds_read_b64_tr_b16 v[84:85], v29 offset:8192\n"
    "ds_read_b64_tr_b16 v[86:87], v29 offset:8704\n"
    "ds_read_b64_tr_b16 v[88:89], v30 offset:8192\n"
    "ds_read_b64_tr_b16 v[90:91], v30 offset:8704\n"
    "ds_read_b64_tr_b16 v[92:93], v31 offset:8192\n"
    "ds_read_b64_tr_b16 v[94:95], v31 offset:8704\n"
    "ds_read_b128 v[48:51], v27 offset:0\n"
    "ds_read_b128 v[52:55], v27 offset:2048\n"
    "s_waitcnt lgkmcnt(2)\n"
    "v_mfma_f32_16x16x32_bf16 v[128:131], v[64:67], v[32:35], v[128:131]\n"
    "ds_read_b64_tr_b16 v[96:97], v28 offset:4096\n"
    "s_add_u32 m0, s63, 0\n"
    "v_mfma_f32_16x16x32_bf16 v[132:135], v[64:67], v[36:39], v[132:135]\n"
    "ds_read_b64_tr_b16 v[98:99], v28 offset:4608\n"
    "buffer_load_dwordx4 v14, s[40:43], s60 offen lds\n"
    "v_mfma_f32_16x16x32_bf16 v[136:139], v[68:71], v[32:35], v[136:139]\n"
    "ds_read_b64_tr_b16 v[100:101], v29 offset:4096\n"
    "v_mfma_f32_16x16x32_bf16 v[140:143], v[68:71], v[36:39], v[140:143]\n"
    "ds_read_b64_tr_b16 v[102:103], v29 offset:4608\n"
    "v_mfma_f32_16x16x32_bf16 v[144:147], v[72:75], v[32:35], v[144:147]\n"
    "ds_read_b64_tr_b16 v[104:105], v30 offset:4096\n"
    "s_add_u32 m0, s63, 1024\n"
    "v_mfma_f32_16x16x32_bf16 v[148:151], v[72:75], v[36:39], v[148:151]\n"
    "ds_read_b64_tr_b16 v[106:107], v30 offset:4608\n"
    "buffer_load_dwordx4 v15, s[40:43], s60 offen lds\n"
    "v_mfma_f32_16x16x32_bf16 v[152:155], v[76:79], v[32:35], v[152:155]\n"
    "ds_read_b64_tr_b16 v[108:109], v31 offset:4096\n"
    "v_mfma_f32_16x16x32_bf16 v[156:159], v[76:79], v[36:39], v[156:159]\n"
    "ds_read_b64_tr_b16 v[110:111], v31 offset:4608\n"
    "v_mfma_f32_16x16x32_bf16 v[160:163], v[80:83], v[32:35], v[160:163]\n"
    "ds_read_b64_tr_b16 v[112:113], v28 offset:12288\n"
    "s_add_u32 m0, s63, 2048\n"
    "v_mfma_f32_16x16x32_bf16 v[164:167], v[80:83], v[36:39], v[164:167]\n"
    "ds_read_b64_tr_b16 v[114:115], v28 offset:12800\n"
    "buffer_load_dwordx4 v16, s[40:43], s60 offen lds\n"
    "v_mfma_f32_16x16x32_bf16 v[168:171], v[84:87], v[32:35], v[168:171]\n"
    "ds_read_b64_tr_b16 v[116:117], v29 offset:12288\n"
    "v_mfma_f32_16x16x32_bf16 v[172:175], v[84:87], v[36:39], v[172:175]\n"
    "ds_read_b64_tr_b16 v[118:119], v29 offset:12800\n"
    "v_mfma_f32_16x16x32_bf16 v[176:179], v[88:91], v[32:35], v[176:179]\n"
    "ds_read_b64_tr_b16 v[120:121], v30 offset:12288\n"
    "s_add_u32 m0, s63, 3072\n"
    "v_mfma_f32_16x16x32_bf16 v[180:183], v[88:91], v[36:39], v[180:183]\n"
    "ds_read_b64_tr_b16 v[122:123], v30 offset:12800\n"
    "buffer_load_dwordx4 v17, s[40:43], s60 offen lds\n"
    "v_mfma_f32_16x16x32_bf16 v[184:187], v[92:95], v[32:35], v[184:187]\n"
    "ds_read_b64_tr_b16 v[124:125], v31 offset:12288\n"
    "v_mfma_f32_16x16x32_bf16 v[188:191], v[92:95], v[36:39], v[188:191]\n"
    "ds_read_b64_tr_b16 v[126:127], v31 offset:12800\n"
    "s_waitcnt lgkmcnt(0)\n"
    "v_mfma_f32_16x16x32_bf16 v[128:131], v[96:99], v[48:51], v[128:131]\n"
    "v_add_u32 v26, 32768, v26\n"
    "s_add_u32 m0, s64, 0\n"
    "v_mfma_f32_16x16x32_bf16 v[132:135], v[96:99], v[52:55], v[132:135]\n"
    "v_and_b32 v26, s68, v26\n"
    "buffer_load_dwordx4 v18, s[44:47], s61 offen lds\n"
    "v_mfma_f32_16x16x32_bf16 v[136:139], v[100:103], v[48:51], v[136:139]\n"
    "v_add_u32 v27, 32768, v27\n"
    "v_mfma_f32_16x16x32_bf16 v[140:143], v[100:103], v[52:55], v[140:143]\n"
    "v_and_b32 v27, s68, v27\n"
    "v_mfma_f32_16x16x32_bf16 v[144:147], v[104:107], v[48:51], v[144:147]\n"
    "v_add_u32 v28, 32768, v28\n"
    "s_add_u32 m0, s64, 8192\n"
    "v_mfma_f32_16x16x32_bf16 v[148:151], v[104:107], v[52:55], v[148:151]\n"
    "v_and_b32 v28, s68, v28\n"
    "buffer_load_dwordx4 v19, s[44:47], s61 offen lds\n"
    "v_mfma_f32_16x16x32_bf16 v[152:155], v[108:111], v[48:51], v[152:155]\n"
    "v_add_u32 v29, 32768, v29\n"
    "v_mfma_f32_16x16x32_bf16 v[156:159], v[108:111], v[52:55], v[156:159]\n"
    "v_and_b32 v29, s68, v29\n"
    "v_mfma_f32_16x16x32_bf16 v[160:163], v[112:115], v[48:51], v[160:163]\n"
    "v_add_u32 v30, 32768, v30\n"
    "s_add_u32 m0, s64, 4096\n"
    "v_mfma_f32_16x16x32_bf16 v[164:167], v[112:115], v[52:55], v[164:167]\n"
    "v_and_b32 v30, s68, v30\n"
    "buffer_load_dwordx4 v22, s[44:47], s61 offen lds\n"
    "v_mfma_f32_16x16x32_bf16 v[168:171], v[116:119], v[48:51], v[168:171]\n"
    "v_add_u32 v31, 32768, v31\n"
    "v_mfma_f32_16x16x32_bf16 v[172:175], v[116:119], v[52:55], v[172:175]\n"
    "v_and_b32 v31, s68, v31\n"
    "v_mfma_f32_16x16x32_bf16 v[176:179], v[120:123], v[48:51], v[176:179]\n"
    "s_add_u32 m0, s64, 12288\n"
    "v_mfma_f32_16x16x32_bf16 v[180:183], v[120:123], v[52:55], v[180:183]\n"
    "buffer_load_dwordx4 v23, s[44:47], s61 offen lds\n"
    "v_mfma_f32_16x16x32_bf16 v[184:187], v[124:127], v[48:51], v[184:187]\n"
    "v_mfma_f32_16x16x32_bf16 v[188:191], v[124:127], v[52:55], v[188:191]\n"
    "s_add_u32 s60, s60, 128\n"
    "s_add_u32 s61, s61, s58\n"
    "s_add_u32 s67, s67, 1\n"
    "s_cmp_ge_u32 s67, 16\n"
    "s_cselect_b32 s42, 0, s42\n"
    "s_cselect_b32 s46, 0, s46\n"
    "s_add_u32 s66, s66, 32768\n"
    "s_and_b32 s66, s66, 131071\n"
    "s_add_u32 s63, s66, s54\n"
    "s_add_u32 s64, s66, s55\n"
    "s_cmp_lt_u32 s67, 19\n"
    "s_cbranch_scc1 .LMT_loop%=\n"
    "s_waitcnt vmcnt(0)\n"
    "s_barrier\n"
    "s_nop 7\n"
    :
      "={v[128:143]}"(acc[0]), "={v[144:159]}"(acc[1]), "={v[160:175]}"(acc[2]), "={v[176:191]}"(acc[3]), "+{v[26:27]}"(ax), "+{v[28:31]}"(aw), "+{s[40:43]}"(rx),
      "+{s[44:47]}"(rw0), "+{s[48:51]}"(rw1)
    :
      "{v[14:17]}"(vox), "{v[18:21]}"(vow0), "{v[22:25]}"(vow1), "{s[52:53]}"(alt), "{s54}"(mx), "{s55}"(mw), "{s56}"(nt), "{s57}"(sw), "{s58}"(wstep)
    :
      "memory", "scc", "v32", "v33", "v34", "v35", "v36", "v37", "v38", "v39", "v40", "v41", "v42", "v43", "v44", "v45", "v46", "v47", "v48", "v49",
      "v50", "v51", "v52", "v53", "v54", "v55", "v56", "v57", "v58", "v59", "v60", "v61", "v62", "v63", "v64", "v65", "v66", "v67", "v68", "v69", "v70",
      "v71", "v72", "v73", "v74", "v75", "v76", "v77", "v78", "v79", "v80", "v81", "v82", "v83", "v84", "v85", "v86", "v87", "v88", "v89", "v90", "v91",
      "v92", "v93", "v94", "v95", "v96", "v97", "v98", "v99", "v100", "v101", "v102", "v103", "v104", "v105", "v106", "v107", "v108", "v109", "v110",
      "v111", "v112", "v113", "v114", "v115", "v116", "v117", "v118", "v119", "v120", "v121", "v122", "v123", "v124", "v125", "v126", "v127", "s59",
      "s60", "s61", "s62", "s63", "s64", "s65", "s66", "s67", "s68");
}
DI void mini_nt(GemmArgs& g, f16v (&acc)[4]) { GEMM_UNPACK
  asm volatile(
    "s_nop 4\n"
    "s_mov_b32 s60, 0\n"
    "s_mov_b32 s61, 0\n"
    "s_mov_b32 s67, 0\n"
    "s_mov_b32 s66, 0\n"
    "s_mov_b32 s63, s54\n"
    "s_mov_b32 s64, s55\n"
    "s_mov_b32 s68, 131071\n"
    ".LMN_pro%=:\n"
    "s_add_u32 m0, s63, 0\n"
    "s_nop 0\n"
    "buffer_load_dwordx4 v14, s[40:43], s60 offen lds\n"
    "s_add_u32 m0, s63, 1024\n"
    "s_nop 0\n"
    "buffer_load_dwordx4 v15, s[40:43], s60 offen lds\n"
    "s_add_u32 m0, s63, 2048\n"
    "s_nop 0\n"
    "buffer_load_dwordx4 v16, s[40:43], s60 offen lds\n"
    "s_add_u32 m0, s63, 3072\n"
    "s_nop 0\n"
    "buffer_load_dwordx4 v17, s[40:43], s60 offen lds\n"
    "s_add_u32 m0, s64, 0\n"
    "s_nop 0\n"
    "buffer_load_dwordx4 v18, s[44:47], s61 offen lds\n"
    "s_add_u32 m0, s64, 8192\n"
    "s_nop 0\n"
    "buffer_load_dwordx4 v19, s[44:47], s61 offen lds\n"
    "s_add_u32 m0, s64, 4096\n"
    "s_nop 0\n"
    "buffer_load_dwordx4 v22, s[44:47], s61 offen lds\n"
    "s_add_u32 m0, s64, 12288\n"
    "s_nop 0\n"
    "buffer_load_dwordx4 v23, s[44:47], s61 offen lds\n"
    "s_add_u32 s60, s60, 128\n"
    "s_add_u32 s61, s61, s58\n"
    "s_add_u32 s67, s67, 1\n"
    "s_cmp_ge_u32 s67, 16\n"
    "s_cselect_b32 s42, 0, s42\n"
    "s_cselect_b32 s46, 0, s46\n"
    "s_add_u32 s66, s66, 32768\n"
    "s_and_b32 s66, s66, 131071\n"
    "s_add_u32 s63, s66, s54\n"
    "s_add_u32 s64, s66, s55\n"
    "s_cmp_lt_u32 s67, 3\n"
    "s_cbranch_scc1 .LMN_pro%=\n"
    "v_mov_b64 v[128:129], 0\n"
    "v_mov_b64 v[130:131], 0\n"
    "v_mov_b64 v[132:133], 0\n"
    "v_mov_b64 v[134:135], 0\n"
    "v_mov_b64 v[136:137], 0\n"
    "v_mov_b64 v[138:139], 0\n"
    "v_mov_b64 v[140:141], 0\n"
    "v_mov_b64 v[142:143], 0\n"
    "v_mov_b64 v[144:145], 0\n"
    "v_mov_b64 v[146:147], 0\n"
    "v_mov_b64 v[148:149], 0\n"
    "v_mov_b64 v[150:151], 0\n"
    "v_mov_b64 v[152:153], 0\n"
    "v_mov_b64 v[154:155], 0\n"
    "v_mov_b64 v[156:157], 0\n"
    "v_mov_b64 v[158:159], 0\n"
    "v_mov_b64 v[160:161], 0\n"
    "v_mov_b64 v[162:163], 0\n"
    "v_mov_b64 v[164:165], 0\n"
    "v_mov_b64 v[166:167], 0\n"
    "v_mov_b64 v[168:169], 0\n"
    "v_mov_b64 v[170:171], 0\n"
    "v_mov_b64 v[172:173], 0\n"
    "v_mov_b64 v[174:175], 0\n"
    "v_mov_b64 v[176:177], 0\n"
    "v_mov_b64 v[178:179], 0\n"
    "v_mov_b64 v[180:181], 0\n"
    "v_mov_b64 v[182:183], 0\n"
    "v_mov_b64 v[184:185], 0\n"
    "v_mov_b64 v[186:187], 0\n"
    "v_mov_b64 v[188:189], 0\n"
    "v_mov_b64 v[190:191], 0\n"
    ".LMN_loop%=:\n"
    "s_waitcnt vmcnt(16)\n"
    "s_barrier\n"
    "ds_read_b128 v[32:35], v26 offset:0\n"
    "ds_read_b128 v[36:39], v26 offset:2048\n"
    "ds_read_b64_tr_b16 v[64:65], v28 offset:0\n"
    "ds_read_b64_tr_b16 v[66:67], v28 offset:512\n"
    "ds_read_b64_tr_b16 v[68:69], v29 offset:0\n"
    "ds_read_b64_tr_b16 v[70:71], v29 offset:512\n"
    "ds_read_b64_tr_b16 v[72:73], v30 offset:0\n"
    "ds_read_b64_tr_b16 v[74:75], v30 offset:512\n"
    "ds_read_b64_tr_b16 v[76:77], v31 offset:0\n"
    "ds_read_b64_tr_b16 v[78:79], v31 offset:512\n"
    "ds_read_b64_tr_b16 v[80:81], v28 offset:8192\n"
    "ds_read_b64_tr_b16 v[82:83], v28 offset:8704\n"
    "ds_read_b64_tr_b16 v[84:85], v29 offset:8192\n"
    "ds_read_b64_tr_b16 v[86:87], v29 offset:8704\n"
    "ds_read_b64_tr_b16 v[88:89], v30 offset:8192\n"
    "ds_read_b64_tr_b16 v[90:91], v30 offset:8704\n"
    "ds_read_b64_tr_b16 v[92:93], v31 offset:8192\n"
    "ds_read_b64_tr_b16 v[94:95], v31 offset:8704\n"
    "ds_read_b128 v[48:51], v27 offset:0\n"
    "ds_read_b128 v[52:55], v27 offset:2048\n"
    "s_waitcnt lgkmcnt(2)\n"
    "v_mfma_f32_16x16x32_bf16 v[128:131], v[32:35], v[64:67], v[128:131]\n"
    "ds_read_b64_tr_b16 v[96:97], v28 offset:4096\n"
    "s_add_u32 m0, s63, 0\n"
    "v_mfma_f32_16x16x32_bf16 v[132:135], v[36:39], v[64:67], v[132:135]\n"
    "ds_read_b64_tr_b16 v[98:99], v28 offset:4608\n"
    "buffer_load_dwordx4 v14, s[40:43], s60 offen lds\n"
    "v_mfma_f32_16x16x32_bf16 v[136:139], v[32:35], v[68:71], v[136:139]\n"
    "ds_read_b64_tr_b16 v[100:101], v29 offset:4096\n"
    "v_mfma_f32_16x16x32_bf16 v[140:143], v[36:39], v[68:71], v[140:143]\n"
    "ds_read_b64_tr_b16 v[102:103], v29 offset:4608\n"
    "v_mfma_f32_16x16x32_bf16 v[144:147], v[32:35], v[72:75], v[144:147]\n"
    "ds_read_b64_tr_b16 v[104:105], v30 offset:4096\n"
    "s_add_u32 m0, s63, 1024\n"
    "v_mfma_f32_16x16x32_bf16 v[148:151], v[36:39], v[72:75], v[148:151]\n"
    "ds_read_b64_tr_b16 v[106:107], v30 offset:4608\n"
    "buffer_load_dwordx4 v15, s[40:43], s60 offen lds\n"
    "v_mfma_f32_16x16x32_bf16 v[152:155], v[32:35], v[76:79], v[152:155]\n"
    "ds_read_b64_tr_b16 v[108:109], v31 offset:4096\n"
    "v_mfma_f32_16x16x32_bf16 v[156:159], v[36:39], v[76:79], v[156:159]\n"
    "ds_read_b64_tr_b16 v[110:111], v31 offset:4608\n"
    "v_mfma_f32_16x16x32_bf16 v[160:163], v[32:35], v[80:83], v[160:163]\n"
    "ds_read_b64_tr_b16 v[112:113], v28 offset:12288\n"
    "s_add_u32 m0, s63, 2048\n"
    "v_mfma_f32_16x16x32_bf16 v[164:167], v[36:39], v[80:83], v[164:167]\n"
    "ds_read_b64_tr_b16 v[114:115], v28 offset:12800\n"
    "buffer_load_dwordx4 v16, s[40:43], s60 offen lds\n"
    "v_mfma_f32_16x16x32_bf16 v[168:171], v[32:35], v[84:87], v[168:171]\n"
    "ds_read_b64_tr_b16 v[116:117], v29 offset:12288\n"
    "v_mfma_f32_16x16x32_bf16 v[172:175], v[36:39], v[84:87], v[172:175]\n"
    "ds_read_b64_tr_b16 v[118:119], v29 offset:12800\n"
    "v_mfma_f32_16x16x32_bf16 v[176:179], v[32:35], v[88:91], v[176:179]\n"
    "ds_read_b64_tr_b16 v[120:121], v30 offset:12288\n"
    "s_add_u32 m0, s63, 3072\n"
    "v_mfma_f32_16x16x32_bf16 v[180:183], v[36:39], v[88:91], v[180:183]\n"
    "ds_read_b64_tr_b16 v[122:123], v30 offset:12800\n"
    "buffer_load_dwordx4 v17, s[40:43], s60 offen lds\n"
    "v_mfma_f32_16x16x32_bf16 v[184:187], v[32:35], v[92:95], v[184:187]\n"
    "ds_read_b64_tr_b16 v[124:125], v31 offset:12288\n"
    "v_mfma_f32_16x16x32_bf16 v[188:191], v[36:39], v[92:95], v[188:191]\n"
    "ds_read_b64_tr_b16 v[126:127], v31 offset:12800\n"
    "s_waitcnt lgkmcnt(0)\n"
    "v_mfma_f32_16x16x32_bf16 v[128:131], v[48:51], v[96:99], v[128:131]\n"
    "v_add_u32 v26, 32768, v26\n"
    "s_add_u32 m0, s64, 0\n"
    "v_mfma_f32_16x16x32_bf16 v[132:135], v[52:55], v[96:99], v[132:135]\n"
    "v_and_b32 v26, s68, v26\n"
    "buffer_load_dwordx4 v18, s[44:47], s61 offen lds\n"
    "v_mfma_f32_16x16x32_bf16 v[136:139], v[48:51], v[100:103], v[136:139]\n"
    "v_add_u32 v27, 32768, v27\n"
    "v_mfma_f32_16x16x32_bf16 v[140:143], v[52:55], v[100:103], v[140:143]\n"
    "v_and_b32 v27, s68, v27\n"
    "v_mfma_f32_16x16x32_bf16 v[144:147], v[48:51], v[104:107], v[144:147]\n"
    "v_add_u32 v28, 32768, v28\n"
    "s_add_u32 m0, s64, 8192\n"
    "v_mfma_f32_16x16x32_bf16 v[148:151], v[52:55], v[104:107], v[148:151]\n"
    "v_and_b32 v28, s68, v28\n"
    "buffer_load_dwordx4 v19, s[44:47], s61 offen lds\n"
    "v_mfma_f32_16x16x32_bf16 v[152:155], v[48:51], v[108:111], v[152:155]\n"
    "v_add_u32 v29, 32768, v29\n"
    "v_mfma_f32_16x16x32_bf16 v[156:159], v[52:55], v[108:111], v[156:159]\n"
    "v_and_b32 v29, s68, v29\n"
    "v_mfma_f32_16x16x32_bf16 v[160:163], v[48:51], v[112:115], v[160:163]\n"
    "v_add_u32 v30, 32768, v30\n"
    "s_add_u32 m0, s64, 4096\n"
    "v_mfma_f32_16x16x32_bf16 v[164:167], v[52:55], v[112:115], v[164:167]\n"
    "v_and_b32 v30, s68, v30\n"
    "buffer_load_dwordx4 v22, s[44:47], s61 offen lds\n"
    "v_mfma_f32_16x16x32_bf16 v[168:171], v[48:51], v[116:119], v[168:171]\n"
    "v_add_u32 v31, 32768, v31\n"
    "v_mfma_f32_16x16x32_bf16 v[172:175], v[52:55], v[116:119], v[172:175]\n"
    "v_and_b32 v31, s68, v31\n"
    "v_mfma_f32_16x16x32_bf16 v[176:179], v[48:51], v[120:123], v[176:179]\n"
    "s_add_u32 m0, s64, 12288\n"
    "v_mfma_f32_16x16x32_bf16 v[180:183], v[52:55], v[120:123], v[180:183]\n"
    "buffer_load_dwordx4 v23, s[44:47], s61 offen lds\n"
    "v_mfma_f32_16x16x32_bf16 v[184:187], v[48:51], v[124:127], v[184:187]\n"
    "v_mfma_f32_16x16x32_bf16 v[188:191], v[52:55], v[124:127], v[188:191]\n"
    "s_add_u32 s60, s60, 128\n"
    "s_add_u32 s61, s61, s58\n"
    "s_add_u32 s67, s67, 1\n"
    "s_cmp_ge_u32 s67, 16\n"
    "s_cselect_b32 s42, 0, s42\n"
    "s_cselect_b32 s46, 0, s46\n"
    "s_add_u32 s66, s66, 32768\n"
    "s_and_b32 s66, s66, 131071\n"
    "s_add_u32 s63, s66, s54\n"
    "s_add_u32 s64, s66, s55\n"
    "s_cmp_lt_u32 s67, 19\n"
    "s_cbranch_scc1 .LMN_loop%=\n"
    "s_waitcnt vmcnt(0)\n"
    "s_barrier\n"
    "s_nop 7\n"
    :
      "={v[128:143]}"(acc[0]), "={v[144:159]}"(acc[1]), "={v[160:175]}"(acc[2]), "={v[176:191]}"(acc[3]), "+{v[26:27]}"(ax), "+{v[28:31]}"(aw), "+{s[40:43]}"(rx),
      "+{s[44:47]}"(rw0), "+{s[48:51]}"(rw1)
    :
      "{v[14:17]}"(vox), "{v[18:21]}"(vow0), "{v[22:25]}"(vow1), "{s[52:53]}"(alt), "{s54}"(mx), "{s55}"(mw), "{s56}"(nt), "{s57}"(sw), "{s58}"(wstep)
    :
      "memory", "scc", "v32", "v33", "v34", "v35", "v36", "v37", "v38", "v39", "v40", "v41", "v42", "v43", "v44", "v45", "v46", "v47", "v48", "v49",
      "v50", "v51", "v52", "v53", "v54", "v55", "v56", "v57", "v58", "v59", "v60", "v61", "v62", "v63", "v64", "v65", "v66", "v67", "v68", "v69", "v70",
      "v71", "v72", "v73", "v74", "v75", "v76", "v77", "v78", "v79", "v80", "v81", "v82", "v83", "v84", "v85", "v86", "v87", "v88", "v89", "v90", "v91",
      "v92", "v93", "v94", "v95", "v96", "v97", "v98", "v99", "v100", "v101", "v102", "v103", "v104", "v105", "v106", "v107", "v108", "v109", "v110",
      "v111", "v112", "v113", "v114", "v115", "v116", "v117", "v118", "v119", "v120", "v121", "v122", "v123", "v124", "v125", "v126", "v127", "s59",
      "s60", "s61", "s62", "s63", "s64", "s65", "s66", "s67", "s68");
}

template <int PERM> DI void setup_x(GemmArgs& g, const void* X, u32 bytes, u32 pitch, u32 row0, int w, int lane) {
  const u32 r = lane >> 3, ch = (lane & 7) ^ r, c = lane & 15, gq = lane >> 4;
#pragma unroll
  for (int i = 0; i < 4; ++i) {
    u32 rho = (4 * w + i) * 8 + r;
    if (PERM == 1) rho = (rho & 64) + 16 * ((rho >> 2) & 3) + 4 * ((rho >> 4) & 3) + (rho & 3);
    if (PERM == 2) rho = (rho & 31) + ((rho >> 1) & 32) + ((rho << 1) & 64);
    g.vox[i] = (row0 + rho) * pitch + ch * 16;
  }
  g.rx = mkrv(X, bytes); g.mx = w * 4096;
  const u32 base = ((w >> 1) * 64 + c) * 128;
  g.ax = (v2u){base + ((gq ^ (c & 7)) * 16), base + (((4 + gq) ^ (c & 7)) * 16)};
}
DI void setup_kn(GemmArgs& g, const void* W, u32 col0, int w, int lane) {
  const u32 r = lane >> 3, ch = lane & 7, c = lane & 15, gq = lane >> 4;
  const u32 u = (ch >> 1) ^ (((r >> 1) & 1) | ((w & 1) << 1));
#pragma unroll
  for (int i = 0; i < 4; ++i) {
    const u32 o = (8 * w + r) * 8192 + (col0 + 64 * i) * 2 + u * 32 + (ch & 1) * 16;
    g.vow0[i] = o; g.vow1[i] = o + 32 * 8192;
  }
  g.rw0 = g.rw1 = mkrv(W, 8u << 20); g.alt = g.rw0.xy; g.mw = 16384 + w * 1024; g.nt = 16; g.sw = 1000; g.wstep = 64 * 8192;
  const u32 k = 8 * gq + (c >> 2), x = ((k >> 1) & 1) | (((k >> 3) & 1) << 1);
#pragma unroll
  for (int i = 0; i < 4; ++i) g.aw[i] = 16384 + (w & 1) * 16384 + k * 128 + ((i ^ x) * 32) + (c & 3) * 8;
}
template <bool SCANPERM> DI void setup_nk(GemmArgs& g, const void* W0, const void* W1, u32 n0, int w, int lane) {
  const u32 r = lane >> 3, ch = (lane & 7) ^ r, c = lane & 15, gq = lane >> 4;
#pragma unroll
  for (int i = 0; i < 4; ++i) {
    const u32 rho = (4 * (w & 1) + i) * 8 + r;
    if (SCANPERM) g.vow0[i] = g.vow1[i] = (n0 + 64 * (w >> 1) + 4 * (rho & 15) + (rho >> 4)) * 2048 + ch * 16;
    else { g.vow0[i] = (n0 + 128 * (w >> 1) + 4 * (rho & 15) + (rho >> 4)) * 2048 + ch * 16; g.vow1[i] = g.vow0[i] + 64 * 2048; }
  }
  g.rw0 = mkrv(W0, 2u << 20); g.rw1 = mkrv(SCANPERM ? W1 : W0, 2u << 20); g.alt = mkrv(W1, 0).xy;
  g.mw = 16384 + (w >> 1) * 16384 + (w & 1) * 4096; g.nt = SCANPERM ? 16 : 32; g.sw = SCANPERM ? 1000 : 16; g.wstep = 128;
  const u32 base = 16384 + ((w & 1) * 128 + c) * 128;
  g.aw = (v4u){base + ((gq ^ (c & 7)) * 16), base + (((4 + gq) ^ (c & 7)) * 16), 0u, 0u};
}
DI void tile_done(gu32* ctl, u32 idx, bool add, bool first) {
  gu32* word = ctl + idx;
  if (first) wait_init(ctl, threadIdx.x, true);
  WAITV(0);
  BARRIER();
  if (threadIdx.x == 0) { if (add) __hip_atomic_fetch_add(word, 1u, RLX_AGENT); else __hip_atomic_store(word, 2u, RLX_AGENT); }
}

DI void task_K(const CAS Params& p, int rt, int ct, int w, int lane, bool first) {
  GemmArgs g; f16v acc[8];
  setup_x<0>(g, p.xB, 4u << 20, 2048, rt * 128, w, lane); setup_kn(g, p.Wk, ct * 256, w, lane);
  gemm_kn_tr(g, acc);
  asm volatile("" : "+s"(rt), "+s"(ct), "+s"(w), "+v"(lane));
  GST(1)
  const rsrc_t rx = mkrs(p.wb, WS_END);
  const u32 so = OFF_KF + (rt * 4 + (w >> 1) * 2) * 262144 + (ct * 2 + (w & 1)) * 8192;
#pragma unroll
  for (int m = 0; m < 4; ++m)
#pragma unroll
    for (int j = 0; j < 4; ++j) stx4(pk8(acc[2 * j], acc[2 * j + 1], m), rx, lane * 16 + j * 1024, so + (m >> 1) * 262144 + (m & 1) * 4096);
  tile_done((gu32*)(p.wb + OFF_CTL), C_KVF + 16 * (32 * rt + ct), false, first && w == 0);
}
DI void task_V(const CAS Params& p, int rt, int ct, int w, int lane, bool first) {
  GemmArgs g; f16v acc[8];
  setup_x<0>(g, p.xB, 4u << 20, 2048, rt * 128, w, lane); setup_kn(g, p.Wv, ct * 256, w, lane);
  gemm_kn_nt(g, acc);
  asm volatile("" : "+s"(rt), "+s"(ct), "+s"(w), "+v"(lane));
  GST(2)
  const rsrc_t rx = mkrs(p.wb, WS_END);
  const u32 so = OFF_VF + (rt * 4 + (w >> 1) * 2) * 262144 + (ct * 2 + (w & 1)) * 8192;
#pragma unroll
  for (int sg = 0; sg < 2; ++sg)
#pragma unroll
    for (int n = 0; n < 8; ++n) {
      const f16v& a = acc[n];
      stx4((v4u){pk(a[8 * sg], a[8 * sg + 1]), pk(a[8 * sg + 2], a[8 * sg + 3]), pk(a[8 * sg + 4], a[8 * sg + 5]), pk(a[8 * sg + 6], a[8 * sg + 7])}, rx,
           lane * 16 + (n & 3) * 1024, so + sg * 262144 + (n >> 2) * 4096);
    }
  tile_done((gu32*)(p.wb + OFF_CTL), C_KVF + 16 * (32 * rt + 16 + ct), false, first && w == 0);
}
DI void task_M(const CAS Params& p, int rt, int c64, int w, int lane, bool first) {
  const int isv = c64 & 1, h = c64 >> 1;
  GemmArgs g; f16v acc[4];
  setup_x<0>(g, p.xB, 4u << 20, 2048, rt * 128, w, lane); setup_kn(g, isv ? p.Wv : p.Wk, h * 128, w, lane);
  g.ax += (u32)(w * 32 - (w >> 1) * 64) * 128; g.aw -= (u32)(w & 1) * 16384;
  const rsrc_t rx = mkrs(p.wb, WS_END);
  const u32 so = (rt * 4 + w) * 262144 + h * 8192;
  if (isv) {
    mini_nt(g, acc);
#pragma unroll
    for (int n = 0; n < 8; ++n) { const f16v& a = acc[n >> 1]; const int o = 8 * (n & 1);
      stx4((v4u){pk(a[o], a[o + 1]), pk(a[o + 2], a[o + 3]), pk(a[o + 4], a[o + 5]), pk(a[o + 6], a[o + 7])}, rx, lane * 16 + n * 1024, OFF_VF + so); }
  } else {
    mini_tr(g, acc);
#pragma unroll
    for (int j = 0; j < 4; ++j)
#pragma unroll
      for (int m = 0; m < 2; ++m) { const f16v& a = acc[j]; const int o = 4 * m;
        stx4((v4u){pk(a[o], a[o + 1]), pk(a[o + 2], a[o + 3]), pk(a[o + 8], a[o + 9]), pk(a[o + 10], a[o + 11])}, rx, lane * 16 + j * 1024, OFF_KF + so + m * 4096); }
  }
  tile_done((gu32*)(p.wb + OFF_CTL), C_KVF + 16 * (32 * rt + 16 * isv + (h >> 1)), true, first && w == 0);
}
DI void task_Q(const CAS Params& p, int rt, int ct, int w, int lane) {
  wait_lanes((gu32*)(p.wb + OFF_CTL), C_KVF + 16 * (32 * rt + 16 * (lane & 1) + ct), 2u);
  GemmArgs g;
  setup_x<2>(g, p.xB, 4u << 20, 2048, rt * 128, w, lane); setup_kn(g, p.Wq, ct * 256, w, lane);
  const u32 c = lane & 15, gq = lane >> 4, h = ct * 2 + (w & 1), seg0 = rt * 4 + (w >> 1);
  const v4u av0 = {OFF_OF + h * 8192 + lane * 16, OFF_KVM + h * 4096 + (gq * 8 + (c & 7)) * 16, OFF_KVM + h * 4096 + 2048 + ((gq & 1) * 16 + c) * 16, OFF_CTL + 4 * (C_PROG + 64 * h)};
  const v4u av1 = {lane == 0 ? OFF_CTL + 4 * C_OCNT : OOB, 4 * gq - c, OFF_KF + h * 8192 + lane * 16, OFF_VF + h * 8192 + lane * 16};
  const v4u rwsv = mkrv(p.wb, WS_END);
  GEMM_UNPACK
  asm volatile(
    "s_nop 4\n"
    "s_mov_b32 s60, 0\n"
    "s_mov_b32 s61, 0\n"
    "s_mov_b32 s67, 0\n"
    "s_mov_b32 s66, 0\n"
    "s_mov_b32 s63, s54\n"
    "s_mov_b32 s64, s55\n"
    "s_add_u32 m0, s63, 0\n"
    "s_nop 0\n"
    "buffer_load_dwordx4 v14, s[40:43], s60 offen lds\n"
    "s_add_u32 m0, s63, 1024\n"
    "s_nop 0\n"
    "buffer_load_dwordx4 v15, s[40:43], s60 offen lds\n"
    "s_add_u32 m0, s63, 2048\n"
    "s_nop 0\n"
    "buffer_load_dwordx4 v16, s[40:43], s60 offen lds\n"
    "s_add_u32 m0, s63, 3072\n"
    "s_nop 0\n"
    "buffer_load_dwordx4 v17, s[40:43], s60 offen lds\n"
    "s_add_u32 m0, s64, 0\n"
    "s_nop 0\n"
    "buffer_load_dwordx4 v18, s[44:47], s61 offen lds\n"
    "s_add_u32 m0, s64, 8192\n"
    "s_nop 0\n"
    "buffer_load_dwordx4 v19, s[44:47], s61 offen lds\n"
    "s_add_u32 m0, s64, 16384\n"
    "s_nop 0\n"
    "buffer_load_dwordx4 v20, s[44:47], s61 offen lds\n"
    "s_add_u32 m0, s64, 24576\n"
    "s_nop 0\n"
    "buffer_load_dwordx4 v21, s[44:47], s61 offen lds\n"
    "s_add_u32 m0, s64, 4096\n"
    "s_nop 0\n"
    "buffer_load_dwordx4 v22, s[48:51], s61 offen lds\n"
    "s_add_u32 m0, s64, 12288\n"
    "s_nop 0\n"
    "buffer_load_dwordx4 v23, s[48:51], s61 offen lds\n"
    "s_add_u32 m0, s64, 20480\n"
    "s_nop 0\n"
    "buffer_load_dwordx4 v24, s[48:51], s61 offen lds\n"
    "s_add_u32 m0, s64, 28672\n"
    "s_nop 0\n"
    "buffer_load_dwordx4 v25, s[48:51], s61 offen lds\n"
    "s_add_u32 s60, s60, 128\n"
    "s_add_u32 s67, s67, 1\n"
    "s_cmp_ge_u32 s67, s56\n"
    "s_cselect_b32 s42, 0, s42\n"
    "s_add_u32 s66, s66, 49152\n"
    "s_cmp_eq_u32 s66, 147456\n"
    "s_cselect_b32 s66, 0, s66\n"
    "s_add_u32 s63, s66, s54\n"
    "s_add_u32 s61, s61, s58\n"
    "s_add_u32 s64, s66, s55\n"
    "s_cmp_eq_u32 s67, s57\n"
    "s_cselect_b32 s61, 0, s61\n"
    "s_cselect_b64 s[44:45], s[52:53], s[44:45]\n"
    "s_cselect_b64 s[48:49], s[52:53], s[48:49]\n"
    "s_cmp_ge_u32 s67, s56\n"
    "s_cselect_b32 s46, 0, s46\n"
    "s_cselect_b32 s50, 0, s50\n"
    "s_add_u32 m0, s63, 0\n"
    "s_nop 0\n"
    "buffer_load_dwordx4 v14, s[40:43], s60 offen lds\n"
    "s_add_u32 m0, s63, 1024\n"
    "s_nop 0\n"
    "buffer_load_dwordx4 v15, s[40:43], s60 offen lds\n"
    "s_add_u32 m0, s63, 2048\n"
    "s_nop 0\n"
    "buffer_load_dwordx4 v16, s[40:43], s60 offen lds\n"
    "s_add_u32 m0, s63, 3072\n"
    "s_nop 0\n"
    "buffer_load_dwordx4 v17, s[40:43], s60 offen lds\n"
    "s_add_u32 m0, s64, 0\n"
    "s_nop 0\n"
    "buffer_load_dwordx4 v18, s[44:47], s61 offen lds\n"
    "s_add_u32 m0, s64, 8192\n"
    "s_nop 0\n"
    "buffer_load_dwordx4 v19, s[44:47], s61 offen lds\n"
    "s_add_u32 m0, s64, 16384\n"
    "s_nop 0\n"
    "buffer_load_dwordx4 v20, s[44:47], s61 offen lds\n"
    "s_add_u32 m0, s64, 24576\n"
    "s_nop 0\n"
    "buffer_load_dwordx4 v21, s[44:47], s61 offen lds\n"
    "s_add_u32 m0, s64, 4096\n"
    "s_nop 0\n"
    "buffer_load_dwordx4 v22, s[48:51], s61 offen lds\n"
    "s_add_u32 m0, s64, 12288\n"
    "s_nop 0\n"
    "buffer_load_dwordx4 v23, s[48:51], s61 offen lds\n"
    "s_add_u32 m0, s64, 20480\n"
    "s_nop 0\n"
    "buffer_load_dwordx4 v24, s[48:51], s61 offen lds\n"
    "s_add_u32 m0, s64, 28672\n"
    "s_nop 0\n"
    "buffer_load_dwordx4 v25, s[48:51], s61 offen lds\n"
    "s_add_u32 s60, s60, 128\n"
    "s_add_u32 s67, s67, 1\n"
    "s_cmp_ge_u32 s67, s56\n"
    "s_cselect_b32 s42, 0, s42\n"
    "s_add_u32 s66, s66, 49152\n"
    "s_cmp_eq_u32 s66, 147456\n"
    "s_cselect_b32 s66, 0, s66\n"
    "s_add_u32 s63, s66, s54\n"
    "s_add_u32 s61, s61, s58\n"
    "s_add_u32 s64, s66, s55\n"
    "s_cmp_eq_u32 s67, s57\n"
    "s_cselect_b32 s61, 0, s61\n"
    "s_cselect_b64 s[44:45], s[52:53], s[44:45]\n"
    "s_cselect_b64 s[48:49], s[52:53], s[48:49]\n"
    "s_cmp_ge_u32 s67, s56\n"
    "s_cselect_b32 s46, 0, s46\n"
    "s_cselect_b32 s50, 0, s50\n"
    "s_add_u32 m0, s63, 0\n"
    "s_nop 0\n"
    "buffer_load_dwordx4 v14, s[40:43], s60 offen lds\n"
    "s_add_u32 m0, s63, 1024\n"
    "s_nop 0\n"
    "buffer_load_dwordx4 v15, s[40:43], s60 offen lds\n"
    "s_add_u32 m0, s63, 2048\n"
    "s_nop 0\n"
    "buffer_load_dwordx4 v16, s[40:43], s60 offen lds\n"
    "s_add_u32 m0, s63, 3072\n"
    "s_nop 0\n"
    "buffer_load_dwordx4 v17, s[40:43], s60 offen lds\n"
    "s_add_u32 m0, s64, 0\n"
    "s_nop 0\n"
    "buffer_load_dwordx4 v18, s[44:47], s61 offen lds\n"
    "s_add_u32 m0, s64, 8192\n"
    "s_nop 0\n"
    "buffer_load_dwordx4 v19, s[44:47], s61 offen lds\n"
    "v_mov_b64 v[128:129], 0\n"
    "v_mov_b64 v[130:131], 0\n"
    "v_mov_b64 v[132:133], 0\n"
    "v_mov_b64 v[134:135], 0\n"
    "v_mov_b64 v[136:137], 0\n"
    "v_mov_b64 v[138:139], 0\n"
    "v_mov_b64 v[140:141], 0\n"
    "v_mov_b64 v[142:143], 0\n"
    "v_mov_b64 v[144:145], 0\n"
    "v_mov_b64 v[146:147], 0\n"
    "v_mov_b64 v[148:149], 0\n"
    "v_mov_b64 v[150:151], 0\n"
    "v_mov_b64 v[152:153], 0\n"
    "v_mov_b64 v[154:155], 0\n"
    "v_mov_b64 v[156:157], 0\n"
    "v_mov_b64 v[158:159], 0\n"
    "v_mov_b64 v[160:161], 0\n"
    "v_mov_b64 v[162:163], 0\n"
    "v_mov_b64 v[164:165], 0\n"
    "v_mov_b64 v[166:167], 0\n"
    "v_mov_b64 v[168:169], 0\n"
    "v_mov_b64 v[170:171], 0\n"
    "v_mov_b64 v[172:173], 0\n"
    "v_mov_b64 v[174:175], 0\n"
    "v_mov_b64 v[176:177], 0\n"
    "v_mov_b64 v[178:179], 0\n"
    "v_mov_b64 v[180:181], 0\n"
    "v_mov_b64 v[182:183], 0\n"
    "v_mov_b64 v[184:185], 0\n"
    "v_mov_b64 v[186:187], 0\n"
    "v_mov_b64 v[188:189], 0\n"
    "v_mov_b64 v[190:191], 0\n"
    "v_mov_b64 v[192:193], 0\n"
    "v_mov_b64 v[194:195], 0\n"
    "v_mov_b64 v[196:197], 0\n"
    "v_mov_b64 v[198:199], 0\n"
    "v_mov_b64 v[200:201], 0\n"
    "v_mov_b64 v[202:203], 0\n"
    "v_mov_b64 v[204:205], 0\n"
    "v_mov_b64 v[206:207], 0\n"
    "v_mov_b64 v[208:209], 0\n"
    "v_mov_b64 v[210:211], 0\n"
    "v_mov_b64 v[212:213], 0\n"
    "v_mov_b64 v[214:215], 0\n"
    "v_mov_b64 v[216:217], 0\n"
    "v_mov_b64 v[218:219], 0\n"
    "v_mov_b64 v[220:221], 0\n"
    "v_mov_b64 v[222:223], 0\n"
    "v_mov_b64 v[224:225], 0\n"
    "v_mov_b64 v[226:227], 0\n"
    "v_mov_b64 v[228:229], 0\n"
    "v_mov_b64 v[230:231], 0\n"
    "v_mov_b64 v[232:233], 0\n"
    "v_mov_b64 v[234:235], 0\n"
    "v_mov_b64 v[236:237], 0\n"
    "v_mov_b64 v[238:239], 0\n"
    "v_mov_b64 v[240:241], 0\n"
    "v_mov_b64 v[242:243], 0\n"
    "v_mov_b64 v[244:245], 0\n"
    "v_mov_b64 v[246:247], 0\n"
    "v_mov_b64 v[248:249], 0\n"
    "v_mov_b64 v[250:251], 0\n"
    "v_mov_b64 v[252:253], 0\n"
    "v_mov_b64 v[254:255], 0\n"
    "s_mov_b32 s68, 4294868992\n"
    "s_sub_u32 s62, s56, 3\n"
    "s_mov_b32 s65, 0\n"
    "s_waitcnt vmcnt(18)\n"
    "s_barrier\n"
    "ds_read_b128 v[32:35], v26 offset:0\n"
    "ds_read_b128 v[36:39], v26 offset:2048\n"
    "ds_read_b128 v[40:43], v26 offset:4096\n"
    "ds_read_b128 v[44:47], v26 offset:6144\n"
    "ds_read_b64_tr_b16 v[64:65], v28 offset:0\n"
    "ds_read_b64_tr_b16 v[66:67], v28 offset:512\n"
    "ds_read_b64_tr_b16 v[68:69], v29 offset:0\n"
    "ds_read_b64_tr_b16 v[70:71], v29 offset:512\n"
    "ds_read_b64_tr_b16 v[72:73], v30 offset:0\n"
    "ds_read_b64_tr_b16 v[74:75], v30 offset:512\n"
    "ds_read_b64_tr_b16 v[76:77], v31 offset:0\n"
    "ds_read_b64_tr_b16 v[78:79], v31 offset:512\n"
    "ds_read_b64_tr_b16 v[80:81], v28 offset:8192\n"
    "ds_read_b64_tr_b16 v[82:83], v28 offset:8704\n"
    "ds_read_b64_tr_b16 v[84:85], v29 offset:8192\n"
    "ds_read_b64_tr_b16 v[86:87], v29 offset:8704\n"
    "ds_read_b64_tr_b16 v[88:89], v30 offset:8192\n"
    "ds_read_b64_tr_b16 v[90:91], v30 offset:8704\n"
    "ds_read_b64_tr_b16 v[92:93], v31 offset:8192\n"
    "ds_read_b64_tr_b16 v[94:95], v31 offset:8704\n"
    "s_waitcnt lgkmcnt(0)\n"
    ".LGQ_loop%=:\n"
    "v_mfma_f32_16x16x32_bf16 v[128:131], v[64:67], v[32:35], v[128:131]\n"
    "ds_read_b128 v[48:51], v27 offset:0\n"
    "s_add_u32 m0, s64, 16384\n"
    "v_mfma_f32_16x16x32_bf16 v[132:135], v[64:67], v[36:39], v[132:135]\n"
    "ds_read_b128 v[52:55], v27 offset:2048\n"
    "buffer_load_dwordx4 v20, s[44:47], s61 offen lds\n"
    "v_mfma_f32_16x16x32_bf16 v[136:139], v[64:67], v[40:43], v[136:139]\n"
    "ds_read_b128 v[56:59], v27 offset:4096\n"
    "s_add_u32 s60, s60, 128\n"
    "v_mfma_f32_16x16x32_bf16 v[140:143], v[64:67], v[44:47], v[140:143]\n"
    "ds_read_b128 v[60:63], v27 offset:6144\n"
    "s_add_u32 s67, s67, 1\n"
    "v_mfma_f32_16x16x32_bf16 v[144:147], v[68:71], v[32:35], v[144:147]\n"
    "ds_read_b64_tr_b16 v[96:97], v28 offset:4096\n"
    "s_add_u32 s65, s65, 1\n"
    "v_mfma_f32_16x16x32_bf16 v[148:151], v[68:71], v[36:39], v[148:151]\n"
    "ds_read_b64_tr_b16 v[98:99], v28 offset:4608\n"
    "s_add_u32 m0, s64, 24576\n"
    "v_mfma_f32_16x16x32_bf16 v[152:155], v[68:71], v[40:43], v[152:155]\n"
    "ds_read_b64_tr_b16 v[100:101], v29 offset:4096\n"
    "buffer_load_dwordx4 v21, s[44:47], s61 offen lds\n"
    "v_mfma_f32_16x16x32_bf16 v[156:159], v[68:71], v[44:47], v[156:159]\n"
    "ds_read_b64_tr_b16 v[102:103], v29 offset:4608\n"
    "s_cmp_eq_u32 s65, 3\n"
    "v_mfma_f32_16x16x32_bf16 v[160:163], v[72:75], v[32:35], v[160:163]\n"
    "ds_read_b64_tr_b16 v[104:105], v30 offset:4096\n"
    "s_cselect_b32 s65, 0, s65\n"
    "v_mfma_f32_16x16x32_bf16 v[164:167], v[72:75], v[36:39], v[164:167]\n"
    "ds_read_b64_tr_b16 v[106:107], v30 offset:4608\n"
    "s_cselect_b32 s59, s68, 49152\n"
    "v_mfma_f32_16x16x32_bf16 v[168:171], v[72:75], v[40:43], v[168:171]\n"
    "ds_read_b64_tr_b16 v[108:109], v31 offset:4096\n"
    "s_add_u32 m0, s64, 4096\n"
    "v_mfma_f32_16x16x32_bf16 v[172:175], v[72:75], v[44:47], v[172:175]\n"
    "ds_read_b64_tr_b16 v[110:111], v31 offset:4608\n"
    "buffer_load_dwordx4 v22, s[48:51], s61 offen lds\n"
    "v_mfma_f32_16x16x32_bf16 v[176:179], v[76:79], v[32:35], v[176:179]\n"
    "ds_read_b64_tr_b16 v[112:113], v28 offset:12288\n"
    "s_cmp_ge_u32 s67, s56\n"
    "v_mfma_f32_16x16x32_bf16 v[180:183], v[76:79], v[36:39], v[180:183]\n"
    "ds_read_b64_tr_b16 v[114:115], v28 offset:12800\n"
    "s_cselect_b32 s42, 0, s42\n"
    "v_mfma_f32_16x16x32_bf16 v[184:187], v[76:79], v[40:43], v[184:187]\n"
    "ds_read_b64_tr_b16 v[116:117], v29 offset:12288\n"
    "s_add_u32 s66, s66, 49152\n"
    "v_mfma_f32_16x16x32_bf16 v[188:191], v[76:79], v[44:47], v[188:191]\n"
    "ds_read_b64_tr_b16 v[118:119], v29 offset:12800\n"
    "s_add_u32 m0, s64, 12288\n"
    "v_mfma_f32_16x16x32_bf16 v[192:195], v[80:83], v[32:35], v[192:195]\n"
    "ds_read_b64_tr_b16 v[120:121], v30 offset:12288\n"
    "buffer_load_dwordx4 v23, s[48:51], s61 offen lds\n"
    "v_mfma_f32_16x16x32_bf16 v[196:199], v[80:83], v[36:39], v[196:199]\n"
    "ds_read_b64_tr_b16 v[122:123], v30 offset:12800\n"
    "s_cmp_eq_u32 s66, 147456\n"
    "v_mfma_f32_16x16x32_bf16 v[200:203], v[80:83], v[40:43], v[200:203]\n"
    "ds_read_b64_tr_b16 v[124:125], v31 offset:12288\n"
    "s_cselect_b32 s66, 0, s66\n"
    "v_mfma_f32_16x16x32_bf16 v[204:207], v[80:83], v[44:47], v[204:207]\n"
    "ds_read_b64_tr_b16 v[126:127], v31 offset:12800\n"
    "s_add_u32 s63, s66, s54\n"
    "v_mfma_f32_16x16x32_bf16 v[208:211], v[84:87], v[32:35], v[208:211]\n"
    "v_add_u32 v26, s59, v26\n"
    "s_add_u32 m0, s64, 20480\n"
    "v_mfma_f32_16x16x32_bf16 v[212:215], v[84:87], v[36:39], v[212:215]\n"
    "v_add_u32 v27, s59, v27\n"
    "buffer_load_dwordx4 v24, s[48:51], s61 offen lds\n"
    "v_mfma_f32_16x16x32_bf16 v[216:219], v[84:87], v[40:43], v[216:219]\n"
    "v_add_u32 v28, s59, v28\n"
    "v_mfma_f32_16x16x32_bf16 v[220:223], v[84:87], v[44:47], v[220:223]\n"
    "v_add_u32 v29, s59, v29\n"
    "v_mfma_f32_16x16x32_bf16 v[224:227], v[88:91], v[32:35], v[224:227]\n"
    "v_add_u32 v30, s59, v30\n"
    "v_mfma_f32_16x16x32_bf16 v[228:231], v[88:91], v[36:39], v[228:231]\n"
    "v_add_u32 v31, s59, v31\n"
    "s_add_u32 m0, s64, 28672\n"
    "v_mfma_f32_16x16x32_bf16 v[232:235], v[88:91], v[40:43], v[232:235]\n"
    "buffer_load_dwordx4 v25, s[48:51], s61 offen lds\n"
    "v_mfma_f32_16x16x32_bf16 v[236:239], v[88:91], v[44:47], v[236:239]\n"
    "v_mfma_f32_16x16x32_bf16 v[240:243], v[92:95], v[32:35], v[240:243]\n"
    "v_mfma_f32_16x16x32_bf16 v[244:247], v[92:95], v[36:39], v[244:247]\n"
    "v_mfma_f32_16x16x32_bf16 v[248:251], v[92:95], v[40:43], v[248:251]\n"
    "v_mfma_f32_16x16x32_bf16 v[252:255], v[92:95], v[44:47], v[252:255]\n"
    "s_waitcnt vmcnt(12) lgkmcnt(0)\n"
    "s_barrier\n"
    "v_mfma_f32_16x16x32_bf16 v[128:131], v[96:99], v[48:51], v[128:131]\n"
    "ds_read_b128 v[32:35], v26 offset:0\n"
    "s_add_u32 m0, s63, 0\n"
    "v_mfma_f32_16x16x32_bf16 v[132:135], v[96:99], v[52:55], v[132:135]\n"
    "ds_read_b128 v[36:39], v26 offset:2048\n"
    "buffer_load_dwordx4 v14, s[40:43], s60 offen lds\n"
    "v_mfma_f32_16x16x32_bf16 v[136:139], v[96:99], v[56:59], v[136:139]\n"
    "ds_read_b128 v[40:43], v26 offset:4096\n"
    "s_add_u32 s61, s61, s58\n"
    "v_mfma_f32_16x16x32_bf16 v[140:143], v[96:99], v[60:63], v[140:143]\n"
    "ds_read_b128 v[44:47], v26 offset:6144\n"
    "s_add_u32 s64, s66, s55\n"
    "v_mfma_f32_16x16x32_bf16 v[144:147], v[100:103], v[48:51], v[144:147]\n"
    "ds_read_b64_tr_b16 v[64:65], v28 offset:0\n"
    "v_mfma_f32_16x16x32_bf16 v[148:151], v[100:103], v[52:55], v[148:151]\n"
    "ds_read_b64_tr_b16 v[66:67], v28 offset:512\n"
    "s_add_u32 m0, s63, 1024\n"
    "v_mfma_f32_16x16x32_bf16 v[152:155], v[100:103], v[56:59], v[152:155]\n"
    "ds_read_b64_tr_b16 v[68:69], v29 offset:0\n"
    "buffer_load_dwordx4 v15, s[40:43], s60 offen lds\n"
    "v_mfma_f32_16x16x32_bf16 v[156:159], v[100:103], v[60:63], v[156:159]\n"
    "ds_read_b64_tr_b16 v[70:71], v29 offset:512\n"
    "s_cmp_eq_u32 s67, s57\n"
    "v_mfma_f32_16x16x32_bf16 v[160:163], v[104:107], v[48:51], v[160:163]\n"
    "ds_read_b64_tr_b16 v[72:73], v30 offset:0\n"
    "s_cselect_b32 s61, 0, s61\n"
    "v_mfma_f32_16x16x32_bf16 v[164:167], v[104:107], v[52:55], v[164:167]\n"
    "ds_read_b64_tr_b16 v[74:75], v30 offset:512\n"
    "s_cselect_b64 s[44:45], s[52:53], s[44:45]\n"
    "s_cselect_b64 s[48:49], s[52:53], s[48:49]\n"
    "v_mfma_f32_16x16x32_bf16 v[168:171], v[104:107], v[56:59], v[168:171]\n"
    "ds_read_b64_tr_b16 v[76:77], v31 offset:0\n"
    "s_add_u32 m0, s63, 2048\n"
    "v_mfma_f32_16x16x32_bf16 v[172:175], v[104:107], v[60:63], v[172:175]\n"
    "ds_read_b64_tr_b16 v[78:79], v31 offset:512\n"
    "buffer_load_dwordx4 v16, s[40:43], s60 offen lds\n"
    "v_mfma_f32_16x16x32_bf16 v[176:179], v[108:111], v[48:51], v[176:179]\n"
    "ds_read_b64_tr_b16 v[80:81], v28 offset:8192\n"
    "s_cmp_ge_u32 s67, s56\n"
    "v_mfma_f32_16x16x32_bf16 v[180:183], v[108:111], v[52:55], v[180:183]\n"
    "ds_read_b64_tr_b16 v[82:83], v28 offset:8704\n"
    "s_cselect_b32 s46, 0, s46\n"
    "v_mfma_f32_16x16x32_bf16 v[184:187], v[108:111], v[56:59], v[184:187]\n"
    "ds_read_b64_tr_b16 v[84:85], v29 offset:8192\n"
    "s_cselect_b32 s50, 0, s50\n"
    "v_mfma_f32_16x16x32_bf16 v[188:191], v[108:111], v[60:63], v[188:191]\n"
    "ds_read_b64_tr_b16 v[86:87], v29 offset:8704\n"
    "s_add_u32 m0, s63, 3072\n"
    "v_mfma_f32_16x16x32_bf16 v[192:195], v[112:115], v[48:51], v[192:195]\n"
    "ds_read_b64_tr_b16 v[88:89], v30 offset:8192\n"
    "buffer_load_dwordx4 v17, s[40:43], s60 offen lds\n"
    "v_mfma_f32_16x16x32_bf16 v[196:199], v[112:115], v[52:55], v[196:199]\n"
    "ds_read_b64_tr_b16 v[90:91], v30 offset:8704\n"
    "v_mfma_f32_16x16x32_bf16 v[200:203], v[112:115], v[56:59], v[200:203]\n"
    "ds_read_b64_tr_b16 v[92:93], v31 offset:8192\n"
    "v_mfma_f32_16x16x32_bf16 v[204:207], v[112:115], v[60:63], v[204:207]\n"
    "ds_read_b64_tr_b16 v[94:95], v31 offset:8704\n"
    "v_mfma_f32_16x16x32_bf16 v[208:211], v[116:119], v[48:51], v[208:211]\n"
    "s_add_u32 m0, s64, 0\n"
    "v_mfma_f32_16x16x32_bf16 v[212:215], v[116:119], v[52:55], v[212:215]\n"
    "buffer_load_dwordx4 v18, s[44:47], s61 offen lds\n"
    "v_mfma_f32_16x16x32_bf16 v[216:219], v[116:119], v[56:59], v[216:219]\n"
    "v_mfma_f32_16x16x32_bf16 v[220:223], v[116:119], v[60:63], v[220:223]\n"
    "v_mfma_f32_16x16x32_bf16 v[224:227], v[120:123], v[48:51], v[224:227]\n"
    "v_mfma_f32_16x16x32_bf16 v[228:231], v[120:123], v[52:55], v[228:231]\n"
    "s_add_u32 m0, s64, 8192\n"
    "v_mfma_f32_16x16x32_bf16 v[232:235], v[120:123], v[56:59], v[232:235]\n"
    "buffer_load_dwordx4 v19, s[44:47], s61 offen lds\n"
    "v_mfma_f32_16x16x32_bf16 v[236:239], v[120:123], v[60:63], v[236:239]\n"
    "s_sub_u32 s62, s62, 1\n"
    "v_mfma_f32_16x16x32_bf16 v[240:243], v[124:127], v[48:51], v[240:243]\n"
    "s_cmp_eq_u32 s62, 0\n"
    "v_mfma_f32_16x16x32_bf16 v[244:247], v[124:127], v[52:55], v[244:247]\n"
    "v_mfma_f32_16x16x32_bf16 v[248:251], v[124:127], v[56:59], v[248:251]\n"
    "v_mfma_f32_16x16x32_bf16 v[252:255], v[124:127], v[60:63], v[252:255]\n"
    "s_waitcnt lgkmcnt(0)\n"
    "s_cbranch_scc0 .LGQ_loop%=\n"
    "s_lshl_b32 s80, s76, 18\n"
    "s_add_u32 s81, s80, 4096\n"
    "s_add_u32 s82, s80, 0x80000\n"
    "s_add_u32 s83, s80, 0x81000\n"
    "v_mfma_f32_16x16x32_bf16 v[128:131], v[64:67], v[32:35], v[128:131]\n"
    "ds_read_b128 v[48:51], v27 offset:0\n"
    "s_add_u32 m0, s64, 16384\n"
    "v_mfma_f32_16x16x32_bf16 v[132:135], v[64:67], v[36:39], v[132:135]\n"
    "ds_read_b128 v[52:55], v27 offset:2048\n"
    "buffer_load_dwordx4 v20, s[44:47], s61 offen lds\n"
    "v_mfma_f32_16x16x32_bf16 v[136:139], v[64:67], v[40:43], v[136:139]\n"
    "ds_read_b128 v[56:59], v27 offset:4096\n"
    "s_add_u32 s65, s65, 1\n"
    "v_mfma_f32_16x16x32_bf16 v[140:143], v[64:67], v[44:47], v[140:143]\n"
    "ds_read_b128 v[60:63], v27 offset:6144\n"
    "v_mfma_f32_16x16x32_bf16 v[144:147], v[68:71], v[32:35], v[144:147]\n"
    "ds_read_b64_tr_b16 v[96:97], v28 offset:4096\n"
    "v_mfma_f32_16x16x32_bf16 v[148:151], v[68:71], v[36:39], v[148:151]\n"
    "ds_read_b64_tr_b16 v[98:99], v28 offset:4608\n"
    "s_add_u32 m0, s64, 24576\n"
    "v_mfma_f32_16x16x32_bf16 v[152:155], v[68:71], v[40:43], v[152:155]\n"
    "ds_read_b64_tr_b16 v[100:101], v29 offset:4096\n"
    "buffer_load_dwordx4 v21, s[44:47], s61 offen lds\n"
    "v_mfma_f32_16x16x32_bf16 v[156:159], v[68:71], v[44:47], v[156:159]\n"
    "ds_read_b64_tr_b16 v[102:103], v29 offset:4608\n"
    "s_cmp_eq_u32 s65, 3\n"
    "v_mfma_f32_16x16x32_bf16 v[160:163], v[72:75], v[32:35], v[160:163]\n"
    "ds_read_b64_tr_b16 v[104:105], v30 offset:4096\n"
    "s_cselect_b32 s65, 0, s65\n"
    "v_mfma_f32_16x16x32_bf16 v[164:167], v[72:75], v[36:39], v[164:167]\n"
    "ds_read_b64_tr_b16 v[106:107], v30 offset:4608\n"
    "s_cselect_b32 s59, s68, 49152\n"
    "v_mfma_f32_16x16x32_bf16 v[168:171], v[72:75], v[40:43], v[168:171]\n"
    "ds_read_b64_tr_b16 v[108:109], v31 offset:4096\n"
    "s_add_u32 m0, s64, 4096\n"
    "v_mfma_f32_16x16x32_bf16 v[172:175], v[72:75], v[44:47], v[172:175]\n"
    "ds_read_b64_tr_b16 v[110:111], v31 offset:4608\n"
    "buffer_load_dwordx4 v22, s[48:51], s61 offen lds\n"
    "v_mfma_f32_16x16x32_bf16 v[176:179], v[76:79], v[32:35], v[176:179]\n"
    "ds_read_b64_tr_b16 v[112:113], v28 offset:12288\n"
    "v_mfma_f32_16x16x32_bf16 v[180:183], v[76:79], v[36:39], v[180:183]\n"
    "ds_read_b64_tr_b16 v[114:115], v28 offset:12800\n"
    "v_mfma_f32_16x16x32_bf16 v[184:187], v[76:79], v[40:43], v[184:187]\n"
    "ds_read_b64_tr_b16 v[116:117], v29 offset:12288\n"
    "v_mfma_f32_16x16x32_bf16 v[188:191], v[76:79], v[44:47], v[188:191]\n"
    "ds_read_b64_tr_b16 v[118:119], v29 offset:12800\n"
    "s_add_u32 m0, s64, 12288\n"
    "v_mfma_f32_16x16x32_bf16 v[192:195], v[80:83], v[32:35], v[192:195]\n"
    "ds_read_b64_tr_b16 v[120:121], v30 offset:12288\n"
    "buffer_load_dwordx4 v23, s[48:51], s61 offen lds\n"
    "v_mfma_f32_16x16x32_bf16 v[196:199], v[80:83], v[36:39], v[196:199]\n"
    "ds_read_b64_tr_b16 v[122:123], v30 offset:12800\n"
    "v_mfma_f32_16x16x32_bf16 v[200:203], v[80:83], v[40:43], v[200:203]\n"
    "ds_read_b64_tr_b16 v[124:125], v31 offset:12288\n"
    "v_mfma_f32_16x16x32_bf16 v[204:207], v[80:83], v[44:47], v[204:207]\n"
    "ds_read_b64_tr_b16 v[126:127], v31 offset:12800\n"
    "v_mfma_f32_16x16x32_bf16 v[208:211], v[84:87], v[32:35], v[208:211]\n"
    "v_add_u32 v26, s59, v26\n"
    "s_add_u32 m0, s64, 20480\n"
    "v_mfma_f32_16x16x32_bf16 v[212:215], v[84:87], v[36:39], v[212:215]\n"
    "v_add_u32 v27, s59, v27\n"
    "buffer_load_dwordx4 v24, s[48:51], s61 offen lds\n"
    "v_mfma_f32_16x16x32_bf16 v[216:219], v[84:87], v[40:43], v[216:219]\n"
    "v_add_u32 v28, s59, v28\n"
    "v_mfma_f32_16x16x32_bf16 v[220:223], v[84:87], v[44:47], v[220:223]\n"
    "v_add_u32 v29, s59, v29\n"
    "v_mfma_f32_16x16x32_bf16 v[224:227], v[88:91], v[32:35], v[224:227]\n"
    "v_add_u32 v30, s59, v30\n"
    "v_mfma_f32_16x16x32_bf16 v[228:231], v[88:91], v[36:39], v[228:231]\n"
    "v_add_u32 v31, s59, v31\n"
    "s_add_u32 m0, s64, 28672\n"
    "v_mfma_f32_16x16x32_bf16 v[232:235], v[88:91], v[40:43], v[232:235]\n"
    "buffer_load_dwordx4 v25, s[48:51], s61 offen lds\n"
    "v_mfma_f32_16x16x32_bf16 v[236:239], v[88:91], v[44:47], v[236:239]\n"
    "v_mfma_f32_16x16x32_bf16 v[240:243], v[92:95], v[32:35], v[240:243]\n"
    "v_mfma_f32_16x16x32_bf16 v[244:247], v[92:95], v[36:39], v[244:247]\n"
    "v_mfma_f32_16x16x32_bf16 v[248:251], v[92:95], v[40:43], v[248:251]\n"
    "v_mfma_f32_16x16x32_bf16 v[252:255], v[92:95], v[44:47], v[252:255]\n"
    "s_waitcnt vmcnt(12) lgkmcnt(0)\n"
    "s_barrier\n"
    "v_mfma_f32_16x16x32_bf16 v[128:131], v[96:99], v[48:51], v[128:131]\n"
    "ds_read_b128 v[32:35], v26 offset:0\n"
    "v_mfma_f32_16x16x32_bf16 v[132:135], v[96:99], v[52:55], v[132:135]\n"
    "ds_read_b128 v[36:39], v26 offset:2048\n"
    "buffer_load_dwordx4 a[0:3], v8, s[72:75], s80 offen offset:0 sc1\n"
    "v_mfma_f32_16x16x32_bf16 v[136:139], v[96:99], v[56:59], v[136:139]\n"
    "ds_read_b128 v[40:43], v26 offset:4096\n"
    "v_mfma_f32_16x16x32_bf16 v[140:143], v[96:99], v[60:63], v[140:143]\n"
    "ds_read_b128 v[44:47], v26 offset:6144\n"
    "v_mfma_f32_16x16x32_bf16 v[144:147], v[100:103], v[48:51], v[144:147]\n"
    "ds_read_b64_tr_b16 v[64:65], v28 offset:0\n"
    "v_mfma_f32_16x16x32_bf16 v[148:151], v[100:103], v[52:55], v[148:151]\n"
    "ds_read_b64_tr_b16 v[66:67], v28 offset:512\n"
    "buffer_load_dwordx4 a[4:7], v8, s[72:75], s80 offen offset:1024 sc1\n"
    "v_mfma_f32_16x16x32_bf16 v[152:155], v[100:103], v[56:59], v[152:155]\n"
    "ds_read_b64_tr_b16 v[68:69], v29 offset:0\n"
    "v_mfma_f32_16x16x32_bf16 v[156:159], v[100:103], v[60:63], v[156:159]\n"
    "ds_read_b64_tr_b16 v[70:71], v29 offset:512\n"
    "v_mfma_f32_16x16x32_bf16 v[160:163], v[104:107], v[48:51], v[160:163]\n"
    "ds_read_b64_tr_b16 v[72:73], v30 offset:0\n"
    "v_mfma_f32_16x16x32_bf16 v[164:167], v[104:107], v[52:55], v[164:167]\n"
    "ds_read_b64_tr_b16 v[74:75], v30 offset:512\n"
    "buffer_load_dwordx4 a[8:11], v8, s[72:75], s80 offen offset:2048 sc1\n"
    "v_mfma_f32_16x16x32_bf16 v[168:171], v[104:107], v[56:59], v[168:171]\n"
    "ds_read_b64_tr_b16 v[76:77], v31 offset:0\n"
    "v_mfma_f32_16x16x32_bf16 v[172:175], v[104:107], v[60:63], v[172:175]\n"
    "ds_read_b64_tr_b16 v[78:79], v31 offset:512\n"
    "v_mfma_f32_16x16x32_bf16 v[176:179], v[108:111], v[48:51], v[176:179]\n"
    "ds_read_b64_tr_b16 v[80:81], v28 offset:8192\n"
    "v_mfma_f32_16x16x32_bf16 v[180:183], v[108:111], v[52:55], v[180:183]\n"
    "ds_read_b64_tr_b16 v[82:83], v28 offset:8704\n"
    "buffer_load_dwordx4 a[12:15], v8, s[72:75], s80 offen offset:3072 sc1\n"
    "v_mfma_f32_16x16x32_bf16 v[184:187], v[108:111], v[56:59], v[184:187]\n"
    "ds_read_b64_tr_b16 v[84:85], v29 offset:8192\n"
    "v_mfma_f32_16x16x32_bf16 v[188:191], v[108:111], v[60:63], v[188:191]\n"
    "ds_read_b64_tr_b16 v[86:87], v29 offset:8704\n"
    "v_mfma_f32_16x16x32_bf16 v[192:195], v[112:115], v[48:51], v[192:195]\n"
    "ds_read_b64_tr_b16 v[88:89], v30 offset:8192\n"
    "v_mfma_f32_16x16x32_bf16 v[196:199], v[112:115], v[52:55], v[196:199]\n"
    "ds_read_b64_tr_b16 v[90:91], v30 offset:8704\n"
    "buffer_load_dwordx4 a[16:19], v8, s[72:75], s81 offen offset:0 sc1\n"
    "v_mfma_f32_16x16x32_bf16 v[200:203], v[112:115], v[56:59], v[200:203]\n"
    "ds_read_b64_tr_b16 v[92:93], v31 offset:8192\n"
    "v_mfma_f32_16x16x32_bf16 v[204:207], v[112:115], v[60:63], v[204:207]\n"
    "ds_read_b64_tr_b16 v[94:95], v31 offset:8704\n"
    "v_mfma_f32_16x16x32_bf16 v[208:211], v[116:119], v[48:51], v[208:211]\n"
    "v_mfma_f32_16x16x32_bf16 v[212:215], v[116:119], v[52:55], v[212:215]\n"
    "buffer_load_dwordx4 a[20:23], v8, s[72:75], s81 offen offset:1024 sc1\n"
    "v_mfma_f32_16x16x32_bf16 v[216:219], v[116:119], v[56:59], v[216:219]\n"
    "v_mfma_f32_16x16x32_bf16 v[220:223], v[116:119], v[60:63], v[220:223]\n"
    "v_mfma_f32_16x16x32_bf16 v[224:227], v[120:123], v[48:51], v[224:227]\n"
    "v_mfma_f32_16x16x32_bf16 v[228:231], v[120:123], v[52:55], v[228:231]\n"
    "buffer_load_dwordx4 a[24:27], v8, s[72:75], s81 offen offset:2048 sc1\n"
    "v_mfma_f32_16x16x32_bf16 v[232:235], v[120:123], v[56:59], v[232:235]\n"
    "v_mfma_f32_16x16x32_bf16 v[236:239], v[120:123], v[60:63], v[236:239]\n"
    "v_mfma_f32_16x16x32_bf16 v[240:243], v[124:127], v[48:51], v[240:243]\n"
    "v_mfma_f32_16x16x32_bf16 v[244:247], v[124:127], v[52:55], v[244:247]\n"
    "buffer_load_dwordx4 a[28:31], v8, s[72:75], s81 offen offset:3072 sc1\n"
    "v_mfma_f32_16x16x32_bf16 v[248:251], v[124:127], v[56:59], v[248:251]\n"
    "v_mfma_f32_16x16x32_bf16 v[252:255], v[124:127], v[60:63], v[252:255]\n"
    "s_waitcnt lgkmcnt(0)\n"
    "v_mfma_f32_16x16x32_bf16 v[128:131], v[64:67], v[32:35], v[128:131]\n"
    "ds_read_b128 v[48:51], v27 offset:0\n"
    "v_mfma_f32_16x16x32_bf16 v[132:135], v[64:67], v[36:39], v[132:135]\n"
    "ds_read_b128 v[52:55], v27 offset:2048\n"
    "buffer_load_dwordx4 a[32:35], v9, s[72:75], s80 offen offset:0 sc1\n"
    "v_mfma_f32_16x16x32_bf16 v[136:139], v[64:67], v[40:43], v[136:139]\n"
    "ds_read_b128 v[56:59], v27 offset:4096\n"
    "s_add_u32 s65, s65, 1\n"
    "v_mfma_f32_16x16x32_bf16 v[140:143], v[64:67], v[44:47], v[140:143]\n"
    "ds_read_b128 v[60:63], v27 offset:6144\n"
    "v_mfma_f32_16x16x32_bf16 v[144:147], v[68:71], v[32:35], v[144:147]\n"
    "ds_read_b64_tr_b16 v[96:97], v28 offset:4096\n"
    "v_mfma_f32_16x16x32_bf16 v[148:151], v[68:71], v[36:39], v[148:151]\n"
    "ds_read_b64_tr_b16 v[98:99], v28 offset:4608\n"
    "buffer_load_dwordx4 a[36:39], v9, s[72:75], s80 offen offset:1024 sc1\n"
    "v_mfma_f32_16x16x32_bf16 v[152:155], v[68:71], v[40:43], v[152:155]\n"
    "ds_read_b64_tr_b16 v[100:101], v29 offset:4096\n"
    "v_mfma_f32_16x16x32_bf16 v[156:159], v[68:71], v[44:47], v[156:159]\n"
    "ds_read_b64_tr_b16 v[102:103], v29 offset:4608\n"
    "s_cmp_eq_u32 s65, 3\n"
    "v_mfma_f32_16x16x32_bf16 v[160:163], v[72:75], v[32:35], v[160:163]\n"
    "ds_read_b64_tr_b16 v[104:105], v30 offset:4096\n"
    "s_cselect_b32 s65, 0, s65\n"
    "v_mfma_f32_16x16x32_bf16 v[164:167], v[72:75], v[36:39], v[164:167]\n"
    "ds_read_b64_tr_b16 v[106:107], v30 offset:4608\n"
    "buffer_load_dwordx4 a[40:43], v9, s[72:75], s80 offen offset:2048 sc1\n"
    "s_cselect_b32 s59, s68, 49152\n"
    "v_mfma_f32_16x16x32_bf16 v[168:171], v[72:75], v[40:43], v[168:171]\n"
    "ds_read_b64_tr_b16 v[108:109], v31 offset:4096\n"
    "v_mfma_f32_16x16x32_bf16 v[172:175], v[72:75], v[44:47], v[172:175]\n"
    "ds_read_b64_tr_b16 v[110:111], v31 offset:4608\n"
    "v_mfma_f32_16x16x32_bf16 v[176:179], v[76:79], v[32:35], v[176:179]\n"
    "ds_read_b64_tr_b16 v[112:113], v28 offset:12288\n"
    "v_mfma_f32_16x16x32_bf16 v[180:183], v[76:79], v[36:39], v[180:183]\n"
    "ds_read_b64_tr_b16 v[114:115], v28 offset:12800\n"
    "buffer_load_dwordx4 a[44:47], v9, s[72:75], s80 offen offset:3072 sc1\n"
    "v_mfma_f32_16x16x32_bf16 v[184:187], v[76:79], v[40:43], v[184:187]\n"
    "ds_read_b64_tr_b16 v[116:117], v29 offset:12288\n"
    "v_mfma_f32_16x16x32_bf16 v[188:191], v[76:79], v[44:47], v[188:191]\n"
    "ds_read_b64_tr_b16 v[118:119], v29 offset:12800\n"
    "v_mfma_f32_16x16x32_bf16 v[192:195], v[80:83], v[32:35], v[192:195]\n"
    "ds_read_b64_tr_b16 v[120:121], v30 offset:12288\n"
    "v_mfma_f32_16x16x32_bf16 v[196:199], v[80:83], v[36:39], v[196:199]\n"
    "ds_read_b64_tr_b16 v[122:123], v30 offset:12800\n"
    "buffer_load_dwordx4 a[48:51], v9, s[72:75], s81 offen offset:0 sc1\n"
    "v_mfma_f32_16x16x32_bf16 v[200:203], v[80:83], v[40:43], v[200:203]\n"
    "ds_read_b64_tr_b16 v[124:125], v31 offset:12288\n"
    "v_mfma_f32_16x16x32_bf16 v[204:207], v[80:83], v[44:47], v[204:207]\n"
    "ds_read_b64_tr_b16 v[126:127], v31 offset:12800\n"
    "v_mfma_f32_16x16x32_bf16 v[208:211], v[84:87], v[32:35], v[208:211]\n"
    "v_add_u32 v26, s59, v26\n"
    "v_mfma_f32_16x16x32_bf16 v[212:215], v[84:87], v[36:39], v[212:215]\n"
    "v_add_u32 v27, s59, v27\n"
    "buffer_load_dwordx4 a[52:55], v9, s[72:75], s81 offen offset:1024 sc1\n"
    "v_mfma_f32_16x16x32_bf16 v[216:219], v[84:87], v[40:43], v[216:219]\n"
    "v_add_u32 v28, s59, v28\n"
    "v_mfma_f32_16x16x32_bf16 v[220:223], v[84:87], v[44:47], v[220:223]\n"
    "v_add_u32 v29, s59, v29\n"
    "v_mfma_f32_16x16x32_bf16 v[224:227], v[88:91], v[32:35], v[224:227]\n"
    "v_add_u32 v30, s59, v30\n"
    "v_mfma_f32_16x16x32_bf16 v[228:231], v[88:91], v[36:39], v[228:231]\n"
    "v_add_u32 v31, s59, v31\n"
    "buffer_load_dwordx4 a[56:59], v9, s[72:75], s81 offen offset:2048 sc1\n"
    "v_mfma_f32_16x16x32_bf16 v[232:235], v[88:91], v[40:43], v[232:235]\n"
    "v_mfma_f32_16x16x32_bf16 v[236:239], v[88:91], v[44:47], v[236:239]\n"
    "v_mfma_f32_16x16x32_bf16 v[240:243], v[92:95], v[32:35], v[240:243]\n"
    "v_mfma_f32_16x16x32_bf16 v[244:247], v[92:95], v[36:39], v[244:247]\n"
    "buffer_load_dwordx4 a[60:63], v9, s[72:75], s81 offen offset:3072 sc1\n"
    "v_mfma_f32_16x16x32_bf16 v[248:251], v[92:95], v[40:43], v[248:251]\n"
    "v_mfma_f32_16x16x32_bf16 v[252:255], v[92:95], v[44:47], v[252:255]\n"
    "s_waitcnt vmcnt(16) lgkmcnt(0)\n"
    "s_barrier\n"
    "v_mfma_f32_16x16x32_bf16 v[128:131], v[96:99], v[48:51], v[128:131]\n"
    "ds_read_b128 v[32:35], v26 offset:0\n"
    "v_mfma_f32_16x16x32_bf16 v[132:135], v[96:99], v[52:55], v[132:135]\n"
    "ds_read_b128 v[36:39], v26 offset:2048\n"
    "buffer_load_dwordx4 a[64:67], v8, s[72:75], s82 offen offset:0 sc1\n"
    "v_mfma_f32_16x16x32_bf16 v[136:139], v[96:99], v[56:59], v[136:139]\n"
    "ds_read_b128 v[40:43], v26 offset:4096\n"
    "v_mfma_f32_16x16x32_bf16 v[140:143], v[96:99], v[60:63], v[140:143]\n"
    "ds_read_b128 v[44:47], v26 offset:6144\n"
    "v_mfma_f32_16x16x32_bf16 v[144:147], v[100:103], v[48:51], v[144:147]\n"
    "ds_read_b64_tr_b16 v[64:65], v28 offset:0\n"
    "v_mfma_f32_16x16x32_bf16 v[148:151], v[100:103], v[52:55], v[148:151]\n"
    "ds_read_b64_tr_b16 v[66:67], v28 offset:512\n"
    "buffer_load_dwordx4 a[68:71], v8, s[72:75], s82 offen offset:1024 sc1\n"
    "v_mfma_f32_16x16x32_bf16 v[152:155], v[100:103], v[56:59], v[152:155]\n"
    "ds_read_b64_tr_b16 v[68:69], v29 offset:0\n"
    "v_mfma_f32_16x16x32_bf16 v[156:159], v[100:103], v[60:63], v[156:159]\n"
    "ds_read_b64_tr_b16 v[70:71], v29 offset:512\n"
    "v_mfma_f32_16x16x32_bf16 v[160:163], v[104:107], v[48:51], v[160:163]\n"
    "ds_read_b64_tr_b16 v[72:73], v30 offset:0\n"
    "v_mfma_f32_16x16x32_bf16 v[164:167], v[104:107], v[52:55], v[164:167]\n"
    "ds_read_b64_tr_b16 v[74:75], v30 offset:512\n"
    "buffer_load_dwordx4 a[72:75], v8, s[72:75], s82 offen offset:2048 sc1\n"
    "v_mfma_f32_16x16x32_bf16 v[168:171], v[104:107], v[56:59], v[168:171]\n"
    "ds_read_b64_tr_b16 v[76:77], v31 offset:0\n"
    "v_mfma_f32_16x16x32_bf16 v[172:175], v[104:107], v[60:63], v[172:175]\n"
    "ds_read_b64_tr_b16 v[78:79], v31 offset:512\n"
    "v_mfma_f32_16x16x32_bf16 v[176:179], v[108:111], v[48:51], v[176:179]\n"
    "ds_read_b64_tr_b16 v[80:81], v28 offset:8192\n"
    "v_mfma_f32_16x16x32_bf16 v[180:183], v[108:111], v[52:55], v[180:183]\n"
    "ds_read_b64_tr_b16 v[82:83], v28 offset:8704\n"
    "buffer_load_dwordx4 a[76:79], v8, s[72:75], s82 offen offset:3072 sc1\n"
    "v_mfma_f32_16x16x32_bf16 v[184:187], v[108:111], v[56:59], v[184:187]\n"
    "ds_read_b64_tr_b16 v[84:85], v29 offset:8192\n"
    "v_mfma_f32_16x16x32_bf16 v[188:191], v[108:111], v[60:63], v[188:191]\n"
    "ds_read_b64_tr_b16 v[86:87], v29 offset:8704\n"
    "v_mfma_f32_16x16x32_bf16 v[192:195], v[112:115], v[48:51], v[192:195]\n"
    "ds_read_b64_tr_b16 v[88:89], v30 offset:8192\n"
    "v_mfma_f32_16x16x32_bf16 v[196:199], v[112:115], v[52:55], v[196:199]\n"
    "ds_read_b64_tr_b16 v[90:91], v30 offset:8704\n"
    "buffer_load_dwordx4 a[80:83], v8, s[72:75], s83 offen offset:0 sc1\n"
    "v_mfma_f32_16x16x32_bf16 v[200:203], v[112:115], v[56:59], v[200:203]\n"
    "ds_read_b64_tr_b16 v[92:93], v31 offset:8192\n"
    "v_mfma_f32_16x16x32_bf16 v[204:207], v[112:115], v[60:63], v[204:207]\n"
    "ds_read_b64_tr_b16 v[94:95], v31 offset:8704\n"
    "v_mfma_f32_16x16x32_bf16 v[208:211], v[116:119], v[48:51], v[208:211]\n"
    "v_mfma_f32_16x16x32_bf16 v[212:215], v[116:119], v[52:55], v[212:215]\n"
    "buffer_load_dwordx4 a[84:87], v8, s[72:75], s83 offen offset:1024 sc1\n"
    "v_mfma_f32_16x16x32_bf16 v[216:219], v[116:119], v[56:59], v[216:219]\n"
    "v_mfma_f32_16x16x32_bf16 v[220:223], v[116:119], v[60:63], v[220:223]\n"
    "v_mfma_f32_16x16x32_bf16 v[224:227], v[120:123], v[48:51], v[224:227]\n"
    "v_mfma_f32_16x16x32_bf16 v[228:231], v[120:123], v[52:55], v[228:231]\n"
    "buffer_load_dwordx4 a[88:91], v8, s[72:75], s83 offen offset:2048 sc1\n"
    "v_mfma_f32_16x16x32_bf16 v[232:235], v[120:123], v[56:59], v[232:235]\n"
    "v_mfma_f32_16x16x32_bf16 v[236:239], v[120:123], v[60:63], v[236:239]\n"
    "v_mfma_f32_16x16x32_bf16 v[240:243], v[124:127], v[48:51], v[240:243]\n"
    "v_mfma_f32_16x16x32_bf16 v[244:247], v[124:127], v[52:55], v[244:247]\n"
    "buffer_load_dwordx4 a[92:95], v8, s[72:75], s83 offen offset:3072 sc1\n"
    "v_mfma_f32_16x16x32_bf16 v[248:251], v[124:127], v[56:59], v[248:251]\n"
    "v_mfma_f32_16x16x32_bf16 v[252:255], v[124:127], v[60:63], v[252:255]\n"
    "s_waitcnt lgkmcnt(0)\n"
    "v_mfma_f32_16x16x32_bf16 v[128:131], v[64:67], v[32:35], v[128:131]\n"
    "ds_read_b128 v[48:51], v27 offset:0\n"
    "v_mfma_f32_16x16x32_bf16 v[132:135], v[64:67], v[36:39], v[132:135]\n"
    "ds_read_b128 v[52:55], v27 offset:2048\n"
    "buffer_load_dwordx4 a[96:99], v9, s[72:75], s82 offen offset:0 sc1\n"
    "v_mfma_f32_16x16x32_bf16 v[136:139], v[64:67], v[40:43], v[136:139]\n"
    "ds_read_b128 v[56:59], v27 offset:4096\n"
    "v_mfma_f32_16x16x32_bf16 v[140:143], v[64:67], v[44:47], v[140:143]\n"
    "ds_read_b128 v[60:63], v27 offset:6144\n"
    "v_mfma_f32_16x16x32_bf16 v[144:147], v[68:71], v[32:35], v[144:147]\n"
    "ds_read_b64_tr_b16 v[96:97], v28 offset:4096\n"
    "v_mfma_f32_16x16x32_bf16 v[148:151], v[68:71], v[36:39], v[148:151]\n"
    "ds_read_b64_tr_b16 v[98:99], v28 offset:4608\n"
    "buffer_load_dwordx4 a[100:103], v9, s[72:75], s82 offen offset:1024 sc1\n"
    "v_mfma_f32_16x16x32_bf16 v[152:155], v[68:71], v[40:43], v[152:155]\n"
    "ds_read_b64_tr_b16 v[100:101], v29 offset:4096\n"
    "v_mfma_f32_16x16x32_bf16 v[156:159], v[68:71], v[44:47], v[156:159]\n"
    "ds_read_b64_tr_b16 v[102:103], v29 offset:4608\n"
    "v_mfma_f32_16x16x32_bf16 v[160:163], v[72:75], v[32:35], v[160:163]\n"
    "ds_read_b64_tr_b16 v[104:105], v30 offset:4096\n"
    "v_mfma_f32_16x16x32_bf16 v[164:167], v[72:75], v[36:39], v[164:167]\n"
    "ds_read_b64_tr_b16 v[106:107], v30 offset:4608\n"
    "buffer_load_dwordx4 a[104:107], v9, s[72:75], s82 offen offset:2048 sc1\n"
    "v_mfma_f32_16x16x32_bf16 v[168:171], v[72:75], v[40:43], v[168:171]\n"
    "ds_read_b64_tr_b16 v[108:109], v31 offset:4096\n"
    "v_mfma_f32_16x16x32_bf16 v[172:175], v[72:75], v[44:47], v[172:175]\n"
    "ds_read_b64_tr_b16 v[110:111], v31 offset:4608\n"
    "v_mfma_f32_16x16x32_bf16 v[176:179], v[76:79], v[32:35], v[176:179]\n"
    "ds_read_b64_tr_b16 v[112:113], v28 offset:12288\n"
    "v_mfma_f32_16x16x32_bf16 v[180:183], v[76:79], v[36:39], v[180:183]\n"
    "ds_read_b64_tr_b16 v[114:115], v28 offset:12800\n"
    "buffer_load_dwordx4 a[108:111], v9, s[72:75], s82 offen offset:3072 sc1\n"
    "v_mfma_f32_16x16x32_bf16 v[184:187], v[76:79], v[40:43], v[184:187]\n"
    "ds_read_b64_tr_b16 v[116:117], v29 offset:12288\n"
    "v_mfma_f32_16x16x32_bf16 v[188:191], v[76:79], v[44:47], v[188:191]\n"
    "ds_read_b64_tr_b16 v[118:119], v29 offset:12800\n"
    "v_mfma_f32_16x16x32_bf16 v[192:195], v[80:83], v[32:35], v[192:195]\n"
    "ds_read_b64_tr_b16 v[120:121], v30 offset:12288\n"
    "v_mfma_f32_16x16x32_bf16 v[196:199], v[80:83], v[36:39], v[196:199]\n"
    "ds_read_b64_tr_b16 v[122:123], v30 offset:12800\n"
    "buffer_load_dwordx4 a[112:115], v9, s[72:75], s83 offen offset:0 sc1\n"
    "v_mfma_f32_16x16x32_bf16 v[200:203], v[80:83], v[40:43], v[200:203]\n"
    "ds_read_b64_tr_b16 v[124:125], v31 offset:12288\n"
    "v_mfma_f32_16x16x32_bf16 v[204:207], v[80:83], v[44:47], v[204:207]\n"
    "ds_read_b64_tr_b16 v[126:127], v31 offset:12800\n"
    "v_mfma_f32_16x16x32_bf16 v[208:211], v[84:87], v[32:35], v[208:211]\n"
    "v_mfma_f32_16x16x32_bf16 v[212:215], v[84:87], v[36:39], v[212:215]\n"
    "buffer_load_dwordx4 a[116:119], v9, s[72:75], s83 offen offset:1024 sc1\n"
    "v_mfma_f32_16x16x32_bf16 v[216:219], v[84:87], v[40:43], v[216:219]\n"
    "v_mfma_f32_16x16x32_bf16 v[220:223], v[84:87], v[44:47], v[220:223]\n"
    "v_mfma_f32_16x16x32_bf16 v[224:227], v[88:91], v[32:35], v[224:227]\n"
    "v_mfma_f32_16x16x32_bf16 v[228:231], v[88:91], v[36:39], v[228:231]\n"
    "buffer_load_dwordx4 a[120:123], v9, s[72:75], s83 offen offset:2048 sc1\n"
    "v_mfma_f32_16x16x32_bf16 v[232:235], v[88:91], v[40:43], v[232:235]\n"
    "v_mfma_f32_16x16x32_bf16 v[236:239], v[88:91], v[44:47], v[236:239]\n"
    "v_mfma_f32_16x16x32_bf16 v[240:243], v[92:95], v[32:35], v[240:243]\n"
    "v_mfma_f32_16x16x32_bf16 v[244:247], v[92:95], v[36:39], v[244:247]\n"
    "buffer_load_dwordx4 a[124:127], v9, s[72:75], s83 offen offset:3072 sc1\n"
    "v_mfma_f32_16x16x32_bf16 v[248:251], v[92:95], v[40:43], v[248:251]\n"
    "v_mfma_f32_16x16x32_bf16 v[252:255], v[92:95], v[44:47], v[252:255]\n"
    "s_waitcnt lgkmcnt(0)\n"
    "v_mfma_f32_16x16x32_bf16 v[128:131], v[96:99], v[48:51], v[128:131]\n"
    "v_mfma_f32_16x16x32_bf16 v[132:135], v[96:99], v[52:55], v[132:135]\n"
    "v_mfma_f32_16x16x32_bf16 v[136:139], v[96:99], v[56:59], v[136:139]\n"
    "v_mfma_f32_16x16x32_bf16 v[140:143], v[96:99], v[60:63], v[140:143]\n"
    "v_mfma_f32_16x16x32_bf16 v[144:147], v[100:103], v[48:51], v[144:147]\n"
    "v_mfma_f32_16x16x32_bf16 v[148:151], v[100:103], v[52:55], v[148:151]\n"
    "v_mfma_f32_16x16x32_bf16 v[152:155], v[100:103], v[56:59], v[152:155]\n"
    "v_mfma_f32_16x16x32_bf16 v[156:159], v[100:103], v[60:63], v[156:159]\n"
    "v_mfma_f32_16x16x32_bf16 v[160:163], v[104:107], v[48:51], v[160:163]\n"
    "v_mfma_f32_16x16x32_bf16 v[164:167], v[104:107], v[52:55], v[164:167]\n"
    "v_mfma_f32_16x16x32_bf16 v[168:171], v[104:107], v[56:59], v[168:171]\n"
    "v_mfma_f32_16x16x32_bf16 v[172:175], v[104:107], v[60:63], v[172:175]\n"
    "v_mfma_f32_16x16x32_bf16 v[176:179], v[108:111], v[48:51], v[176:179]\n"
    "v_mfma_f32_16x16x32_bf16 v[180:183], v[108:111], v[52:55], v[180:183]\n"
    "v_mfma_f32_16x16x32_bf16 v[184:187], v[108:111], v[56:59], v[184:187]\n"
    "v_mfma_f32_16x16x32_bf16 v[188:191], v[108:111], v[60:63], v[188:191]\n"
    "v_mfma_f32_16x16x32_bf16 v[192:195], v[112:115], v[48:51], v[192:195]\n"
    "v_mfma_f32_16x16x32_bf16 v[196:199], v[112:115], v[52:55], v[196:199]\n"
    "v_mfma_f32_16x16x32_bf16 v[200:203], v[112:115], v[56:59], v[200:203]\n"
    "v_mfma_f32_16x16x32_bf16 v[204:207], v[112:115], v[60:63], v[204:207]\n"
    "v_mfma_f32_16x16x32_bf16 v[208:211], v[116:119], v[48:51], v[208:211]\n"
    "v_mfma_f32_16x16x32_bf16 v[212:215], v[116:119], v[52:55], v[212:215]\n"
    "v_mfma_f32_16x16x32_bf16 v[216:219], v[116:119], v[56:59], v[216:219]\n"
    "v_mfma_f32_16x16x32_bf16 v[220:223], v[116:119], v[60:63], v[220:223]\n"
    "v_mfma_f32_16x16x32_bf16 v[224:227], v[120:123], v[48:51], v[224:227]\n"
    "v_mfma_f32_16x16x32_bf16 v[228:231], v[120:123], v[52:55], v[228:231]\n"
    "v_mfma_f32_16x16x32_bf16 v[232:235], v[120:123], v[56:59], v[232:235]\n"
    "v_mfma_f32_16x16x32_bf16 v[236:239], v[120:123], v[60:63], v[236:239]\n"
    "v_mfma_f32_16x16x32_bf16 v[240:243], v[124:127], v[48:51], v[240:243]\n"
    "v_mfma_f32_16x16x32_bf16 v[244:247], v[124:127], v[52:55], v[244:247]\n"
    "v_mfma_f32_16x16x32_bf16 v[248:251], v[124:127], v[56:59], v[248:251]\n"
    "v_mfma_f32_16x16x32_bf16 v[252:255], v[124:127], v[60:63], v[252:255]\n"
    "s_waitcnt vmcnt(0)\n"
    "s_barrier\n"
    "buffer_load_dword v10, v5, s[72:75], 0 offen sc1\n"
    "v_cvt_pk_bf16_f32 v32, v128, v129\n"
    "v_cvt_pk_bf16_f32 v33, v130, v131\n"
    "v_cvt_pk_bf16_f32 v34, v144, v145\n"
    "v_cvt_pk_bf16_f32 v35, v146, v147\n"
    "v_cvt_pk_bf16_f32 v36, v160, v161\n"
    "v_cvt_pk_bf16_f32 v37, v162, v163\n"
    "v_cvt_pk_bf16_f32 v38, v176, v177\n"
    "v_cvt_pk_bf16_f32 v39, v178, v179\n"
    "v_cvt_pk_bf16_f32 v40, v192, v193\n"
    "v_cvt_pk_bf16_f32 v41, v194, v195\n"
    "v_cvt_pk_bf16_f32 v42, v208, v209\n"
    "v_cvt_pk_bf16_f32 v43, v210, v211\n"
    "v_cvt_pk_bf16_f32 v44, v224, v225\n"
    "v_cvt_pk_bf16_f32 v45, v226, v227\n"
    "v_cvt_pk_bf16_f32 v46, v240, v241\n"
    "v_cvt_pk_bf16_f32 v47, v242, v243\n"
    "v_cvt_pk_bf16_f32 v48, v132, v133\n"
    "v_cvt_pk_bf16_f32 v49, v134, v135\n"
    "v_cvt_pk_bf16_f32 v50, v148, v149\n"
    "v_cvt_pk_bf16_f32 v51, v150, v151\n"
    "v_cvt_pk_bf16_f32 v52, v164, v165\n"
    "v_cvt_pk_bf16_f32 v53, v166, v167\n"
    "v_cvt_pk_bf16_f32 v54, v180, v181\n"
    "v_cvt_pk_bf16_f32 v55, v182, v183\n"
    "v_cvt_pk_bf16_f32 v56, v196, v197\n"
    "v_cvt_pk_bf16_f32 v57, v198, v199\n"
    "v_cvt_pk_bf16_f32 v58, v212, v213\n"
    "v_cvt_pk_bf16_f32 v59, v214, v215\n"
    "v_cvt_pk_bf16_f32 v60, v228, v229\n"
    "v_cvt_pk_bf16_f32 v61, v230, v231\n"
    "v_cvt_pk_bf16_f32 v62, v244, v245\n"
    "v_cvt_pk_bf16_f32 v63, v246, v247\n"
    "v_cvt_pk_bf16_f32 v64, v136, v137\n"
    "v_cvt_pk_bf16_f32 v65, v138, v139\n"
    "v_cvt_pk_bf16_f32 v66, v152, v153\n"
    "v_cvt_pk_bf16_f32 v67, v154, v155\n"
    "v_cvt_pk_bf16_f32 v68, v168, v169\n"
    "v_cvt_pk_bf16_f32 v69, v170, v171\n"
    "v_cvt_pk_bf16_f32 v70, v184, v185\n"
    "v_cvt_pk_bf16_f32 v71, v186, v187\n"
    "v_cvt_pk_bf16_f32 v72, v200, v201\n"
    "v_cvt_pk_bf16_f32 v73, v202, v203\n"
    "v_cvt_pk_bf16_f32 v74, v216, v217\n"
    "v_cvt_pk_bf16_f32 v75, v218, v219\n"
    "v_cvt_pk_bf16_f32 v76, v232, v233\n"
    "v_cvt_pk_bf16_f32 v77, v234, v235\n"
    "v_cvt_pk_bf16_f32 v78, v248, v249\n"
    "v_cvt_pk_bf16_f32 v79, v250, v251\n"
    "v_cvt_pk_bf16_f32 v80, v140, v141\n"
    "v_cvt_pk_bf16_f32 v81, v142, v143\n"
    "v_cvt_pk_bf16_f32 v82, v156, v157\n"
    "v_cvt_pk_bf16_f32 v83, v158, v159\n"
    "v_cvt_pk_bf16_f32 v84, v172, v173\n"
    "v_cvt_pk_bf16_f32 v85, v174, v175\n"
    "v_cvt_pk_bf16_f32 v86, v188, v189\n"
    "v_cvt_pk_bf16_f32 v87, v190, v191\n"
    "v_cvt_pk_bf16_f32 v88, v204, v205\n"
    "v_cvt_pk_bf16_f32 v89, v206, v207\n"
    "v_cvt_pk_bf16_f32 v90, v220, v221\n"
    "v_cvt_pk_bf16_f32 v91, v222, v223\n"
    "v_cvt_pk_bf16_f32 v92, v236, v237\n"
    "v_cvt_pk_bf16_f32 v93, v238, v239\n"
    "v_cvt_pk_bf16_f32 v94, v252, v253\n"
    "v_cvt_pk_bf16_f32 v95, v254, v255\n"
    "s_mov_b32 s70, 0x3e0293ee\n"
    "s_mov_b32 s79, 0\n"
    "v_mov_b32 v12, 0xd028965f\n"
    "v_mov_b32 v120, 0x3f803f80\n"
    "v_mov_b32 v121, 0x3f803f80\n"
    "v_mov_b32 v122, 0x3f803f80\n"
    "v_mov_b32 v123, 0x3f803f80\n"
    "v_cmp_ge_i32 s[84:85], 0, v7\n"
    "v_cmp_ge_i32 s[86:87], -1, v7\n"
    "v_cmp_ge_i32 s[88:89], -2, v7\n"
    "v_cmp_ge_i32 s[90:91], -3, v7\n"
    "v_mfma_f32_16x16x32_bf16 v[96:99], a[0:3], v[32:35], 0\n"
    "v_mfma_f32_16x16x32_bf16 v[100:103], a[0:3], v[48:51], 0\n"
    "v_mfma_f32_16x16x32_bf16 v[104:107], a[16:19], v[48:51], 0\n"
    "v_mfma_f32_16x16x32_bf16 v[96:99], a[4:7], v[36:39], v[96:99]\n"
    "v_mfma_f32_16x16x32_bf16 v[100:103], a[4:7], v[52:55], v[100:103]\n"
    "v_mfma_f32_16x16x32_bf16 v[104:107], a[20:23], v[52:55], v[104:107]\n"
    "v_mfma_f32_16x16x32_bf16 v[96:99], a[8:11], v[40:43], v[96:99]\n"
    "v_mfma_f32_16x16x32_bf16 v[100:103], a[8:11], v[56:59], v[100:103]\n"
    "v_mfma_f32_16x16x32_bf16 v[104:107], a[24:27], v[56:59], v[104:107]\n"
    "v_mfma_f32_16x16x32_bf16 v[96:99], a[12:15], v[44:47], v[96:99]\n"
    "v_mfma_f32_16x16x32_bf16 v[100:103], a[12:15], v[60:63], v[100:103]\n"
    "v_mfma_f32_16x16x32_bf16 v[104:107], a[28:31], v[60:63], v[104:107]\n"
    "v_mfma_f32_16x16x32_bf16 v[108:111], a[64:67], v[64:67], 0\n"
    "v_mfma_f32_16x16x32_bf16 v[112:115], a[64:67], v[80:83], 0\n"
    "v_mfma_f32_16x16x32_bf16 v[116:119], a[80:83], v[80:83], 0\n"
    "v_mfma_f32_16x16x32_bf16 v[108:111], a[68:71], v[68:71], v[108:111]\n"
    "v_mfma_f32_16x16x32_bf16 v[112:115], a[68:71], v[84:87], v[112:115]\n"
    "v_mfma_f32_16x16x32_bf16 v[116:119], a[84:87], v[84:87], v[116:119]\n"
    "v_mfma_f32_16x16x32_bf16 v[108:111], a[72:75], v[72:75], v[108:111]\n"
    "v_mfma_f32_16x16x32_bf16 v[112:115], a[72:75], v[88:91], v[112:115]\n"
    "v_mfma_f32_16x16x32_bf16 v[116:119], a[88:91], v[88:91], v[116:119]\n"
    "v_mfma_f32_16x16x32_bf16 v[108:111], a[76:79], v[76:79], v[108:111]\n"
    "v_mfma_f32_16x16x32_bf16 v[112:115], a[76:79], v[92:95], v[112:115]\n"
    "v_mfma_f32_16x16x32_bf16 v[116:119], a[92:95], v[92:95], v[116:119]\n"
    "v_cndmask_b32 v96, v12, v96, s[84:85]\n"
    "v_cndmask_b32 v97, v12, v97, s[86:87]\n"
    "v_cndmask_b32 v98, v12, v98, s[88:89]\n"
    "v_cndmask_b32 v99, v12, v99, s[90:91]\n"
    "v_cndmask_b32 v104, v12, v104, s[84:85]\n"
    "v_cndmask_b32 v105, v12, v105, s[86:87]\n"
    "v_cndmask_b32 v106, v12, v106, s[88:89]\n"
    "v_cndmask_b32 v107, v12, v107, s[90:91]\n"
    "v_cndmask_b32 v108, v12, v108, s[84:85]\n"
    "v_cndmask_b32 v109, v12, v109, s[86:87]\n"
    "v_cndmask_b32 v110, v12, v110, s[88:89]\n"
    "v_cndmask_b32 v111, v12, v111, s[90:91]\n"
    "v_cndmask_b32 v116, v12, v116, s[84:85]\n"
    "v_cndmask_b32 v117, v12, v117, s[86:87]\n"
    "v_cndmask_b32 v118, v12, v118, s[88:89]\n"
    "v_cndmask_b32 v119, v12, v119, s[90:91]\n"
    "v_mov_b64 v[202:203], 0\n"
    "v_mov_b64 v[208:209], 0\n"
    "v_mov_b64 v[212:213], 0\n"
    "v_mov_b64 v[214:215], 0\n"
    "v_mov_b64 v[218:219], 0\n"
    ".LGQ_pa%=:\n"
    "s_waitcnt vmcnt(0)\n"
    "v_readfirstlane_b32 s78, v10\n"
    "s_nop 0\n"
    "s_sub_u32 s78, s78, s76\n"
    "s_cmp_ge_i32 s78, 1\n"
    "s_cbranch_scc1 .LGQ_paok%=\n"
    "s_sleep 1\n"
    "buffer_load_dword v10, v5, s[72:75], 0 offen sc1\n"
    "s_add_u32 s79, s79, 1\n"
    "s_cmp_lt_u32 s79, 0x80000\n"
    "s_cbranch_scc1 .LGQ_pa%=\n"
    "v_mov_b32 v10, 1\n"
    "v_mov_b32 v11, 1572864\n"
    "buffer_store_dword v10, v11, s[72:75], 0 offen sc1\n"
    "s_waitcnt vmcnt(0)\n"
    "s_endpgm\n"
    ".LGQ_paok%=:\n"
    "s_add_u32 s77, s76, 0\n"
    "s_lshl_b32 s77, s77, 17\n"
    "buffer_load_dwordx4 v[128:131], v3, s[72:75], s77 offen offset:0 sc1\n"
    "buffer_load_dwordx4 v[132:135], v3, s[72:75], s77 offen offset:512 sc1\n"
    "buffer_load_dwordx4 v[136:139], v3, s[72:75], s77 offen offset:1024 sc1\n"
    "buffer_load_dwordx4 v[140:143], v3, s[72:75], s77 offen offset:1536 sc1\n"
    "buffer_load_dwordx4 v[160:163], v4, s[72:75], s77 offen offset:0 sc1\n"
    "buffer_load_dwordx4 v[164:167], v4, s[72:75], s77 offen offset:512 sc1\n"
    "buffer_load_dwordx4 v[168:171], v4, s[72:75], s77 offen offset:1024 sc1\n"
    "buffer_load_dwordx4 v[172:175], v4, s[72:75], s77 offen offset:1536 sc1\n"
    "s_cmp_ge_i32 s78, 3\n"
    "s_cselect_b32 s71, 1, 0\n"
    "s_cbranch_scc0 .LGQ_n2%=\n"
    "s_add_u32 s77, s76, 2\n"
    "s_lshl_b32 s77, s77, 17\n"
    "buffer_load_dwordx4 v[144:147], v3, s[72:75], s77 offen offset:0 sc1\n"
    "buffer_load_dwordx4 v[148:151], v3, s[72:75], s77 offen offset:512 sc1\n"
    "buffer_load_dwordx4 v[152:155], v3, s[72:75], s77 offen offset:1024 sc1\n"
    "buffer_load_dwordx4 v[156:159], v3, s[72:75], s77 offen offset:1536 sc1\n"
    "buffer_load_dwordx4 v[176:179], v4, s[72:75], s77 offen offset:0 sc1\n"
    "buffer_load_dwordx4 v[180:183], v4, s[72:75], s77 offen offset:512 sc1\n"
    "buffer_load_dwordx4 v[184:187], v4, s[72:75], s77 offen offset:1024 sc1\n"
    "buffer_load_dwordx4 v[188:191], v4, s[72:75], s77 offen offset:1536 sc1\n"
    ".LGQ_n2%=:\n"
    "s_waitcnt vmcnt(0)\n"
    "v_mfma_f32_16x16x32_bf16 v[192:195], v[128:131], v[32:35], 0\n"
    "v_mfma_f32_16x16x32_bf16 v[196:199], v[128:131], v[48:51], 0\n"
    "v_mfma_f32_16x16x32_bf16 v[192:195], v[132:135], v[36:39], v[192:195]\n"
    "v_mfma_f32_16x16x32_bf16 v[196:199], v[132:135], v[52:55], v[196:199]\n"
    "v_mfma_f32_16x16x32_bf16 v[192:195], v[136:139], v[40:43], v[192:195]\n"
    "v_mfma_f32_16x16x32_bf16 v[196:199], v[136:139], v[56:59], v[196:199]\n"
    "v_mfma_f32_16x16x32_bf16 v[192:195], v[140:143], v[44:47], v[192:195]\n"
    "v_mfma_f32_16x16x32_bf16 v[196:199], v[140:143], v[60:63], v[196:199]\n"
    "s_add_u32 s77, s76, 0\n"
    "s_lshl_b32 s80, s77, 18\n"
    "s_add_u32 s81, s80, 4096\n"
    "s_lshl_b32 s82, s77, 8\n"
    "v_max3_f32 v222, v100, v101, v102\n"
    "v_max3_f32 v222, v222, v104, v105\n"
    "v_max3_f32 v222, v222, v106, v107\n"
    "v_max3_f32 v220, v96, v97, v98\n"
    "v_max3_f32 v220, v220, v99, v192\n"
    "v_max3_f32 v220, v220, v193, v194\n"
    "v_max_f32 v221, v220, v195\n"
    "v_max_f32 v220, v220, v195\n"
    "v_max3_f32 v222, v222, v103, v196\n"
    "v_max3_f32 v222, v222, v197, v198\n"
    "v_max_f32 v223, v222, v199\n"
    "v_max_f32 v222, v222, v199\n"
    "v_permlane32_swap_b32 v220, v221\n"
    "s_nop 0\n"
    "v_permlane32_swap_b32 v222, v223\n"
    "v_max_f32 v220, v220, v221\n"
    "v_mov_b32 v221, v220\n"
    "v_max_f32 v222, v222, v223\n"
    "v_mov_b32 v223, v222\n"
    "v_permlane16_swap_b32 v220, v221\n"
    "s_nop 0\n"
    "v_permlane16_swap_b32 v222, v223\n"
    "v_max_f32 v220, v220, v221\n"
    "v_max_f32 v222, v222, v223\n"
    "v_mul_f32 v220, s70, v220\n"
    "v_add_f32 v221, 1.0, v220\n"
    "v_mul_f32 v222, s70, v222\n"
    "v_add_f32 v223, 1.0, v222\n"
    "v_fma_f32 v96, v96, s70, -v220\n"
    "v_fma_f32 v97, v97, s70, -v220\n"
    "v_fma_f32 v98, v98, s70, -v220\n"
    "v_fma_f32 v99, v99, s70, -v220\n"
    "v_fma_f32 v192, v192, s70, -v221\n"
    "v_fma_f32 v193, v193, s70, -v221\n"
    "v_fma_f32 v194, v194, s70, -v221\n"
    "v_fma_f32 v195, v195, s70, -v221\n"
    "v_fma_f32 v100, v100, s70, -v222\n"
    "v_fma_f32 v101, v101, s70, -v222\n"
    "v_fma_f32 v102, v102, s70, -v222\n"
    "v_fma_f32 v103, v103, s70, -v222\n"
    "v_fma_f32 v104, v104, s70, -v222\n"
    "v_fma_f32 v105, v105, s70, -v222\n"
    "v_fma_f32 v106, v106, s70, -v222\n"
    "v_fma_f32 v107, v107, s70, -v222\n"
    "v_fma_f32 v196, v196, s70, -v223\n"
    "v_fma_f32 v197, v197, s70, -v223\n"
    "v_fma_f32 v198, v198, s70, -v223\n"
    "v_fma_f32 v199, v199, s70, -v223\n"
    "v_exp_f32 v96, v96\n"
    "v_exp_f32 v97, v97\n"
    "v_exp_f32 v98, v98\n"
    "v_exp_f32 v99, v99\n"
    "v_exp_f32 v192, v192\n"
    "v_exp_f32 v193, v193\n"
    "v_exp_f32 v194, v194\n"
    "v_exp_f32 v195, v195\n"
    "v_cvt_pk_bf16_f32 v200, v96, v97\n"
    "v_cvt_pk_bf16_f32 v201, v98, v99\n"
    "v_cvt_pk_bf16_f32 v210, v192, v193\n"
    "v_cvt_pk_bf16_f32 v211, v194, v195\n"
    "v_exp_f32 v100, v100\n"
    "v_exp_f32 v101, v101\n"
    "v_exp_f32 v102, v102\n"
    "v_exp_f32 v103, v103\n"
    "v_exp_f32 v104, v104\n"
    "v_exp_f32 v105, v105\n"
    "v_exp_f32 v106, v106\n"
    "v_exp_f32 v107, v107\n"
    "v_exp_f32 v196, v196\n"
    "v_exp_f32 v197, v197\n"
    "v_exp_f32 v198, v198\n"
    "v_exp_f32 v199, v199\n"
    "v_cvt_pk_bf16_f32 v204, v100, v101\n"
    "v_cvt_pk_bf16_f32 v205, v102, v103\n"
    "v_cvt_pk_bf16_f32 v206, v104, v105\n"
    "v_cvt_pk_bf16_f32 v207, v106, v107\n"
    "v_cvt_pk_bf16_f32 v216, v196, v197\n"
    "v_cvt_pk_bf16_f32 v217, v198, v199\n"
    "v_mfma_f32_16x16x32_bf16 v[192:195], v[120:123], v[200:203], 0\n"
    "v_mfma_f32_16x16x32_bf16 v[192:195], v[120:123], v[210:213], v[192:195]\n"
    "v_mfma_f32_16x16x32_bf16 v[224:227], a[32:35], v[200:203], 0\n"
    "v_mfma_f32_16x16x32_bf16 v[228:231], a[36:39], v[200:203], 0\n"
    "v_mfma_f32_16x16x32_bf16 v[232:235], a[40:43], v[200:203], 0\n"
    "v_mfma_f32_16x16x32_bf16 v[236:239], a[44:47], v[200:203], 0\n"
    "v_mfma_f32_16x16x32_bf16 v[240:243], a[48:51], v[200:203], 0\n"
    "v_mfma_f32_16x16x32_bf16 v[244:247], a[52:55], v[200:203], 0\n"
    "v_mfma_f32_16x16x32_bf16 v[248:251], a[56:59], v[200:203], 0\n"
    "v_mfma_f32_16x16x32_bf16 v[252:255], a[60:63], v[200:203], 0\n"
    "v_rcp_f32 v11, v192\n"
    "v_mfma_f32_16x16x32_bf16 v[224:227], v[160:163], v[210:213], v[224:227]\n"
    "v_mfma_f32_16x16x32_bf16 v[228:231], v[160:163], v[208:211], v[228:231]\n"
    "v_mfma_f32_16x16x32_bf16 v[232:235], v[164:167], v[210:213], v[232:235]\n"
    "v_mfma_f32_16x16x32_bf16 v[236:239], v[164:167], v[208:211], v[236:239]\n"
    "v_mfma_f32_16x16x32_bf16 v[240:243], v[168:171], v[210:213], v[240:243]\n"
    "v_mfma_f32_16x16x32_bf16 v[244:247], v[168:171], v[208:211], v[244:247]\n"
    "v_mfma_f32_16x16x32_bf16 v[248:251], v[172:175], v[210:213], v[248:251]\n"
    "v_mfma_f32_16x16x32_bf16 v[252:255], v[172:175], v[208:211], v[252:255]\n"
    "v_mul_f32 v224, v224, v11\n"
    "v_mul_f32 v225, v225, v11\n"
    "v_mul_f32 v226, v226, v11\n"
    "v_mul_f32 v227, v227, v11\n"
    "v_mul_f32 v228, v228, v11\n"
    "v_mul_f32 v229, v229, v11\n"
    "v_mul_f32 v230, v230, v11\n"
    "v_mul_f32 v231, v231, v11\n"
    "v_cvt_pk_bf16_f32 v224, v224, v225\n"
    "v_cvt_pk_bf16_f32 v225, v226, v227\n"
    "v_cvt_pk_bf16_f32 v226, v228, v229\n"
    "v_cvt_pk_bf16_f32 v227, v230, v231\n"
    "buffer_store_dwordx4 v[224:227], v2, s[72:75], s80 offen offset:0 sc1\n"
    "v_mul_f32 v232, v232, v11\n"
    "v_mul_f32 v233, v233, v11\n"
    "v_mul_f32 v234, v234, v11\n"
    "v_mul_f32 v235, v235, v11\n"
    "v_mul_f32 v236, v236, v11\n"
    "v_mul_f32 v237, v237, v11\n"
    "v_mul_f32 v238, v238, v11\n"
    "v_mul_f32 v239, v239, v11\n"
    "v_cvt_pk_bf16_f32 v232, v232, v233\n"
    "v_cvt_pk_bf16_f32 v233, v234, v235\n"
    "v_cvt_pk_bf16_f32 v234, v236, v237\n"
    "v_cvt_pk_bf16_f32 v235, v238, v239\n"
    "buffer_store_dwordx4 v[232:235], v2, s[72:75], s80 offen offset:2048 sc1\n"
    "v_mul_f32 v240, v240, v11\n"
    "v_mul_f32 v241, v241, v11\n"
    "v_mul_f32 v242, v242, v11\n"
    "v_mul_f32 v243, v243, v11\n"
    "v_mul_f32 v244, v244, v11\n"
    "v_mul_f32 v245, v245, v11\n"
    "v_mul_f32 v246, v246, v11\n"
    "v_mul_f32 v247, v247, v11\n"
    "v_cvt_pk_bf16_f32 v240, v240, v241\n"
    "v_cvt_pk_bf16_f32 v241, v242, v243\n"
    "v_cvt_pk_bf16_f32 v242, v244, v245\n"
    "v_cvt_pk_bf16_f32 v243, v246, v247\n"
    "buffer_store_dwordx4 v[240:243], v2, s[72:75], s81 offen offset:0 sc1\n"
    "v_mul_f32 v248, v248, v11\n"
    "v_mul_f32 v249, v249, v11\n"
    "v_mul_f32 v250, v250, v11\n"
    "v_mul_f32 v251, v251, v11\n"
    "v_mul_f32 v252, v252, v11\n"
    "v_mul_f32 v253, v253, v11\n"
    "v_mul_f32 v254, v254, v11\n"
    "v_mul_f32 v255, v255, v11\n"
    "v_cvt_pk_bf16_f32 v248, v248, v249\n"
    "v_cvt_pk_bf16_f32 v249, v250, v251\n"
    "v_cvt_pk_bf16_f32 v250, v252, v253\n"
    "v_cvt_pk_bf16_f32 v251, v254, v255\n"
    "buffer_store_dwordx4 v[248:251], v2, s[72:75], s81 offen offset:2048 sc1\n"
    "v_mfma_f32_16x16x32_bf16 v[196:199], v[120:123], v[204:207], 0\n"
    "v_mfma_f32_16x16x32_bf16 v[196:199], v[120:123], v[216:219], v[196:199]\n"
    "v_mfma_f32_16x16x32_bf16 v[224:227], a[32:35], v[204:207], 0\n"
    "v_mfma_f32_16x16x32_bf16 v[228:231], a[36:39], v[204:207], 0\n"
    "v_mfma_f32_16x16x32_bf16 v[232:235], a[40:43], v[204:207], 0\n"
    "v_mfma_f32_16x16x32_bf16 v[236:239], a[44:47], v[204:207], 0\n"
    "v_mfma_f32_16x16x32_bf16 v[240:243], a[48:51], v[204:207], 0\n"
    "v_mfma_f32_16x16x32_bf16 v[244:247], a[52:55], v[204:207], 0\n"
    "v_mfma_f32_16x16x32_bf16 v[248:251], a[56:59], v[204:207], 0\n"
    "v_mfma_f32_16x16x32_bf16 v[252:255], a[60:63], v[204:207], 0\n"
    "v_rcp_f32 v11, v196\n"
    "v_mfma_f32_16x16x32_bf16 v[224:227], v[160:163], v[216:219], v[224:227]\n"
    "v_mfma_f32_16x16x32_bf16 v[228:231], v[160:163], v[214:217], v[228:231]\n"
    "v_mfma_f32_16x16x32_bf16 v[232:235], v[164:167], v[216:219], v[232:235]\n"
    "v_mfma_f32_16x16x32_bf16 v[236:239], v[164:167], v[214:217], v[236:239]\n"
    "v_mfma_f32_16x16x32_bf16 v[240:243], v[168:171], v[216:219], v[240:243]\n"
    "v_mfma_f32_16x16x32_bf16 v[244:247], v[168:171], v[214:217], v[244:247]\n"
    "v_mfma_f32_16x16x32_bf16 v[248:251], v[172:175], v[216:219], v[248:251]\n"
    "v_mfma_f32_16x16x32_bf16 v[252:255], v[172:175], v[214:217], v[252:255]\n"
    "v_mul_f32 v224, v224, v11\n"
    "v_mul_f32 v225, v225, v11\n"
    "v_mul_f32 v226, v226, v11\n"
    "v_mul_f32 v227, v227, v11\n"
    "v_mul_f32 v228, v228, v11\n"
    "v_mul_f32 v229, v229, v11\n"
    "v_mul_f32 v230, v230, v11\n"
    "v_mul_f32 v231, v231, v11\n"
    "v_cvt_pk_bf16_f32 v224, v224, v225\n"
    "v_cvt_pk_bf16_f32 v225, v226, v227\n"
    "v_cvt_pk_bf16_f32 v226, v228, v229\n"
    "v_cvt_pk_bf16_f32 v227, v230, v231\n"
    "buffer_store_dwordx4 v[224:227], v2, s[72:75], s80 offen offset:1024 sc1\n"
    "v_mul_f32 v232, v232, v11\n"
    "v_mul_f32 v233, v233, v11\n"
    "v_mul_f32 v234, v234, v11\n"
    "v_mul_f32 v235, v235, v11\n"
    "v_mul_f32 v236, v236, v11\n"
    "v_mul_f32 v237, v237, v11\n"
    "v_mul_f32 v238, v238, v11\n"
    "v_mul_f32 v239, v239, v11\n"
    "v_cvt_pk_bf16_f32 v232, v232, v233\n"
    "v_cvt_pk_bf16_f32 v233, v234, v235\n"
    "v_cvt_pk_bf16_f32 v234, v236, v237\n"
    "v_cvt_pk_bf16_f32 v235, v238, v239\n"
    "buffer_store_dwordx4 v[232:235], v2, s[72:75], s80 offen offset:3072 sc1\n"
    "v_mul_f32 v240, v240, v11\n"
    "v_mul_f32 v241, v241, v11\n"
    "v_mul_f32 v242, v242, v11\n"
    "v_mul_f32 v243, v243, v11\n"
    "v_mul_f32 v244, v244, v11\n"
    "v_mul_f32 v245, v245, v11\n"
    "v_mul_f32 v246, v246, v11\n"
    "v_mul_f32 v247, v247, v11\n"
    "v_cvt_pk_bf16_f32 v240, v240, v241\n"
    "v_cvt_pk_bf16_f32 v241, v242, v243\n"
    "v_cvt_pk_bf16_f32 v242, v244, v245\n"
    "v_cvt_pk_bf16_f32 v243, v246, v247\n"
    "buffer_store_dwordx4 v[240:243], v2, s[72:75], s81 offen offset:1024 sc1\n"
    "v_mul_f32 v248, v248, v11\n"
    "v_mul_f32 v249, v249, v11\n"
    "v_mul_f32 v250, v250, v11\n"
    "v_mul_f32 v251, v251, v11\n"
    "v_mul_f32 v252, v252, v11\n"
    "v_mul_f32 v253, v253, v11\n"
    "v_mul_f32 v254, v254, v11\n"
    "v_mul_f32 v255, v255, v11\n"
    "v_cvt_pk_bf16_f32 v248, v248, v249\n"
    "v_cvt_pk_bf16_f32 v249, v250, v251\n"
    "v_cvt_pk_bf16_f32 v250, v252, v253\n"
    "v_cvt_pk_bf16_f32 v251, v254, v255\n"
    "buffer_store_dwordx4 v[248:251], v2, s[72:75], s81 offen offset:3072 sc1\n"
    "s_cmp_eq_u32 s71, 1\n"
    "s_cbranch_scc1 .LGQ_h2%=\n"
    "v_mov_b32 v11, 1\n"
    "s_waitcnt vmcnt(0)\n"
    "buffer_atomic_add v11, v6, s[72:75], s82 offen\n"
    "buffer_load_dword v10, v5, s[72:75], 0 offen sc1\n"
    ".LGQ_pb%=:\n"
    "s_waitcnt vmcnt(0)\n"
    "v_readfirstlane_b32 s78, v10\n"
    "s_nop 0\n"
    "s_sub_u32 s78, s78, s76\n"
    "s_cmp_ge_i32 s78, 3\n"
    "s_cbranch_scc1 .LGQ_pbok%=\n"
    "s_sleep 1\n"
    "buffer_load_dword v10, v5, s[72:75], 0 offen sc1\n"
    "s_add_u32 s79, s79, 1\n"
    "s_cmp_lt_u32 s79, 0x80000\n"
    "s_cbranch_scc1 .LGQ_pb%=\n"
    "v_mov_b32 v10, 1\n"
    "v_mov_b32 v11, 1572864\n"
    "buffer_store_dword v10, v11, s[72:75], 0 offen sc1\n"
    "s_waitcnt vmcnt(0)\n"
    "s_endpgm\n"
    ".LGQ_pbok%=:\n"
    "s_add_u32 s77, s76, 2\n"
    "s_lshl_b32 s77, s77, 17\n"
    "buffer_load_dwordx4 v[144:147], v3, s[72:75], s77 offen offset:0 sc1\n"
    "buffer_load_dwordx4 v[148:151], v3, s[72:75], s77 offen offset:512 sc1\n"
    "buffer_load_dwordx4 v[152:155], v3, s[72:75], s77 offen offset:1024 sc1\n"
    "buffer_load_dwordx4 v[156:159], v3, s[72:75], s77 offen offset:1536 sc1\n"
    "buffer_load_dwordx4 v[176:179], v4, s[72:75], s77 offen offset:0 sc1\n"
    "buffer_load_dwordx4 v[180:183], v4, s[72:75], s77 offen offset:512 sc1\n"
    "buffer_load_dwordx4 v[184:187], v4, s[72:75], s77 offen offset:1024 sc1\n"
    "buffer_load_dwordx4 v[188:191], v4, s[72:75], s77 offen offset:1536 sc1\n"
    "s_waitcnt vmcnt(0)\n"
    "s_mov_b32 s83, 0x80000000\n"
    "s_branch .LGQ_g1%=\n"
    ".LGQ_h2%=:\n"
    "s_mov_b32 s83, s82\n"
    "s_nop 1\n"
    ".LGQ_g1%=:\n"
    "v_mfma_f32_16x16x32_bf16 v[192:195], v[144:147], v[64:67], 0\n"
    "v_mfma_f32_16x16x32_bf16 v[196:199], v[144:147], v[80:83], 0\n"
    "v_mfma_f32_16x16x32_bf16 v[192:195], v[148:151], v[68:71], v[192:195]\n"
    "v_mfma_f32_16x16x32_bf16 v[196:199], v[148:151], v[84:87], v[196:199]\n"
    "v_mfma_f32_16x16x32_bf16 v[192:195], v[152:155], v[72:75], v[192:195]\n"
    "v_mfma_f32_16x16x32_bf16 v[196:199], v[152:155], v[88:91], v[196:199]\n"
    "v_mfma_f32_16x16x32_bf16 v[192:195], v[156:159], v[76:79], v[192:195]\n"
    "v_mfma_f32_16x16x32_bf16 v[196:199], v[156:159], v[92:95], v[196:199]\n"
    "s_add_u32 s77, s76, 2\n"
    "s_lshl_b32 s80, s77, 18\n"
    "s_add_u32 s81, s80, 4096\n"
    "s_lshl_b32 s82, s77, 8\n"
    "v_max3_f32 v222, v112, v113, v114\n"
    "v_max3_f32 v222, v222, v116, v117\n"
    "v_max3_f32 v222, v222, v118, v119\n"
    "v_max3_f32 v220, v108, v109, v110\n"
    "v_max3_f32 v220, v220, v111, v192\n"
    "v_max3_f32 v220, v220, v193, v194\n"
    "v_max_f32 v221, v220, v195\n"
    "v_max_f32 v220, v220, v195\n"
    "v_max3_f32 v222, v222, v115, v196\n"
    "v_max3_f32 v222, v222, v197, v198\n"
    "v_max_f32 v223, v222, v199\n"
    "v_max_f32 v222, v222, v199\n"
    "v_permlane32_swap_b32 v220, v221\n"
    "s_nop 0\n"
    "v_permlane32_swap_b32 v222, v223\n"
    "v_max_f32 v220, v220, v221\n"
    "v_mov_b32 v221, v220\n"
    "v_max_f32 v222, v222, v223\n"
    "v_mov_b32 v223, v222\n"
    "v_permlane16_swap_b32 v220, v221\n"
    "s_nop 0\n"
    "v_permlane16_swap_b32 v222, v223\n"
    "v_max_f32 v220, v220, v221\n"
    "v_max_f32 v222, v222, v223\n"
    "v_mul_f32 v220, s70, v220\n"
    "v_add_f32 v221, 1.0, v220\n"
    "v_mul_f32 v222, s70, v222\n"
    "v_add_f32 v223, 1.0, v222\n"
    "v_fma_f32 v108, v108, s70, -v220\n"
    "v_fma_f32 v109, v109, s70, -v220\n"
    "v_fma_f32 v110, v110, s70, -v220\n"
    "v_fma_f32 v111, v111, s70, -v220\n"
    "v_fma_f32 v192, v192, s70, -v221\n"
    "v_fma_f32 v193, v193, s70, -v221\n"
    "v_fma_f32 v194, v194, s70, -v221\n"
    "v_fma_f32 v195, v195, s70, -v221\n"
    "v_fma_f32 v112, v112, s70, -v222\n"
    "v_fma_f32 v113, v113, s70, -v222\n"
    "v_fma_f32 v114, v114, s70, -v222\n"
    "v_fma_f32 v115, v115, s70, -v222\n"
    "v_fma_f32 v116, v116, s70, -v222\n"
    "v_fma_f32 v117, v117, s70, -v222\n"
    "v_fma_f32 v118, v118, s70, -v222\n"
    "v_fma_f32 v119, v119, s70, -v222\n"
    "v_fma_f32 v196, v196, s70, -v223\n"
    "v_fma_f32 v197, v197, s70, -v223\n"
    "v_fma_f32 v198, v198, s70, -v223\n"
    "v_fma_f32 v199, v199, s70, -v223\n"
    "v_exp_f32 v108, v108\n"
    "v_exp_f32 v109, v109\n"
    "v_exp_f32 v110, v110\n"
    "v_exp_f32 v111, v111\n"
    "v_exp_f32 v192, v192\n"
    "v_exp_f32 v193, v193\n"
    "v_exp_f32 v194, v194\n"
    "v_exp_f32 v195, v195\n"
    "v_cvt_pk_bf16_f32 v200, v108, v109\n"
    "v_cvt_pk_bf16_f32 v201, v110, v111\n"
    "v_cvt_pk_bf16_f32 v210, v192, v193\n"
    "v_cvt_pk_bf16_f32 v211, v194, v195\n"
    "v_exp_f32 v112, v112\n"
    "v_exp_f32 v113, v113\n"
    "v_exp_f32 v114, v114\n"
    "v_exp_f32 v115, v115\n"
    "v_exp_f32 v116, v116\n"
    "v_exp_f32 v117, v117\n"
    "v_exp_f32 v118, v118\n"
    "v_exp_f32 v119, v119\n"
    "v_exp_f32 v196, v196\n"
    "v_exp_f32 v197, v197\n"
    "v_exp_f32 v198, v198\n"
    "v_exp_f32 v199, v199\n"
    "v_cvt_pk_bf16_f32 v204, v112, v113\n"
    "v_cvt_pk_bf16_f32 v205, v114, v115\n"
    "v_cvt_pk_bf16_f32 v206, v116, v117\n"
    "v_cvt_pk_bf16_f32 v207, v118, v119\n"
    "v_cvt_pk_bf16_f32 v216, v196, v197\n"
    "v_cvt_pk_bf16_f32 v217, v198, v199\n"
    "v_mfma_f32_16x16x32_bf16 v[192:195], v[120:123], v[200:203], 0\n"
    "v_mfma_f32_16x16x32_bf16 v[192:195], v[120:123], v[210:213], v[192:195]\n"
    "v_mfma_f32_16x16x32_bf16 v[224:227], a[96:99], v[200:203], 0\n"
    "v_mfma_f32_16x16x32_bf16 v[228:231], a[100:103], v[200:203], 0\n"
    "v_mfma_f32_16x16x32_bf16 v[232:235], a[104:107], v[200:203], 0\n"
    "v_mfma_f32_16x16x32_bf16 v[236:239], a[108:111], v[200:203], 0\n"
    "v_mfma_f32_16x16x32_bf16 v[240:243], a[112:115], v[200:203], 0\n"
    "v_mfma_f32_16x16x32_bf16 v[244:247], a[116:119], v[200:203], 0\n"
    "v_mfma_f32_16x16x32_bf16 v[248:251], a[120:123], v[200:203], 0\n"
    "v_mfma_f32_16x16x32_bf16 v[252:255], a[124:127], v[200:203], 0\n"
    "v_rcp_f32 v11, v192\n"
    "v_mfma_f32_16x16x32_bf16 v[224:227], v[176:179], v[210:213], v[224:227]\n"
    "v_mfma_f32_16x16x32_bf16 v[228:231], v[176:179], v[208:211], v[228:231]\n"
    "v_mfma_f32_16x16x32_bf16 v[232:235], v[180:183], v[210:213], v[232:235]\n"
    "v_mfma_f32_16x16x32_bf16 v[236:239], v[180:183], v[208:211], v[236:239]\n"
    "v_mfma_f32_16x16x32_bf16 v[240:243], v[184:187], v[210:213], v[240:243]\n"
    "v_mfma_f32_16x16x32_bf16 v[244:247], v[184:187], v[208:211], v[244:247]\n"
    "v_mfma_f32_16x16x32_bf16 v[248:251], v[188:191], v[210:213], v[248:251]\n"
    "v_mfma_f32_16x16x32_bf16 v[252:255], v[188:191], v[208:211], v[252:255]\n"
    "v_mul_f32 v224, v224, v11\n"
    "v_mul_f32 v225, v225, v11\n"
    "v_mul_f32 v226, v226, v11\n"
    "v_mul_f32 v227, v227, v11\n"
    "v_mul_f32 v228, v228, v11\n"
    "v_mul_f32 v229, v229, v11\n"
    "v_mul_f32 v230, v230, v11\n"
    "v_mul_f32 v231, v231, v11\n"
    "v_cvt_pk_bf16_f32 v224, v224, v225\n"
    "v_cvt_pk_bf16_f32 v225, v226, v227\n"
    "v_cvt_pk_bf16_f32 v226, v228, v229\n"
    "v_cvt_pk_bf16_f32 v227, v230, v231\n"
    "buffer_store_dwordx4 v[224:227], v2, s[72:75], s80 offen offset:0 sc1\n"
    "v_mul_f32 v232, v232, v11\n"
    "v_mul_f32 v233, v233, v11\n"
    "v_mul_f32 v234, v234, v11\n"
    "v_mul_f32 v235, v235, v11\n"
    "v_mul_f32 v236, v236, v11\n"
    "v_mul_f32 v237, v237, v11\n"
    "v_mul_f32 v238, v238, v11\n"
    "v_mul_f32 v239, v239, v11\n"
    "v_cvt_pk_bf16_f32 v232, v232, v233\n"
    "v_cvt_pk_bf16_f32 v233, v234, v235\n"
    "v_cvt_pk_bf16_f32 v234, v236, v237\n"
    "v_cvt_pk_bf16_f32 v235, v238, v239\n"
    "buffer_store_dwordx4 v[232:235], v2, s[72:75], s80 offen offset:2048 sc1\n"
    "v_mul_f32 v240, v240, v11\n"
    "v_mul_f32 v241, v241, v11\n"
    "v_mul_f32 v242, v242, v11\n"
    "v_mul_f32 v243, v243, v11\n"
    "v_mul_f32 v244, v244, v11\n"
    "v_mul_f32 v245, v245, v11\n"
    "v_mul_f32 v246, v246, v11\n"
    "v_mul_f32 v247, v247, v11\n"
    "v_cvt_pk_bf16_f32 v240, v240, v241\n"
    "v_cvt_pk_bf16_f32 v241, v242, v243\n"
    "v_cvt_pk_bf16_f32 v242, v244, v245\n"
    "v_cvt_pk_bf16_f32 v243, v246, v247\n"
    "buffer_store_dwordx4 v[240:243], v2, s[72:75], s81 offen offset:0 sc1\n"
    "v_mul_f32 v248, v248, v11\n"
    "v_mul_f32 v249, v249, v11\n"
    "v_mul_f32 v250, v250, v11\n"
    "v_mul_f32 v251, v251, v11\n"
    "v_mul_f32 v252, v252, v11\n"
    "v_mul_f32 v253, v253, v11\n"
    "v_mul_f32 v254, v254, v11\n"
    "v_mul_f32 v255, v255, v11\n"
    "v_cvt_pk_bf16_f32 v248, v248, v249\n"
    "v_cvt_pk_bf16_f32 v249, v250, v251\n"
    "v_cvt_pk_bf16_f32 v250, v252, v253\n"
    "v_cvt_pk_bf16_f32 v251, v254, v255\n"
    "buffer_store_dwordx4 v[248:251], v2, s[72:75], s81 offen offset:2048 sc1\n"
    "v_mfma_f32_16x16x32_bf16 v[196:199], v[120:123], v[204:207], 0\n"
    "v_mfma_f32_16x16x32_bf16 v[196:199], v[120:123], v[216:219], v[196:199]\n"
    "v_mfma_f32_16x16x32_bf16 v[224:227], a[96:99], v[204:207], 0\n"
    "v_mfma_f32_16x16x32_bf16 v[228:231], a[100:103], v[204:207], 0\n"
    "v_mfma_f32_16x16x32_bf16 v[232:235], a[104:107], v[204:207], 0\n"
    "v_mfma_f32_16x16x32_bf16 v[236:239], a[108:111], v[204:207], 0\n"
    "v_mfma_f32_16x16x32_bf16 v[240:243], a[112:115], v[204:207], 0\n"
    "v_mfma_f32_16x16x32_bf16 v[244:247], a[116:119], v[204:207], 0\n"
    "v_mfma_f32_16x16x32_bf16 v[248:251], a[120:123], v[204:207], 0\n"
    "v_mfma_f32_16x16x32_bf16 v[252:255], a[124:127], v[204:207], 0\n"
    "v_rcp_f32 v11, v196\n"
    "v_mfma_f32_16x16x32_bf16 v[224:227], v[176:179], v[216:219], v[224:227]\n"
    "v_mfma_f32_16x16x32_bf16 v[228:231], v[176:179], v[214:217], v[228:231]\n"
    "v_mfma_f32_16x16x32_bf16 v[232:235], v[180:183], v[216:219], v[232:235]\n"
    "v_mfma_f32_16x16x32_bf16 v[236:239], v[180:183], v[214:217], v[236:239]\n"
    "v_mfma_f32_16x16x32_bf16 v[240:243], v[184:187], v[216:219], v[240:243]\n"
    "v_mfma_f32_16x16x32_bf16 v[244:247], v[184:187], v[214:217], v[244:247]\n"
    "v_mfma_f32_16x16x32_bf16 v[248:251], v[188:191], v[216:219], v[248:251]\n"
    "v_mfma_f32_16x16x32_bf16 v[252:255], v[188:191], v[214:217], v[252:255]\n"
    "v_mul_f32 v224, v224, v11\n"
    "v_mul_f32 v225, v225, v11\n"
    "v_mul_f32 v226, v226, v11\n"
    "v_mul_f32 v227, v227, v11\n"
    "v_mul_f32 v228, v228, v11\n"
    "v_mul_f32 v229, v229, v11\n"
    "v_mul_f32 v230, v230, v11\n"
    "v_mul_f32 v231, v231, v11\n"
    "v_cvt_pk_bf16_f32 v224, v224, v225\n"
    "v_cvt_pk_bf16_f32 v225, v226, v227\n"
    "v_cvt_pk_bf16_f32 v226, v228, v229\n"
    "v_cvt_pk_bf16_f32 v227, v230, v231\n"
    "buffer_store_dwordx4 v[224:227], v2, s[72:75], s80 offen offset:1024 sc1\n"
    "v_mul_f32 v232, v232, v11\n"
    "v_mul_f32 v233, v233, v11\n"
    "v_mul_f32 v234, v234, v11\n"
    "v_mul_f32 v235, v235, v11\n"
    "v_mul_f32 v236, v236, v11\n"
    "v_mul_f32 v237, v237, v11\n"
    "v_mul_f32 v238, v238, v11\n"
    "v_mul_f32 v239, v239, v11\n"
    "v_cvt_pk_bf16_f32 v232, v232, v233\n"
    "v_cvt_pk_bf16_f32 v233, v234, v235\n"
    "v_cvt_pk_bf16_f32 v234, v236, v237\n"
    "v_cvt_pk_bf16_f32 v235, v238, v239\n"
    "buffer_store_dwordx4 v[232:235], v2, s[72:75], s80 offen offset:3072 sc1\n"
    "v_mul_f32 v240, v240, v11\n"
    "v_mul_f32 v241, v241, v11\n"
    "v_mul_f32 v242, v242, v11\n"
    "v_mul_f32 v243, v243, v11\n"
    "v_mul_f32 v244, v244, v11\n"
    "v_mul_f32 v245, v245, v11\n"
    "v_mul_f32 v246, v246, v11\n"
    "v_mul_f32 v247, v247, v11\n"
    "v_cvt_pk_bf16_f32 v240, v240, v241\n"
    "v_cvt_pk_bf16_f32 v241, v242, v243\n"
    "v_cvt_pk_bf16_f32 v242, v244, v245\n"
    "v_cvt_pk_bf16_f32 v243, v246, v247\n"
    "buffer_store_dwordx4 v[240:243], v2, s[72:75], s81 offen offset:1024 sc1\n"
    "v_mul_f32 v248, v248, v11\n"
    "v_mul_f32 v249, v249, v11\n"
    "v_mul_f32 v250, v250, v11\n"
    "v_mul_f32 v251, v251, v11\n"
    "v_mul_f32 v252, v252, v11\n"
    "v_mul_f32 v253, v253, v11\n"
    "v_mul_f32 v254, v254, v11\n"
    "v_mul_f32 v255, v255, v11\n"
    "v_cvt_pk_bf16_f32 v248, v248, v249\n"
    "v_cvt_pk_bf16_f32 v249, v250, v251\n"
    "v_cvt_pk_bf16_f32 v250, v252, v253\n"
    "v_cvt_pk_bf16_f32 v251, v254, v255\n"
    "buffer_store_dwordx4 v[248:251], v2, s[72:75], s81 offen offset:3072 sc1\n"
    "v_mov_b32 v11, 1\n"
    "s_waitcnt vmcnt(0)\n"
    "buffer_atomic_add v11, v6, s[72:75], s82 offen\n"
    "s_cmp_eq_u32 s83, 0x80000000\n"
    "s_cbranch_scc1 .LGQ_e%=\n"
    "buffer_atomic_add v11, v6, s[72:75], s83 offen\n"
    ".LGQ_e%=:\n"
    "s_nop 1\n"
    :
      "+{v[26:27]}"(ax), "+{v[28:31]}"(aw), "+{s[40:43]}"(rx),
      "+{s[44:47]}"(rw0), "+{s[48:51]}"(rw1)
    :
      "{v[14:17]}"(vox), "{v[18:21]}"(vow0), "{v[22:25]}"(vow1), "{s[52:53]}"(alt), "{s54}"(mx), "{s55}"(mw), "{s56}"(nt), "{s57}"(sw), "{s58}"(wstep), "{v[2:5]}"(av0), "{v[6:9]}"(av1), "{s[72:75]}"(rwsv), "{s76}"(seg0)
    :
      "memory", "scc", "vcc", "v10", "v11", "v12", "s70", "s71", "s77", "s78", "s79", "s80", "s81", "s82", "s83", "s84", "s85", "s86", "s87", "s88", "s89", "s90",
      "s91", "v128", "v129", "v130", "v131", "v132", "v133", "v134", "v135", "v136", "v137", "v138", "v139", "v140", "v141", "v142", "v143", "v144", "v145", "v146",
      "v147", "v148", "v149", "v150", "v151", "v152", "v153", "v154", "v155", "v156", "v157", "v158", "v159", "v160", "v161", "v162", "v163", "v164", "v165", "v166",
      "v167", "v168", "v169", "v170", "v171", "v172", "v173", "v174", "v175", "v176", "v177", "v178", "v179", "v180", "v181", "v182", "v183", "v184", "v185", "v186",
      "v187", "v188", "v189", "v190", "v191", "v192", "v193", "v194", "v195", "v196", "v197", "v198", "v199", "v200", "v201", "v202", "v203", "v204", "v205", "v206",
      "v207", "v208", "v209", "v210", "v211", "v212", "v213", "v214", "v215", "v216", "v217", "v218", "v219", "v220", "v221", "v222", "v223", "v224", "v225", "v226",
      "v227", "v228", "v229", "v230", "v231", "v232", "v233", "v234", "v235", "v236", "v237", "v238", "v239", "v240", "v241", "v242", "v243", "v244", "v245", "v246",
      "v247", "v248", "v249", "v250", "v251", "v252", "v253", "v254", "v255", "a0", "a1", "a2", "a3", "a4", "a5", "a6", "a7", "a8", "a9", "a10",
      "a11", "a12", "a13", "a14", "a15", "a16", "a17", "a18", "a19", "a20", "a21", "a22", "a23", "a24", "a25", "a26", "a27", "a28", "a29", "a30",
      "a31", "a32", "a33", "a34", "a35", "a36", "a37", "a38", "a39", "a40", "a41", "a42", "a43", "a44", "a45", "a46", "a47", "a48", "a49", "a50",
      "a51", "a52", "a53", "a54", "a55", "a56", "a57", "a58", "a59", "a60", "a61", "a62", "a63", "a64", "a65", "a66", "a67", "a68", "a69", "a70",
      "a71", "a72", "a73", "a74", "a75", "a76", "a77", "a78", "a79", "a80", "a81", "a82", "a83", "a84", "a85", "a86", "a87", "a88", "a89", "a90",
      "a91", "a92", "a93", "a94", "a95", "a96", "a97", "a98", "a99", "a100", "a101", "a102", "a103", "a104", "a105", "a106", "a107", "a108", "a109", "a110",
      "a111", "a112", "a113", "a114", "a115", "a116", "a117", "a118", "a119", "a120", "a121", "a122", "a123", "a124", "a125", "a126", "a127",
      "v32", "v33", "v34", "v35", "v36", "v37", "v38", "v39", "v40", "v41", "v42", "v43", "v44", "v45", "v46", "v47", "v48", "v49",
      "v50", "v51", "v52", "v53", "v54", "v55", "v56", "v57", "v58", "v59", "v60", "v61", "v62", "v63", "v64", "v65", "v66", "v67", "v68", "v69", "v70",
      "v71", "v72", "v73", "v74", "v75", "v76", "v77", "v78", "v79", "v80", "v81", "v82", "v83", "v84", "v85", "v86", "v87", "v88", "v89", "v90", "v91",
      "v92", "v93", "v94", "v95", "v96", "v97", "v98", "v99", "v100", "v101", "v102", "v103", "v104", "v105", "v106", "v107", "v108", "v109", "v110",
      "v111", "v112", "v113", "v114", "v115", "v116", "v117", "v118", "v119", "v120", "v121", "v122", "v123", "v124", "v125", "v126", "v127", "s59",
      "s60", "s61", "s62", "s63", "s64", "s65", "s66", "s67", "s68");
}
struct cpx { float r, i; };
DI cpx cmul(cpx a, cpx b) { return {a.r * b.r - a.i * b.i, a.r * b.i + a.i * b.r}; }
DI cpx cfma(cpx a, cpx b, cpx c) { return {fmaf(a.r, b.r, fmaf(-a.i, b.i, c.r)), fmaf(a.r, b.i, fmaf(a.i, b.r, c.i))}; }
DI void ssm_par(const CAS Params& p, u32 ch, cpx& lam, cpx& coef) {
  const float lr = bflo(p.Lre[ch]), li = bflo(p.Lim[ch]), dt = rbf(expf(bflo(p.ldt[ch])));
  const float e = expf(dt * lr); float sn, cs; sincosf(dt * li, &sn, &cs);
  lam = {e * cs, e * sn};
  const float d = 1.0f / (lr * lr + li * li), nr = lam.r - 1.0f, ni = lam.i;
  coef = {(nr * lr + ni * li) * d, (ni * lr - nr * li) * d};
}
constexpr u32 LDS_EPI = 147456;
DI void task_U(const CAS Params& p, int rt, int ct, int w, int lane, bool first) {
  const u32 tid = threadIdx.x;
  LAS f4* lpar = (LAS f4*)(LDS_EPI);
  LAS f2* lagg = (LAS f2*)(LDS_EPI + 2048);
  cpx lam1 = {0.f, 0.f};
  if (tid < 128) { cpx cf; ssm_par(p, ct * 128 + tid, lam1, cf); lpar[tid] = (f4){lam1.r, lam1.i, 2.0f * cf.r, 2.0f * cf.i}; }
  GemmArgs g; f16v acc[8];
  setup_x<1>(g, p.xA, 4u << 20, 2048, rt * 128, w, lane); setup_nk<true>(g, p.Bre, p.Bim, ct * 128, w, lane);
  asm volatile("s_waitcnt lgkmcnt(0)" ::: "memory");
  gemm_nk_nt(g, acc);
  asm volatile("" : "+s"(rt), "+s"(ct), "+s"(w), "+v"(lane));
  GST(4)
  const u32 c = lane & 15, gq = lane >> 4, ch0 = (w & 1) * 64 + 4 * c, chunk = (w >> 1) * 4 + gq;
  f4 par[4];
#pragma unroll
  for (int j = 0; j < 4; ++j) {
    par[j] = lpar[ch0 + j];
    const cpx lam = {par[j][0], par[j][1]}; cpx hh = {acc[j][0], acc[4 + j][0]};
#pragma unroll
    for (int t = 1; t < 16; ++t) hh = cfma(lam, hh, (cpx){acc[j][t], acc[4 + j][t]});
    lagg[chunk * 128 + ch0 + j] = (f2){hh.r, hh.i};
  }
  BARRIER();
  gu32* ctl = (gu32*)(p.wb + OFF_CTL);
  const rsrc_t rx = mkrs(p.wb, WS_END);
  if (w < 2) {
    const f4 pp = lpar[tid]; cpx l16 = {pp[0], pp[1]};
#pragma unroll
    for (int k = 0; k < 4; ++k) l16 = cmul(l16, l16);
    cpx A[8], T = {0.f, 0.f};
#pragma unroll
    for (int k = 0; k < 8; ++k) { const f2 v = lagg[k * 128 + tid]; A[k] = {v[0], v[1]}; T = cfma(l16, T, A[k]); }
    __builtin_amdgcn_raw_buffer_store_b64((v2u){bf(T.r), bf(T.i)}, rx, tid * 8, OFF_AGG + (rt * 1024 + ct * 128) * 8, SC1);
    if (first) wait_init(ctl, lane, w == 0);
    WAITV(0);
    if (lane == 0) __hip_atomic_store(ctl + C_AGF + 16 * ((ct * 2 + w) * 16 + rt), 1u, RLX_AGENT);
    cpx l128 = l16;
#pragma unroll
    for (int k = 0; k < 3; ++k) l128 = cmul(l128, l128);
    wait_lanes(ctl, C_AGF + 16 * ((ct * 2 + w) * 16 + min((u32)lane & 15u, (u32)rt)), 1u);
    v2u pv[15];
#pragma unroll
    for (int j = 0; j < 15; ++j) pv[j] = __builtin_amdgcn_raw_buffer_load_b64(rx, j < rt ? tid * 8 : OOB, OFF_AGG + (j * 1024 + ct * 128) * 8, SC1);
    cpx H = {0.f, 0.f};
#pragma unroll
    for (int j = 0; j < 15; ++j) if (j < rt) H = cfma(l128, H, (cpx){fb(pv[j].x), fb(pv[j].y)});
#pragma unroll
    for (int k = 0; k < 8; ++k) { lagg[k * 128 + tid] = (f2){H.r, H.i}; H = cfma(l16, H, A[k]); }
  }
  BARRIER();
  cpx hs[4];
#pragma unroll
  for (int j = 0; j < 4; ++j) { const f2 v = lagg[chunk * 128 + ch0 + j]; hs[j] = {v[0], v[1]}; }
  const u32 vo = (chunk * 16) * 4096 + ch0 * 2, so = OFF_H + rt * 128 * 4096 + ct * 256;
#pragma unroll
  for (int t = 0; t < 16; ++t) {
    float yr[4], yi[4];
#pragma unroll
    for (int j = 0; j < 4; ++j) {
      hs[j] = cfma((cpx){par[j][0], par[j][1]}, hs[j], (cpx){acc[j][t], acc[4 + j][t]});
      yr[j] = par[j][2] * hs[j].r - par[j][3] * hs[j].i; yi[j] = -(par[j][2] * hs[j].i + par[j][3] * hs[j].r);
    }
    __builtin_amdgcn_raw_buffer_store_b64((v2u){pk(yr[0], yr[1]), pk(yr[2], yr[3])}, rx, vo, so + t * 4096, SC1);
    __builtin_amdgcn_raw_buffer_store_b64((v2u){pk(yi[0], yi[1]), pk(yi[2], yi[3])}, rx, vo + 2048, so + t * 4096, SC1);
  }
  tile_done(ctl, C_HCNT + 64 * rt, true, false);
}
DI void task_Y(const CAS Params& p, int rt, int ct, int w, int lane) {
  gu32* ctl = (gu32*)(p.wb + OFF_CTL);
  if (w == 0) { wait_lanes(ctl, C_HCNT + 64 * rt, 8u); __builtin_amdgcn_fence(__ATOMIC_ACQUIRE, "agent"); WAITV(0); }
  BARRIER();
  GemmArgs g; f16v acc[8];
  setup_x<0>(g, p.wb + OFF_H, 8u << 20, 4096, rt * 128, w, lane); setup_nk<false>(g, p.Cre, p.Cim, ct * 256, w, lane);
  gemm_nk_nt(g, acc);
  asm volatile("" : "+s"(rt), "+s"(ct), "+s"(w), "+v"(lane));
  GST(5)
  const u32 c = lane & 15, gq = lane >> 4, d0 = ct * 256 + (w & 1) * 128 + 4 * c, t0 = rt * 128 + (w >> 1) * 64 + 4 * gq;
  const rsrc_t rxa = mkrs(p.xA, 4u << 20), ro = mkrs(p.out, 16u << 20);
  v2u dk[2], xv[2][16];
#pragma unroll
  for (int hf = 0; hf < 2; ++hf) {
    dk[hf] = *(const GAS v2u*)(p.Dsk + d0 + 64 * hf);
#pragma unroll
    for (int t = 0; t < 16; ++t) xv[hf][t] = __builtin_amdgcn_raw_buffer_load_b64(rxa, t0 * 2048 + d0 * 2 + (t & 1) * 2048 + hf * 128, ((t >> 2) * 16 + (t & 2)) * 2048, 0);
  }
  __builtin_amdgcn_sched_barrier(0);
#pragma unroll
  for (int hf = 0; hf < 2; ++hf)
#pragma unroll
    for (int t = 0; t < 16; ++t) {
      const v2u x = xv[hf][t], d = dk[hf];
      const f4 o = {acc[4 * hf][t] + rbf(bflo(x.x) * bflo(d.x)), acc[4 * hf + 1][t] + rbf(bfhi(x.x) * bfhi(d.x)),
                    acc[4 * hf + 2][t] + rbf(bflo(x.y) * bflo(d.y)), acc[4 * hf + 3][t] + rbf(bfhi(x.y) * bfhi(d.y))};
      __builtin_amdgcn_raw_buffer_store_b128(__builtin_bit_cast(v4u, o), ro, t0 * 8192 + d0 * 4 + hf * 256 + ((t >> 2) * 16 + (t & 3)) * 8192, 0, OUT_AUX);
    }
}
DI void task_S(const CAS Params& p, int grp, int ct, int w, int lane) {
  const u32 c = lane & 15, gq = lane >> 4, cs = ct & 15, mt = ct >> 4;
  const u32 vo_src = (w * 1024 + (lane >> 3)) * 2048 + cs * 128 + (((lane & 7) * 16) ^ (gq * 32));
  const u32 a_tr = w * 32768 + (4 * gq + (c >> 2)) * 128 + (2 * (gq & 1) + (c >> 3)) * 32 + (c & 3) * 8;
  const u32 vo_of = OFF_OF + w * 65536 + mt * 1024 + lane * 16, vo_cnt = OFF_CTL + 4 * C_OCNT;
  const u32 a_w = w * 32768 + lane * 16, a_rw = LDS_MEM + w * 4096 + lane * 16, a_rr = LDS_MEM + w * 1024 + lane * 16;
  const u32 vo_out = (mt * 16 + c) * 8192 + (1024 + cs * 64 + w * 16 + 4 * gq) * 4;
  const u32 vo_dbg = threadIdx.x == 0 ? OFF_DBG + (5u << 20) + (grp * 32 + ct) * 4 : OOB;
  const v4u rxv = mkrv(p.wb, WS_END), rw = mkrv(p.Wo, 8u << 20), ro = mkrv(p.out, 16u << 20);
  const u32 rho = (cs & 3) * 8;
  const u32 seg0 = grp ? SSPLIT : 0, send = NSGRP == 1 ? 64 : grp ? 62 : SSPLIT, sextra = NSGRP == 1 ? 64 : 62 + grp;
  const u32 vo_ep = (grp == NSGRP - 1 && ct == 0 && w == 0) ? (lane < 49 ? OFF_CTL + 4 * C_F + lane * 16 : lane == 49 ? OFF_CTL + 4 * C_EP : OOB) : OOB;
  WAITV(0);
  BARRIER();
  asm volatile(
    "v_mov_b32 v0, v64\n"
    "v_mov_b32 v1, v65\n"
    "v_mov_b32 v5, v66\n"
    "v_mov_b32 v6, v67\n"
    "v_mov_b32 v7, v68\n"
    "v_mov_b32 v8, v69\n"
    "v_mov_b32 v9, v70\n"
    "v_mov_b32 v10, v71\n"
    "v_mov_b32 v11, v72\n"
    "v_mov_b32 v13, v73\n"
    "s_nop 4\n"
    "s_lshl_b32 s72, s56, 15\n"
    "v_xor_b32 v2, 32, v1\n"
    "v_xor_b32 v3, 64, v1\n"
    "v_xor_b32 v4, 96, v1\n"
    "s_add_u32 m0, s72, 0\n"
    "s_add_u32 s69, s59, 0\n"
    "s_and_b32 s69, s69, 31\n"
    "s_lshl_b32 s69, s69, 16\n"
    "buffer_load_dwordx4 v0, s[44:47], s69 offen lds\n"
    "s_add_u32 m0, m0, 1024\n"
    "s_add_u32 s69, s69, 16384\n"
    "buffer_load_dwordx4 v0, s[44:47], s69 offen lds\n"
    "s_add_u32 m0, m0, 1024\n"
    "s_add_u32 s69, s69, 16384\n"
    "buffer_load_dwordx4 v0, s[44:47], s69 offen lds\n"
    "s_add_u32 m0, m0, 1024\n"
    "s_add_u32 s69, s69, 16384\n"
    "buffer_load_dwordx4 v0, s[44:47], s69 offen lds\n"
    "s_add_u32 m0, m0, 1024\n"
    "s_add_u32 s69, s59, 1\n"
    "s_and_b32 s69, s69, 31\n"
    "s_lshl_b32 s69, s69, 16\n"
    "buffer_load_dwordx4 v0, s[44:47], s69 offen lds\n"
    "s_add_u32 m0, m0, 1024\n"
    "s_add_u32 s69, s69, 16384\n"
    "buffer_load_dwordx4 v0, s[44:47], s69 offen lds\n"
    "s_add_u32 m0, m0, 1024\n"
    "s_add_u32 s69, s69, 16384\n"
    "buffer_load_dwordx4 v0, s[44:47], s69 offen lds\n"
    "s_add_u32 m0, m0, 1024\n"
    "s_add_u32 s69, s69, 16384\n"
    "buffer_load_dwordx4 v0, s[44:47], s69 offen lds\n"
    "s_add_u32 m0, s72, 8192\n"
    "s_add_u32 s69, s59, 2\n"
    "s_and_b32 s69, s69, 31\n"
    "s_lshl_b32 s69, s69, 16\n"
    "buffer_load_dwordx4 v0, s[44:47], s69 offen lds\n"
    "s_add_u32 m0, m0, 1024\n"
    "s_add_u32 s69, s69, 16384\n"
    "buffer_load_dwordx4 v0, s[44:47], s69 offen lds\n"
    "s_add_u32 m0, m0, 1024\n"
    "s_add_u32 s69, s69, 16384\n"
    "buffer_load_dwordx4 v0, s[44:47], s69 offen lds\n"
    "s_add_u32 m0, m0, 1024\n"
    "s_add_u32 s69, s69, 16384\n"
    "buffer_load_dwordx4 v0, s[44:47], s69 offen lds\n"
    "s_add_u32 m0, m0, 1024\n"
    "s_add_u32 s69, s59, 4\n"
    "s_and_b32 s69, s69, 31\n"
    "s_lshl_b32 s69, s69, 16\n"
    "buffer_load_dwordx4 v0, s[44:47], s69 offen lds\n"
    "s_add_u32 m0, m0, 1024\n"
    "s_add_u32 s69, s69, 16384\n"
    "buffer_load_dwordx4 v0, s[44:47], s69 offen lds\n"
    "s_add_u32 m0, m0, 1024\n"
    "s_add_u32 s69, s69, 16384\n"
    "buffer_load_dwordx4 v0, s[44:47], s69 offen lds\n"
    "s_add_u32 m0, m0, 1024\n"
    "s_add_u32 s69, s69, 16384\n"
    "buffer_load_dwordx4 v0, s[44:47], s69 offen lds\n"
    "s_add_u32 m0, s72, 16384\n"
    "s_add_u32 s69, s59, 5\n"
    "s_and_b32 s69, s69, 31\n"
    "s_lshl_b32 s69, s69, 16\n"
    "buffer_load_dwordx4 v0, s[44:47], s69 offen lds\n"
    "s_add_u32 m0, m0, 1024\n"
    "s_add_u32 s69, s69, 16384\n"
    "buffer_load_dwordx4 v0, s[44:47], s69 offen lds\n"
    "s_add_u32 m0, m0, 1024\n"
    "s_add_u32 s69, s69, 16384\n"
    "buffer_load_dwordx4 v0, s[44:47], s69 offen lds\n"
    "s_add_u32 m0, m0, 1024\n"
    "s_add_u32 s69, s69, 16384\n"
    "buffer_load_dwordx4 v0, s[44:47], s69 offen lds\n"
    "s_add_u32 m0, m0, 1024\n"
    "s_add_u32 s69, s59, 6\n"
    "s_and_b32 s69, s69, 31\n"
    "s_lshl_b32 s69, s69, 16\n"
    "buffer_load_dwordx4 v0, s[44:47], s69 offen lds\n"
    "s_add_u32 m0, m0, 1024\n"
    "s_add_u32 s69, s69, 16384\n"
    "buffer_load_dwordx4 v0, s[44:47], s69 offen lds\n"
    "s_add_u32 m0, m0, 1024\n"
    "s_add_u32 s69, s69, 16384\n"
    "buffer_load_dwordx4 v0, s[44:47], s69 offen lds\n"
    "s_add_u32 m0, m0, 1024\n"
    "s_add_u32 s69, s69, 16384\n"
    "buffer_load_dwordx4 v0, s[44:47], s69 offen lds\n"
    "s_add_u32 m0, s72, 24576\n"
    "s_add_u32 s69, s59, 8\n"
    "s_and_b32 s69, s69, 31\n"
    "s_lshl_b32 s69, s69, 16\n"
    "buffer_load_dwordx4 v0, s[44:47], s69 offen lds\n"
    "s_add_u32 m0, m0, 1024\n"
    "s_add_u32 s69, s69, 16384\n"
    "buffer_load_dwordx4 v0, s[44:47], s69 offen lds\n"
    "s_add_u32 m0, m0, 1024\n"
    "s_add_u32 s69, s69, 16384\n"
    "buffer_load_dwordx4 v0, s[44:47], s69 offen lds\n"
    "s_add_u32 m0, m0, 1024\n"
    "s_add_u32 s69, s69, 16384\n"
    "buffer_load_dwordx4 v0, s[44:47], s69 offen lds\n"
    "s_add_u32 m0, m0, 1024\n"
    "s_add_u32 s69, s59, 9\n"
    "s_and_b32 s69, s69, 31\n"
    "s_lshl_b32 s69, s69, 16\n"
    "buffer_load_dwordx4 v0, s[44:47], s69 offen lds\n"
    "s_add_u32 m0, m0, 1024\n"
    "s_add_u32 s69, s69, 16384\n"
    "buffer_load_dwordx4 v0, s[44:47], s69 offen lds\n"
    "s_add_u32 m0, m0, 1024\n"
    "s_add_u32 s69, s69, 16384\n"
    "buffer_load_dwordx4 v0, s[44:47], s69 offen lds\n"
    "s_add_u32 m0, m0, 1024\n"
    "s_add_u32 s69, s69, 16384\n"
    "buffer_load_dwordx4 v0, s[44:47], s69 offen lds\n"
    "s_waitcnt vmcnt(24)\n"
    "ds_read_b64_tr_b16 a[0:1], v1 offset:0\n"
    "ds_read_b64_tr_b16 a[2:3], v1 offset:2048\n"
    "ds_read_b64_tr_b16 a[4:5], v2 offset:0\n"
    "ds_read_b64_tr_b16 a[6:7], v2 offset:2048\n"
    "ds_read_b64_tr_b16 a[8:9], v3 offset:0\n"
    "ds_read_b64_tr_b16 a[10:11], v3 offset:2048\n"
    "ds_read_b64_tr_b16 a[12:13], v4 offset:0\n"
    "ds_read_b64_tr_b16 a[14:15], v4 offset:2048\n"
    "s_waitcnt lgkmcnt(0)\n"
    "ds_read_b64_tr_b16 a[16:17], v1 offset:4096\n"
    "ds_read_b64_tr_b16 a[18:19], v1 offset:6144\n"
    "ds_read_b64_tr_b16 a[20:21], v2 offset:4096\n"
    "ds_read_b64_tr_b16 a[22:23], v2 offset:6144\n"
    "ds_read_b64_tr_b16 a[24:25], v3 offset:4096\n"
    "ds_read_b64_tr_b16 a[26:27], v3 offset:6144\n"
    "ds_read_b64_tr_b16 a[28:29], v4 offset:4096\n"
    "ds_read_b64_tr_b16 a[30:31], v4 offset:6144\n"
    "s_waitcnt lgkmcnt(0)\n"
    "s_add_u32 m0, s72, 0\n"
    "s_add_u32 s69, s59, 10\n"
    "s_and_b32 s69, s69, 31\n"
    "s_lshl_b32 s69, s69, 16\n"
    "buffer_load_dwordx4 v0, s[44:47], s69 offen lds\n"
    "s_add_u32 m0, m0, 1024\n"
    "s_add_u32 s69, s69, 16384\n"
    "buffer_load_dwordx4 v0, s[44:47], s69 offen lds\n"
    "s_add_u32 m0, m0, 1024\n"
    "s_add_u32 s69, s69, 16384\n"
    "buffer_load_dwordx4 v0, s[44:47], s69 offen lds\n"
    "s_add_u32 m0, m0, 1024\n"
    "s_add_u32 s69, s69, 16384\n"
    "buffer_load_dwordx4 v0, s[44:47], s69 offen lds\n"
    "s_add_u32 m0, m0, 1024\n"
    "s_add_u32 s69, s59, 12\n"
    "s_and_b32 s69, s69, 31\n"
    "s_lshl_b32 s69, s69, 16\n"
    "buffer_load_dwordx4 v0, s[44:47], s69 offen lds\n"
    "s_add_u32 m0, m0, 1024\n"
    "s_add_u32 s69, s69, 16384\n"
    "buffer_load_dwordx4 v0, s[44:47], s69 offen lds\n"
    "s_add_u32 m0, m0, 1024\n"
    "s_add_u32 s69, s69, 16384\n"
    "buffer_load_dwordx4 v0, s[44:47], s69 offen lds\n"
    "s_add_u32 m0, m0, 1024\n"
    "s_add_u32 s69, s69, 16384\n"
    "buffer_load_dwordx4 v0, s[44:47], s69 offen lds\n"
    "s_waitcnt vmcnt(24)\n"
    "ds_read_b64_tr_b16 a[32:33], v1 offset:8192\n"
    "ds_read_b64_tr_b16 a[34:35], v1 offset:10240\n"
    "ds_read_b64_tr_b16 a[36:37], v2 offset:8192\n"
    "ds_read_b64_tr_b16 a[38:39], v2 offset:10240\n"
    "ds_read_b64_tr_b16 a[40:41], v3 offset:8192\n"
    "ds_read_b64_tr_b16 a[42:43], v3 offset:10240\n"
    "ds_read_b64_tr_b16 a[44:45], v4 offset:8192\n"
    "ds_read_b64_tr_b16 a[46:47], v4 offset:10240\n"
    "s_waitcnt lgkmcnt(0)\n"
    "ds_read_b64_tr_b16 a[48:49], v1 offset:12288\n"
    "ds_read_b64_tr_b16 a[50:51], v1 offset:14336\n"
    "ds_read_b64_tr_b16 a[52:53], v2 offset:12288\n"
    "ds_read_b64_tr_b16 a[54:55], v2 offset:14336\n"
    "ds_read_b64_tr_b16 a[56:57], v3 offset:12288\n"
    "ds_read_b64_tr_b16 a[58:59], v3 offset:14336\n"
    "ds_read_b64_tr_b16 a[60:61], v4 offset:12288\n"
    "ds_read_b64_tr_b16 a[62:63], v4 offset:14336\n"
    "s_waitcnt lgkmcnt(0)\n"
    "s_add_u32 m0, s72, 8192\n"
    "s_add_u32 s69, s59, 13\n"
    "s_and_b32 s69, s69, 31\n"
    "s_lshl_b32 s69, s69, 16\n"
    "buffer_load_dwordx4 v0, s[44:47], s69 offen lds\n"
    "s_add_u32 m0, m0, 1024\n"
    "s_add_u32 s69, s69, 16384\n"
    "buffer_load_dwordx4 v0, s[44:47], s69 offen lds\n"
    "s_add_u32 m0, m0, 1024\n"
    "s_add_u32 s69, s69, 16384\n"
    "buffer_load_dwordx4 v0, s[44:47], s69 offen lds\n"
    "s_add_u32 m0, m0, 1024\n"
    "s_add_u32 s69, s69, 16384\n"
    "buffer_load_dwordx4 v0, s[44:47], s69 offen lds\n"
    "s_add_u32 m0, m0, 1024\n"
    "s_add_u32 s69, s59, 14\n"
    "s_and_b32 s69, s69, 31\n"
    "s_lshl_b32 s69, s69, 16\n"
    "buffer_load_dwordx4 v0, s[44:47], s69 offen lds\n"
    "s_add_u32 m0, m0, 1024\n"
    "s_add_u32 s69, s69, 16384\n"
    "buffer_load_dwordx4 v0, s[44:47], s69 offen lds\n"
    "s_add_u32 m0, m0, 1024\n"
    "s_add_u32 s69, s69, 16384\n"
    "buffer_load_dwordx4 v0, s[44:47], s69 offen lds\n"
    "s_add_u32 m0, m0, 1024\n"
    "s_add_u32 s69, s69, 16384\n"
    "buffer_load_dwordx4 v0, s[44:47], s69 offen lds\n"
    "s_waitcnt vmcnt(24)\n"
    "ds_read_b64_tr_b16 a[64:65], v1 offset:16384\n"
    "ds_read_b64_tr_b16 a[66:67], v1 offset:18432\n"
    "ds_read_b64_tr_b16 a[68:69], v2 offset:16384\n"
    "ds_read_b64_tr_b16 a[70:71], v2 offset:18432\n"
    "ds_read_b64_tr_b16 a[72:73], v3 offset:16384\n"
    "ds_read_b64_tr_b16 a[74:75], v3 offset:18432\n"
    "ds_read_b64_tr_b16 a[76:77], v4 offset:16384\n"
    "ds_read_b64_tr_b16 a[78:79], v4 offset:18432\n"
    "s_waitcnt lgkmcnt(0)\n"
    "ds_read_b64_tr_b16 a[80:81], v1 offset:20480\n"
    "ds_read_b64_tr_b16 a[82:83], v1 offset:22528\n"
    "ds_read_b64_tr_b16 a[84:85], v2 offset:20480\n"
    "ds_read_b64_tr_b16 a[86:87], v2 offset:22528\n"
    "ds_read_b64_tr_b16 a[88:89], v3 offset:20480\n"
    "ds_read_b64_tr_b16 a[90:91], v3 offset:22528\n"
    "ds_read_b64_tr_b16 a[92:93], v4 offset:20480\n"
    "ds_read_b64_tr_b16 a[94:95], v4 offset:22528\n"
    "s_waitcnt lgkmcnt(0)\n"
    "s_add_u32 m0, s72, 16384\n"
    "s_add_u32 s69, s59, 16\n"
    "s_and_b32 s69, s69, 31\n"
    "s_lshl_b32 s69, s69, 16\n"
    "buffer_load_dwordx4 v0, s[44:47], s69 offen lds\n"
    "s_add_u32 m0, m0, 1024\n"
    "s_add_u32 s69, s69, 16384\n"
    "buffer_load_dwordx4 v0, s[44:47], s69 offen lds\n"
    "s_add_u32 m0, m0, 1024\n"
    "s_add_u32 s69, s69, 16384\n"
    "buffer_load_dwordx4 v0, s[44:47], s69 offen lds\n"
    "s_add_u32 m0, m0, 1024\n"
    "s_add_u32 s69, s69, 16384\n"
    "buffer_load_dwordx4 v0, s[44:47], s69 offen lds\n"
    "s_add_u32 m0, m0, 1024\n"
    "s_add_u32 s69, s59, 17\n"
    "s_and_b32 s69, s69, 31\n"
    "s_lshl_b32 s69, s69, 16\n"
    "buffer_load_dwordx4 v0, s[44:47], s69 offen lds\n"
    "s_add_u32 m0, m0, 1024\n"
    "s_add_u32 s69, s69, 16384\n"
    "buffer_load_dwordx4 v0, s[44:47], s69 offen lds\n"
    "s_add_u32 m0, m0, 1024\n"
    "s_add_u32 s69, s69, 16384\n"
    "buffer_load_dwordx4 v0, s[44:47], s69 offen lds\n"
    "s_add_u32 m0, m0, 1024\n"
    "s_add_u32 s69, s69, 16384\n"
    "buffer_load_dwordx4 v0, s[44:47], s69 offen lds\n"
    "s_waitcnt vmcnt(24)\n"
    "ds_read_b64_tr_b16 a[96:97], v1 offset:24576\n"
    "ds_read_b64_tr_b16 a[98:99], v1 offset:26624\n"
    "ds_read_b64_tr_b16 a[100:101], v2 offset:24576\n"
    "ds_read_b64_tr_b16 a[102:103], v2 offset:26624\n"
    "ds_read_b64_tr_b16 a[104:105], v3 offset:24576\n"
    "ds_read_b64_tr_b16 a[106:107], v3 offset:26624\n"
    "ds_read_b64_tr_b16 a[108:109], v4 offset:24576\n"
    "ds_read_b64_tr_b16 a[110:111], v4 offset:26624\n"
    "s_waitcnt lgkmcnt(0)\n"
    "ds_read_b64_tr_b16 a[112:113], v1 offset:28672\n"
    "ds_read_b64_tr_b16 a[114:115], v1 offset:30720\n"
    "ds_read_b64_tr_b16 a[116:117], v2 offset:28672\n"
    "ds_read_b64_tr_b16 a[118:119], v2 offset:30720\n"
    "ds_read_b64_tr_b16 a[120:121], v3 offset:28672\n"
    "ds_read_b64_tr_b16 a[122:123], v3 offset:30720\n"
    "ds_read_b64_tr_b16 a[124:125], v4 offset:28672\n"
    "ds_read_b64_tr_b16 a[126:127], v4 offset:30720\n"
    "s_waitcnt lgkmcnt(0)\n"
    "s_add_u32 m0, s72, 24576\n"
    "s_add_u32 s69, s59, 18\n"
    "s_and_b32 s69, s69, 31\n"
    "s_lshl_b32 s69, s69, 16\n"
    "buffer_load_dwordx4 v0, s[44:47], s69 offen lds\n"
    "s_add_u32 m0, m0, 1024\n"
    "s_add_u32 s69, s69, 16384\n"
    "buffer_load_dwordx4 v0, s[44:47], s69 offen lds\n"
    "s_add_u32 m0, m0, 1024\n"
    "s_add_u32 s69, s69, 16384\n"
    "buffer_load_dwordx4 v0, s[44:47], s69 offen lds\n"
    "s_add_u32 m0, m0, 1024\n"
    "s_add_u32 s69, s69, 16384\n"
    "buffer_load_dwordx4 v0, s[44:47], s69 offen lds\n"
    "s_add_u32 m0, m0, 1024\n"
    "s_add_u32 s69, s59, 20\n"
    "s_and_b32 s69, s69, 31\n"
    "s_lshl_b32 s69, s69, 16\n"
    "buffer_load_dwordx4 v0, s[44:47], s69 offen lds\n"
    "s_add_u32 m0, m0, 1024\n"
    "s_add_u32 s69, s69, 16384\n"
    "buffer_load_dwordx4 v0, s[44:47], s69 offen lds\n"
    "s_add_u32 m0, m0, 1024\n"
    "s_add_u32 s69, s69, 16384\n"
    "buffer_load_dwordx4 v0, s[44:47], s69 offen lds\n"
    "s_add_u32 m0, m0, 1024\n"
    "s_add_u32 s69, s69, 16384\n"
    "buffer_load_dwordx4 v0, s[44:47], s69 offen lds\n"
    "s_waitcnt vmcnt(24)\n"
    "ds_read_b64_tr_b16 a[128:129], v1 offset:0\n"
    "ds_read_b64_tr_b16 a[130:131], v1 offset:2048\n"
    "ds_read_b64_tr_b16 a[132:133], v2 offset:0\n"
    "ds_read_b64_tr_b16 a[134:135], v2 offset:2048\n"
    "ds_read_b64_tr_b16 a[136:137], v3 offset:0\n"
    "ds_read_b64_tr_b16 a[138:139], v3 offset:2048\n"
    "ds_read_b64_tr_b16 a[140:141], v4 offset:0\n"
    "ds_read_b64_tr_b16 a[142:143], v4 offset:2048\n"
    "s_waitcnt lgkmcnt(0)\n"
    "ds_read_b64_tr_b16 a[144:145], v1 offset:4096\n"
    "ds_read_b64_tr_b16 a[146:147], v1 offset:6144\n"
    "ds_read_b64_tr_b16 a[148:149], v2 offset:4096\n"
    "ds_read_b64_tr_b16 a[150:151], v2 offset:6144\n"
    "ds_read_b64_tr_b16 a[152:153], v3 offset:4096\n"
    "ds_read_b64_tr_b16 a[154:155], v3 offset:6144\n"
    "ds_read_b64_tr_b16 a[156:157], v4 offset:4096\n"
    "ds_read_b64_tr_b16 a[158:159], v4 offset:6144\n"
    "s_waitcnt lgkmcnt(0)\n"
    "s_add_u32 m0, s72, 0\n"
    "s_add_u32 s69, s59, 21\n"
    "s_and_b32 s69, s69, 31\n"
    "s_lshl_b32 s69, s69, 16\n"
    "buffer_load_dwordx4 v0, s[44:47], s69 offen lds\n"
    "s_add_u32 m0, m0, 1024\n"
    "s_add_u32 s69, s69, 16384\n"
    "buffer_load_dwordx4 v0, s[44:47], s69 offen lds\n"
    "s_add_u32 m0, m0, 1024\n"
    "s_add_u32 s69, s69, 16384\n"
    "buffer_load_dwordx4 v0, s[44:47], s69 offen lds\n"
    "s_add_u32 m0, m0, 1024\n"
    "s_add_u32 s69, s69, 16384\n"
    "buffer_load_dwordx4 v0, s[44:47], s69 offen lds\n"
    "s_add_u32 m0, m0, 1024\n"
    "s_add_u32 s69, s59, 22\n"
    "s_and_b32 s69, s69, 31\n"
    "s_lshl_b32 s69, s69, 16\n"
    "buffer_load_dwordx4 v0, s[44:47], s69 offen lds\n"
    "s_add_u32 m0, m0, 1024\n"
    "s_add_u32 s69, s69, 16384\n"
    "buffer_load_dwordx4 v0, s[44:47], s69 offen lds\n"
    "s_add_u32 m0, m0, 1024\n"
    "s_add_u32 s69, s69, 16384\n"
    "buffer_load_dwordx4 v0, s[44:47], s69 offen lds\n"
    "s_add_u32 m0, m0, 1024\n"
    "s_add_u32 s69, s69, 16384\n"
    "buffer_load_dwordx4 v0, s[44:47], s69 offen lds\n"
    "s_waitcnt vmcnt(24)\n"
    "ds_read_b64_tr_b16 a[160:161], v1 offset:8192\n"
    "ds_read_b64_tr_b16 a[162:163], v1 offset:10240\n"
    "ds_read_b64_tr_b16 a[164:165], v2 offset:8192\n"
    "ds_read_b64_tr_b16 a[166:167], v2 offset:10240\n"
    "ds_read_b64_tr_b16 a[168:169], v3 offset:8192\n"
    "ds_read_b64_tr_b16 a[170:171], v3 offset:10240\n"
    "ds_read_b64_tr_b16 a[172:173], v4 offset:8192\n"
    "ds_read_b64_tr_b16 a[174:175], v4 offset:10240\n"
    "s_waitcnt lgkmcnt(0)\n"
    "ds_read_b64_tr_b16 a[176:177], v1 offset:12288\n"
    "ds_read_b64_tr_b16 a[178:179], v1 offset:14336\n"
    "ds_read_b64_tr_b16 a[180:181], v2 offset:12288\n"
    "ds_read_b64_tr_b16 a[182:183], v2 offset:14336\n"
    "ds_read_b64_tr_b16 a[184:185], v3 offset:12288\n"
    "ds_read_b64_tr_b16 a[186:187], v3 offset:14336\n"
    "ds_read_b64_tr_b16 a[188:189], v4 offset:12288\n"
    "ds_read_b64_tr_b16 a[190:191], v4 offset:14336\n"
    "s_waitcnt lgkmcnt(0)\n"
    "s_add_u32 m0, s72, 8192\n"
    "s_add_u32 s69, s59, 24\n"
    "s_and_b32 s69, s69, 31\n"
    "s_lshl_b32 s69, s69, 16\n"
    "buffer_load_dwordx4 v0, s[44:47], s69 offen lds\n"
    "s_add_u32 m0, m0, 1024\n"
    "s_add_u32 s69, s69, 16384\n"
    "buffer_load_dwordx4 v0, s[44:47], s69 offen lds\n"
    "s_add_u32 m0, m0, 1024\n"
    "s_add_u32 s69, s69, 16384\n"
    "buffer_load_dwordx4 v0, s[44:47], s69 offen lds\n"
    "s_add_u32 m0, m0, 1024\n"
    "s_add_u32 s69, s69, 16384\n"
    "buffer_load_dwordx4 v0, s[44:47], s69 offen lds\n"
    "s_add_u32 m0, m0, 1024\n"
    "s_add_u32 s69, s59, 25\n"
    "s_and_b32 s69, s69, 31\n"
    "s_lshl_b32 s69, s69, 16\n"
    "buffer_load_dwordx4 v0, s[44:47], s69 offen lds\n"
    "s_add_u32 m0, m0, 1024\n"
    "s_add_u32 s69, s69, 16384\n"
    "buffer_load_dwordx4 v0, s[44:47], s69 offen lds\n"
    "s_add_u32 m0, m0, 1024\n"
    "s_add_u32 s69, s69, 16384\n"
    "buffer_load_dwordx4 v0, s[44:47], s69 offen lds\n"
    "s_add_u32 m0, m0, 1024\n"
    "s_add_u32 s69, s69, 16384\n"
    "buffer_load_dwordx4 v0, s[44:47], s69 offen lds\n"
    "s_waitcnt vmcnt(24)\n"
    "ds_read_b64_tr_b16 a[192:193], v1 offset:16384\n"
    "ds_read_b64_tr_b16 a[194:195], v1 offset:18432\n"
    "ds_read_b64_tr_b16 a[196:197], v2 offset:16384\n"
    "ds_read_b64_tr_b16 a[198:199], v2 offset:18432\n"
    "ds_read_b64_tr_b16 a[200:201], v3 offset:16384\n"
    "ds_read_b64_tr_b16 a[202:203], v3 offset:18432\n"
    "ds_read_b64_tr_b16 a[204:205], v4 offset:16384\n"
    "ds_read_b64_tr_b16 a[206:207], v4 offset:18432\n"
    "s_waitcnt lgkmcnt(0)\n"
    "ds_read_b64_tr_b16 a[208:209], v1 offset:20480\n"
    "ds_read_b64_tr_b16 a[210:211], v1 offset:22528\n"
    "ds_read_b64_tr_b16 a[212:213], v2 offset:20480\n"
    "ds_read_b64_tr_b16 a[214:215], v2 offset:22528\n"
    "ds_read_b64_tr_b16 a[216:217], v3 offset:20480\n"
    "ds_read_b64_tr_b16 a[218:219], v3 offset:22528\n"
    "ds_read_b64_tr_b16 a[220:221], v4 offset:20480\n"
    "ds_read_b64_tr_b16 a[222:223], v4 offset:22528\n"
    "s_waitcnt lgkmcnt(0)\n"
    "s_add_u32 m0, s72, 16384\n"
    "s_add_u32 s69, s59, 26\n"
    "s_and_b32 s69, s69, 31\n"
    "s_lshl_b32 s69, s69, 16\n"
    "buffer_load_dwordx4 v0, s[44:47], s69 offen lds\n"
    "s_add_u32 m0, m0, 1024\n"
    "s_add_u32 s69, s69, 16384\n"
    "buffer_load_dwordx4 v0, s[44:47], s69 offen lds\n"
    "s_add_u32 m0, m0, 1024\n"
    "s_add_u32 s69, s69, 16384\n"
    "buffer_load_dwordx4 v0, s[44:47], s69 offen lds\n"
    "s_add_u32 m0, m0, 1024\n"
    "s_add_u32 s69, s69, 16384\n"
    "buffer_load_dwordx4 v0, s[44:47], s69 offen lds\n"
    "s_add_u32 m0, m0, 1024\n"
    "s_add_u32 s69, s59, 28\n"
    "s_and_b32 s69, s69, 31\n"
    "s_lshl_b32 s69, s69, 16\n"
    "buffer_load_dwordx4 v0, s[44:47], s69 offen lds\n"
    "s_add_u32 m0, m0, 1024\n"
    "s_add_u32 s69, s69, 16384\n"
    "buffer_load_dwordx4 v0, s[44:47], s69 offen lds\n"
    "s_add_u32 m0, m0, 1024\n"
    "s_add_u32 s69, s69, 16384\n"
    "buffer_load_dwordx4 v0, s[44:47], s69 offen lds\n"
    "s_add_u32 m0, m0, 1024\n"
    "s_add_u32 s69, s69, 16384\n"
    "buffer_load_dwordx4 v0, s[44:47], s69 offen lds\n"
    "s_waitcnt vmcnt(24)\n"
    "ds_read_b64_tr_b16 a[224:225], v1 offset:24576\n"
    "ds_read_b64_tr_b16 a[226:227], v1 offset:26624\n"
    "ds_read_b64_tr_b16 a[228:229], v2 offset:24576\n"
    "ds_read_b64_tr_b16 a[230:231], v2 offset:26624\n"
    "ds_read_b64_tr_b16 a[232:233], v3 offset:24576\n"
    "ds_read_b64_tr_b16 a[234:235], v3 offset:26624\n"
    "ds_read_b64_tr_b16 a[236:237], v4 offset:24576\n"
    "ds_read_b64_tr_b16 a[238:239], v4 offset:26624\n"
    "s_waitcnt lgkmcnt(0)\n"
    "ds_read_b64_tr_b16 a[240:241], v1 offset:28672\n"
    "ds_read_b64_tr_b16 a[242:243], v1 offset:30720\n"
    "ds_read_b64_tr_b16 a[244:245], v2 offset:28672\n"
    "ds_read_b64_tr_b16 a[246:247], v2 offset:30720\n"
    "ds_read_b64_tr_b16 a[248:249], v3 offset:28672\n"
    "ds_read_b64_tr_b16 a[250:251], v3 offset:30720\n"
    "ds_read_b64_tr_b16 a[252:253], v4 offset:28672\n"
    "ds_read_b64_tr_b16 a[254:255], v4 offset:30720\n"
    "s_waitcnt lgkmcnt(0)\n"
    "s_add_u32 m0, s72, 24576\n"
    "s_add_u32 s69, s59, 29\n"
    "s_and_b32 s69, s69, 31\n"
    "s_lshl_b32 s69, s69, 16\n"
    "buffer_load_dwordx4 v0, s[44:47], s69 offen lds\n"
    "s_add_u32 m0, m0, 1024\n"
    "s_add_u32 s69, s69, 16384\n"
    "buffer_load_dwordx4 v0, s[44:47], s69 offen lds\n"
    "s_add_u32 m0, m0, 1024\n"
    "s_add_u32 s69, s69, 16384\n"
    "buffer_load_dwordx4 v0, s[44:47], s69 offen lds\n"
    "s_add_u32 m0, m0, 1024\n"
    "s_add_u32 s69, s69, 16384\n"
    "buffer_load_dwordx4 v0, s[44:47], s69 offen lds\n"
    "s_add_u32 m0, m0, 1024\n"
    "s_add_u32 s69, s59, 30\n"
    "s_and_b32 s69, s69, 31\n"
    "s_lshl_b32 s69, s69, 16\n"
    "buffer_load_dwordx4 v0, s[44:47], s69 offen lds\n"
    "s_add_u32 m0, m0, 1024\n"
    "s_add_u32 s69, s69, 16384\n"
    "buffer_load_dwordx4 v0, s[44:47], s69 offen lds\n"
    "s_add_u32 m0, m0, 1024\n"
    "s_add_u32 s69, s69, 16384\n"
    "buffer_load_dwordx4 v0, s[44:47], s69 offen lds\n"
    "s_add_u32 m0, m0, 1024\n"
    "s_add_u32 s69, s69, 16384\n"
    "buffer_load_dwordx4 v0, s[44:47], s69 offen lds\n"
    "s_waitcnt vmcnt(24)\n"
    "ds_read_b64_tr_b16 v[128:129], v1 offset:0\n"
    "ds_read_b64_tr_b16 v[130:131], v1 offset:2048\n"
    "ds_read_b64_tr_b16 v[132:133], v2 offset:0\n"
    "ds_read_b64_tr_b16 v[134:135], v2 offset:2048\n"
    "ds_read_b64_tr_b16 v[136:137], v3 offset:0\n"
    "ds_read_b64_tr_b16 v[138:139], v3 offset:2048\n"
    "ds_read_b64_tr_b16 v[140:141], v4 offset:0\n"
    "ds_read_b64_tr_b16 v[142:143], v4 offset:2048\n"
    "s_waitcnt lgkmcnt(0)\n"
    "ds_read_b64_tr_b16 v[144:145], v1 offset:4096\n"
    "ds_read_b64_tr_b16 v[146:147], v1 offset:6144\n"
    "ds_read_b64_tr_b16 v[148:149], v2 offset:4096\n"
    "ds_read_b64_tr_b16 v[150:151], v2 offset:6144\n"
    "ds_read_b64_tr_b16 v[152:153], v3 offset:4096\n"
    "ds_read_b64_tr_b16 v[154:155], v3 offset:6144\n"
    "ds_read_b64_tr_b16 v[156:157], v4 offset:4096\n"
    "ds_read_b64_tr_b16 v[158:159], v4 offset:6144\n"
    "s_waitcnt lgkmcnt(0)\n"
    "s_add_u32 m0, s72, 0\n"
    "s_add_u32 s69, s59, 3\n"
    "s_and_b32 s69, s69, 31\n"
    "s_lshl_b32 s69, s69, 16\n"
    "buffer_load_dwordx4 v0, s[44:47], s69 offen lds\n"
    "s_add_u32 m0, m0, 1024\n"
    "s_add_u32 s69, s69, 16384\n"
    "buffer_load_dwordx4 v0, s[44:47], s69 offen lds\n"
    "s_add_u32 m0, m0, 1024\n"
    "s_add_u32 s69, s69, 16384\n"
    "buffer_load_dwordx4 v0, s[44:47], s69 offen lds\n"
    "s_add_u32 m0, m0, 1024\n"
    "s_add_u32 s69, s69, 16384\n"
    "buffer_load_dwordx4 v0, s[44:47], s69 offen lds\n"
    "s_add_u32 m0, m0, 1024\n"
    "s_add_u32 s69, s59, 7\n"
    "s_and_b32 s69, s69, 31\n"
    "s_lshl_b32 s69, s69, 16\n"
    "buffer_load_dwordx4 v0, s[44:47], s69 offen lds\n"
    "s_add_u32 m0, m0, 1024\n"
    "s_add_u32 s69, s69, 16384\n"
    "buffer_load_dwordx4 v0, s[44:47], s69 offen lds\n"
    "s_add_u32 m0, m0, 1024\n"
    "s_add_u32 s69, s69, 16384\n"
    "buffer_load_dwordx4 v0, s[44:47], s69 offen lds\n"
    "s_add_u32 m0, m0, 1024\n"
    "s_add_u32 s69, s69, 16384\n"
    "buffer_load_dwordx4 v0, s[44:47], s69 offen lds\n"
    "s_waitcnt vmcnt(24)\n"
    "ds_read_b64_tr_b16 v[160:161], v1 offset:8192\n"
    "ds_read_b64_tr_b16 v[162:163], v1 offset:10240\n"
    "ds_read_b64_tr_b16 v[164:165], v2 offset:8192\n"
    "ds_read_b64_tr_b16 v[166:167], v2 offset:10240\n"
    "ds_read_b64_tr_b16 v[168:169], v3 offset:8192\n"
    "ds_read_b64_tr_b16 v[170:171], v3 offset:10240\n"
    "ds_read_b64_tr_b16 v[172:173], v4 offset:8192\n"
    "ds_read_b64_tr_b16 v[174:175], v4 offset:10240\n"
    "s_waitcnt lgkmcnt(0)\n"
    "ds_read_b64_tr_b16 v[176:177], v1 offset:12288\n"
    "ds_read_b64_tr_b16 v[178:179], v1 offset:14336\n"
    "ds_read_b64_tr_b16 v[180:181], v2 offset:12288\n"
    "ds_read_b64_tr_b16 v[182:183], v2 offset:14336\n"
    "ds_read_b64_tr_b16 v[184:185], v3 offset:12288\n"
    "ds_read_b64_tr_b16 v[186:187], v3 offset:14336\n"
    "ds_read_b64_tr_b16 v[188:189], v4 offset:12288\n"
    "ds_read_b64_tr_b16 v[190:191], v4 offset:14336\n"
    "s_waitcnt lgkmcnt(0)\n"
    "s_add_u32 m0, s72, 8192\n"
    "s_add_u32 s69, s59, 11\n"
    "s_and_b32 s69, s69, 31\n"
    "s_lshl_b32 s69, s69, 16\n"
    "buffer_load_dwordx4 v0, s[44:47], s69 offen lds\n"
    "s_add_u32 m0, m0, 1024\n"
    "s_add_u32 s69, s69, 16384\n"
    "buffer_load_dwordx4 v0, s[44:47], s69 offen lds\n"
    "s_add_u32 m0, m0, 1024\n"
    "s_add_u32 s69, s69, 16384\n"
    "buffer_load_dwordx4 v0, s[44:47], s69 offen lds\n"
    "s_add_u32 m0, m0, 1024\n"
    "s_add_u32 s69, s69, 16384\n"
    "buffer_load_dwordx4 v0, s[44:47], s69 offen lds\n"
    "s_add_u32 m0, m0, 1024\n"
    "s_add_u32 s69, s59, 15\n"
    "s_and_b32 s69, s69, 31\n"
    "s_lshl_b32 s69, s69, 16\n"
    "buffer_load_dwordx4 v0, s[44:47], s69 offen lds\n"
    "s_add_u32 m0, m0, 1024\n"
    "s_add_u32 s69, s69, 16384\n"
    "buffer_load_dwordx4 v0, s[44:47], s69 offen lds\n"
    "s_add_u32 m0, m0, 1024\n"
    "s_add_u32 s69, s69, 16384\n"
    "buffer_load_dwordx4 v0, s[44:47], s69 offen lds\n"
    "s_add_u32 m0, m0, 1024\n"
    "s_add_u32 s69, s69, 16384\n"
    "buffer_load_dwordx4 v0, s[44:47], s69 offen lds\n"
    "s_waitcnt vmcnt(24)\n"
    "ds_read_b64_tr_b16 v[192:193], v1 offset:16384\n"
    "ds_read_b64_tr_b16 v[194:195], v1 offset:18432\n"
    "ds_read_b64_tr_b16 v[196:197], v2 offset:16384\n"
    "ds_read_b64_tr_b16 v[198:199], v2 offset:18432\n"
    "ds_read_b64_tr_b16 v[200:201], v3 offset:16384\n"
    "ds_read_b64_tr_b16 v[202:203], v3 offset:18432\n"
    "ds_read_b64_tr_b16 v[204:205], v4 offset:16384\n"
    "ds_read_b64_tr_b16 v[206:207], v4 offset:18432\n"
    "s_waitcnt lgkmcnt(0)\n"
    "ds_read_b64_tr_b16 v[208:209], v1 offset:20480\n"
    "ds_read_b64_tr_b16 v[210:211], v1 offset:22528\n"
    "ds_read_b64_tr_b16 v[212:213], v2 offset:20480\n"
    "ds_read_b64_tr_b16 v[214:215], v2 offset:22528\n"
    "ds_read_b64_tr_b16 v[216:217], v3 offset:20480\n"
    "ds_read_b64_tr_b16 v[218:219], v3 offset:22528\n"
    "ds_read_b64_tr_b16 v[220:221], v4 offset:20480\n"
    "ds_read_b64_tr_b16 v[222:223], v4 offset:22528\n"
    "s_waitcnt lgkmcnt(0)\n"
    "s_add_u32 m0, s72, 16384\n"
    "s_add_u32 s69, s59, 19\n"
    "s_and_b32 s69, s69, 31\n"
    "s_lshl_b32 s69, s69, 16\n"
    "buffer_load_dwordx4 v0, s[44:47], s69 offen lds\n"
    "s_add_u32 m0, m0, 1024\n"
    "s_add_u32 s69, s69, 16384\n"
    "buffer_load_dwordx4 v0, s[44:47], s69 offen lds\n"
    "s_add_u32 m0, m0, 1024\n"
    "s_add_u32 s69, s69, 16384\n"
    "buffer_load_dwordx4 v0, s[44:47], s69 offen lds\n"
    "s_add_u32 m0, m0, 1024\n"
    "s_add_u32 s69, s69, 16384\n"
    "buffer_load_dwordx4 v0, s[44:47], s69 offen lds\n"
    "s_add_u32 m0, m0, 1024\n"
    "s_add_u32 s69, s59, 23\n"
    "s_and_b32 s69, s69, 31\n"
    "s_lshl_b32 s69, s69, 16\n"
    "buffer_load_dwordx4 v0, s[44:47], s69 offen lds\n"
    "s_add_u32 m0, m0, 1024\n"
    "s_add_u32 s69, s69, 16384\n"
    "buffer_load_dwordx4 v0, s[44:47], s69 offen lds\n"
    "s_add_u32 m0, m0, 1024\n"
    "s_add_u32 s69, s69, 16384\n"
    "buffer_load_dwordx4 v0, s[44:47], s69 offen lds\n"
    "s_add_u32 m0, m0, 1024\n"
    "s_add_u32 s69, s69, 16384\n"
    "buffer_load_dwordx4 v0, s[44:47], s69 offen lds\n"
    "s_waitcnt vmcnt(24)\n"
    "ds_read_b64_tr_b16 v[224:225], v1 offset:24576\n"
    "ds_read_b64_tr_b16 v[226:227], v1 offset:26624\n"
    "ds_read_b64_tr_b16 v[228:229], v2 offset:24576\n"
    "ds_read_b64_tr_b16 v[230:231], v2 offset:26624\n"
    "ds_read_b64_tr_b16 v[232:233], v3 offset:24576\n"
    "ds_read_b64_tr_b16 v[234:235], v3 offset:26624\n"
    "ds_read_b64_tr_b16 v[236:237], v4 offset:24576\n"
    "ds_read_b64_tr_b16 v[238:239], v4 offset:26624\n"
    "s_waitcnt lgkmcnt(0)\n"
    "ds_read_b64_tr_b16 v[240:241], v1 offset:28672\n"
    "ds_read_b64_tr_b16 v[242:243], v1 offset:30720\n"
    "ds_read_b64_tr_b16 v[244:245], v2 offset:28672\n"
    "ds_read_b64_tr_b16 v[246:247], v2 offset:30720\n"
    "ds_read_b64_tr_b16 v[248:249], v3 offset:28672\n"
    "ds_read_b64_tr_b16 v[250:251], v3 offset:30720\n"
    "ds_read_b64_tr_b16 v[252:253], v4 offset:28672\n"
    "ds_read_b64_tr_b16 v[254:255], v4 offset:30720\n"
    "s_waitcnt lgkmcnt(0)\n"
    "s_add_u32 m0, s72, 24576\n"
    "s_add_u32 s69, s59, 27\n"
    "s_and_b32 s69, s69, 31\n"
    "s_lshl_b32 s69, s69, 16\n"
    "buffer_load_dwordx4 v0, s[44:47], s69 offen lds\n"
    "s_add_u32 m0, m0, 1024\n"
    "s_add_u32 s69, s69, 16384\n"
    "buffer_load_dwordx4 v0, s[44:47], s69 offen lds\n"
    "s_add_u32 m0, m0, 1024\n"
    "s_add_u32 s69, s69, 16384\n"
    "buffer_load_dwordx4 v0, s[44:47], s69 offen lds\n"
    "s_add_u32 m0, m0, 1024\n"
    "s_add_u32 s69, s69, 16384\n"
    "buffer_load_dwordx4 v0, s[44:47], s69 offen lds\n"
    "s_add_u32 m0, m0, 1024\n"
    "s_add_u32 s69, s59, 31\n"
    "s_and_b32 s69, s69, 31\n"
    "s_lshl_b32 s69, s69, 16\n"
    "buffer_load_dwordx4 v0, s[44:47], s69 offen lds\n"
    "s_add_u32 m0, m0, 1024\n"
    "s_add_u32 s69, s69, 16384\n"
    "buffer_load_dwordx4 v0, s[44:47], s69 offen lds\n"
    "s_add_u32 m0, m0, 1024\n"
    "s_add_u32 s69, s69, 16384\n"
    "buffer_load_dwordx4 v0, s[44:47], s69 offen lds\n"
    "s_add_u32 m0, m0, 1024\n"
    "s_add_u32 s69, s69, 16384\n"
    "buffer_load_dwordx4 v0, s[44:47], s69 offen lds\n"
    "s_waitcnt vmcnt(24)\n"
    "ds_read_b64_tr_b16 v[64:65], v1 offset:0\n"
    "ds_read_b64_tr_b16 v[66:67], v1 offset:2048\n"
    "ds_read_b64_tr_b16 v[68:69], v2 offset:0\n"
    "ds_read_b64_tr_b16 v[70:71], v2 offset:2048\n"
    "ds_read_b64_tr_b16 v[72:73], v3 offset:0\n"
    "ds_read_b64_tr_b16 v[74:75], v3 offset:2048\n"
    "ds_read_b64_tr_b16 v[76:77], v4 offset:0\n"
    "ds_read_b64_tr_b16 v[78:79], v4 offset:2048\n"
    "s_waitcnt lgkmcnt(0)\n"
    "ds_read_b64_tr_b16 v[80:81], v1 offset:4096\n"
    "ds_read_b64_tr_b16 v[82:83], v1 offset:6144\n"
    "ds_read_b64_tr_b16 v[84:85], v2 offset:4096\n"
    "ds_read_b64_tr_b16 v[86:87], v2 offset:6144\n"
    "ds_read_b64_tr_b16 v[88:89], v3 offset:4096\n"
    "ds_read_b64_tr_b16 v[90:91], v3 offset:6144\n"
    "ds_read_b64_tr_b16 v[92:93], v4 offset:4096\n"
    "ds_read_b64_tr_b16 v[94:95], v4 offset:6144\n"
    "s_waitcnt lgkmcnt(0)\n"
    "ds_write_b128 v7, v[64:67] offset:0\n"
    "ds_write_b128 v7, v[68:71] offset:1024\n"
    "ds_write_b128 v7, v[72:75] offset:2048\n"
    "ds_write_b128 v7, v[76:79] offset:3072\n"
    "ds_write_b128 v7, v[80:83] offset:4096\n"
    "ds_write_b128 v7, v[84:87] offset:5120\n"
    "ds_write_b128 v7, v[88:91] offset:6144\n"
    "ds_write_b128 v7, v[92:95] offset:7168\n"
    "s_waitcnt lgkmcnt(0)\n"
    "s_waitcnt vmcnt(16)\n"
    "ds_read_b64_tr_b16 v[64:65], v1 offset:8192\n"
    "ds_read_b64_tr_b16 v[66:67], v1 offset:10240\n"
    "ds_read_b64_tr_b16 v[68:69], v2 offset:8192\n"
    "ds_read_b64_tr_b16 v[70:71], v2 offset:10240\n"
    "ds_read_b64_tr_b16 v[72:73], v3 offset:8192\n"
    "ds_read_b64_tr_b16 v[74:75], v3 offset:10240\n"
    "ds_read_b64_tr_b16 v[76:77], v4 offset:8192\n"
    "ds_read_b64_tr_b16 v[78:79], v4 offset:10240\n"
    "s_waitcnt lgkmcnt(0)\n"
    "ds_read_b64_tr_b16 v[80:81], v1 offset:12288\n"
    "ds_read_b64_tr_b16 v[82:83], v1 offset:14336\n"
    "ds_read_b64_tr_b16 v[84:85], v2 offset:12288\n"
    "ds_read_b64_tr_b16 v[86:87], v2 offset:14336\n"
    "ds_read_b64_tr_b16 v[88:89], v3 offset:12288\n"
    "ds_read_b64_tr_b16 v[90:91], v3 offset:14336\n"
    "ds_read_b64_tr_b16 v[92:93], v4 offset:12288\n"
    "ds_read_b64_tr_b16 v[94:95], v4 offset:14336\n"
    "s_waitcnt lgkmcnt(0)\n"
    "ds_write_b128 v7, v[64:67] offset:8192\n"
    "ds_write_b128 v7, v[68:71] offset:9216\n"
    "ds_write_b128 v7, v[72:75] offset:10240\n"
    "ds_write_b128 v7, v[76:79] offset:11264\n"
    "ds_write_b128 v7, v[80:83] offset:12288\n"
    "ds_write_b128 v7, v[84:87] offset:13312\n"
    "ds_write_b128 v7, v[88:91] offset:14336\n"
    "ds_write_b128 v7, v[92:95] offset:15360\n"
    "s_waitcnt lgkmcnt(0)\n"
    "s_waitcnt vmcnt(8)\n"
    "ds_read_b64_tr_b16 v[64:65], v1 offset:16384\n"
    "ds_read_b64_tr_b16 v[66:67], v1 offset:18432\n"
    "ds_read_b64_tr_b16 v[68:69], v2 offset:16384\n"
    "ds_read_b64_tr_b16 v[70:71], v2 offset:18432\n"
    "ds_read_b64_tr_b16 v[72:73], v3 offset:16384\n"
    "ds_read_b64_tr_b16 v[74:75], v3 offset:18432\n"
    "ds_read_b64_tr_b16 v[76:77], v4 offset:16384\n"
    "ds_read_b64_tr_b16 v[78:79], v4 offset:18432\n"
    "s_waitcnt lgkmcnt(0)\n"
    "ds_read_b64_tr_b16 v[80:81], v1 offset:20480\n"
    "ds_read_b64_tr_b16 v[82:83], v1 offset:22528\n"
    "ds_read_b64_tr_b16 v[84:85], v2 offset:20480\n"
    "ds_read_b64_tr_b16 v[86:87], v2 offset:22528\n"
    "ds_read_b64_tr_b16 v[88:89], v3 offset:20480\n"
    "ds_read_b64_tr_b16 v[90:91], v3 offset:22528\n"
    "ds_read_b64_tr_b16 v[92:93], v4 offset:20480\n"
    "ds_read_b64_tr_b16 v[94:95], v4 offset:22528\n"
    "s_waitcnt lgkmcnt(0)\n"
    "ds_write_b128 v7, v[64:67] offset:16384\n"
    "ds_write_b128 v7, v[68:71] offset:17408\n"
    "ds_write_b128 v7, v[72:75] offset:18432\n"
    "ds_write_b128 v7, v[76:79] offset:19456\n"
    "ds_write_b128 v7, v[80:83] offset:20480\n"
    "ds_write_b128 v7, v[84:87] offset:21504\n"
    "ds_write_b128 v7, v[88:91] offset:22528\n"
    "ds_write_b128 v7, v[92:95] offset:23552\n"
    "s_waitcnt lgkmcnt(0)\n"
    "s_waitcnt vmcnt(0)\n"
    "ds_read_b64_tr_b16 v[64:65], v1 offset:24576\n"
    "ds_read_b64_tr_b16 v[66:67], v1 offset:26624\n"
    "ds_read_b64_tr_b16 v[68:69], v2 offset:24576\n"
    "ds_read_b64_tr_b16 v[70:71], v2 offset:26624\n"
    "ds_read_b64_tr_b16 v[72:73], v3 offset:24576\n"
    "ds_read_b64_tr_b16 v[74:75], v3 offset:26624\n"
    "ds_read_b64_tr_b16 v[76:77], v4 offset:24576\n"
    "ds_read_b64_tr_b16 v[78:79], v4 offset:26624\n"
    "s_waitcnt lgkmcnt(0)\n"
    "ds_read_b64_tr_b16 v[80:81], v1 offset:28672\n"
    "ds_read_b64_tr_b16 v[82:83], v1 offset:30720\n"
    "ds_read_b64_tr_b16 v[84:85], v2 offset:28672\n"
    "ds_read_b64_tr_b16 v[86:87], v2 offset:30720\n"
    "ds_read_b64_tr_b16 v[88:89], v3 offset:28672\n"
    "ds_read_b64_tr_b16 v[90:91], v3 offset:30720\n"
    "ds_read_b64_tr_b16 v[92:93], v4 offset:28672\n"
    "ds_read_b64_tr_b16 v[94:95], v4 offset:30720\n"
    "s_waitcnt lgkmcnt(0)\n"
    "ds_write_b128 v7, v[64:67] offset:24576\n"
    "ds_write_b128 v7, v[68:71] offset:25600\n"
    "ds_write_b128 v7, v[72:75] offset:26624\n"
    "ds_write_b128 v7, v[76:79] offset:27648\n"
    "ds_write_b128 v7, v[80:83] offset:28672\n"
    "ds_write_b128 v7, v[84:87] offset:29696\n"
    "ds_write_b128 v7, v[88:91] offset:30720\n"
    "ds_write_b128 v7, v[92:95] offset:31744\n"
    "s_waitcnt lgkmcnt(0)\n"
    "s_mov_b32 s68, 0\n"
    "v_mov_b32 v50, 1573120\n"
    "buffer_load_dwordx2 v[48:49], v50, s[40:43], 0 offen sc1\n"
    "v_mov_b32 v51, 1576704\n"
    ".LS_sn%=:\n"
    "buffer_load_dword v12, v51, s[40:43], 0 offen sc1\n"
    "s_waitcnt vmcnt(0)\n"
    "v_cmp_gt_u32 vcc, 160, v12\n"
    "s_cbranch_vccnz .LS_snr%=\n"
    "v_readfirstlane_b32 s66, v48\n"
    "v_readfirstlane_b32 s67, v49\n"
    "s_nop 0\n"
    "s_mul_i32 s69, s67, 0x85ebca77\n"
    "s_mul_i32 s66, s66, 0x9e3779b1\n"
    "s_add_u32 s66, s66, s69\n"
    "s_add_u32 s66, s66, 0x7f4a7c15\n"
    "s_lshr_b32 s69, s66, 15\n"
    "s_xor_b32 s66, s66, s69\n"
    "s_mul_i32 s69, s66, 0x27d4eb2f\n"
    "s_mul_i32 s67, s67, 0xc2b2ae3d\n"
    "s_add_u32 s67, s67, s69\n"
    "s_add_u32 s67, s67, 0x165667b1\n"
    "s_lshr_b32 s69, s67, 13\n"
    "s_xor_b32 s67, s67, s69\n"
    "s_andn2_b32 s66, s66, 1\n"
    "s_add_u32 s69, s59, 8\n"
    "s_cmp_eq_u32 s69, 32\n"
    "s_cselect_b32 s74, 0xffff0000, 0\n"
    "s_add_u32 s74, s74, 4096\n"
    "s_add_u32 s69, s59, 16\n"
    "s_cmp_eq_u32 s69, 32\n"
    "s_cselect_b32 s75, 0xffff0000, 0\n"
    "s_add_u32 s75, s75, 4096\n"
    "s_add_u32 s69, s59, 24\n"
    "s_cmp_eq_u32 s69, 32\n"
    "s_cselect_b32 s76, 0xffff0000, 0\n"
    "s_add_u32 s76, s76, 4096\n"
    "s_add_u32 s69, s59, 32\n"
    "s_cmp_eq_u32 s69, 32\n"
    "s_cselect_b32 s77, 0xffff0000, 0\n"
    "s_add_u32 s77, s77, 266240\n"
    ".LS_cold%=:\n"
    "s_lshl_b32 s61, s57, 18\n"
    "s_lshl_b32 s69, s59, 11\n"
    "s_add_u32 s61, s61, s69\n"
    "s_mov_b32 s68, 0\n"
    "s_lshl_b32 s62, s57, 8\n"
    ".LS_fl%=:\n"
    "buffer_load_dword v12, v6, s[40:43], s62 offen sc1\n"
    "s_waitcnt vmcnt(0)\n"
    "v_cmp_gt_u32 vcc, 32, v12\n"
    "s_cbranch_vccnz .LS_flr%=\n"
    "buffer_load_dwordx4 v[64:67], v5, s[40:43], s61 offen offset:0 sc1\n"
    "buffer_load_dwordx4 v[68:71], v5, s[40:43], s61 offen offset:2048 sc1\n"
    "s_add_u32 s61, s61, 4096\n"
    "buffer_load_dwordx4 v[72:75], v5, s[40:43], s61 offen offset:0 sc1\n"
    "buffer_load_dwordx4 v[76:79], v5, s[40:43], s61 offen offset:2048 sc1\n"
    "s_add_u32 s61, s61, 4096\n"
    "buffer_load_dwordx4 v[80:83], v5, s[40:43], s61 offen offset:0 sc1\n"
    "buffer_load_dwordx4 v[84:87], v5, s[40:43], s61 offen offset:2048 sc1\n"
    "s_add_u32 s61, s61, 4096\n"
    "buffer_load_dwordx4 v[88:91], v5, s[40:43], s61 offen offset:0 sc1\n"
    "buffer_load_dwordx4 v[92:95], v5, s[40:43], s61 offen offset:2048 sc1\n"
    "s_add_u32 s61, s61, s74\n"
    "buffer_load_dwordx4 v[96:99], v5, s[40:43], s61 offen offset:0 sc1\n"
    "buffer_load_dwordx4 v[100:103], v5, s[40:43], s61 offen offset:2048 sc1\n"
    "s_add_u32 s61, s61, 4096\n"
    "buffer_load_dwordx4 v[104:107], v5, s[40:43], s61 offen offset:0 sc1\n"
    "buffer_load_dwordx4 v[108:111], v5, s[40:43], s61 offen offset:2048 sc1\n"
    "s_add_u32 s61, s61, 4096\n"
    "buffer_load_dwordx4 v[112:115], v5, s[40:43], s61 offen offset:0 sc1\n"
    "buffer_load_dwordx4 v[116:119], v5, s[40:43], s61 offen offset:2048 sc1\n"
    "s_add_u32 s61, s61, 4096\n"
    "buffer_load_dwordx4 v[120:123], v5, s[40:43], s61 offen offset:0 sc1\n"
    "buffer_load_dwordx4 v[124:127], v5, s[40:43], s61 offen offset:2048 sc1\n"
    "s_add_u32 s61, s61, s75\n"
    "buffer_load_dword v12, v6, s[40:43], s62 offen sc1\n"
    ".LS_top%=:\n"
    "s_add_u32 s62, s57, 1\n"
    "s_cmp_lt_u32 s62, s58\n"
    "s_cselect_b32 s62, s62, 64\n"
    "s_lshl_b32 s62, s62, 8\n"
    "buffer_load_dword v12, v6, s[40:43], s62 offen sc1\n"
    "s_waitcnt vmcnt(17)\n"
    "v_mfma_f32_16x16x32_bf16 v[16:19], a[0:3], v[64:67], 0\n"
    "ds_read_b128 v[32:35], v7 offset:0\n"
    "v_mfma_f32_16x16x32_bf16 v[20:23], a[4:7], v[64:67], 0\n"
    "ds_read_b128 v[36:39], v7 offset:1024\n"
    "v_mfma_f32_16x16x32_bf16 v[24:27], a[8:11], v[64:67], 0\n"
    "v_mfma_f32_16x16x32_bf16 v[28:31], a[12:15], v[64:67], 0\n"
    "buffer_load_dwordx4 v[64:67], v5, s[40:43], s61 offen offset:0 sc1\n"
    "s_waitcnt vmcnt(17)\n"
    "v_mfma_f32_16x16x32_bf16 v[16:19], a[16:19], v[68:71], v[16:19]\n"
    "ds_read_b128 v[40:43], v7 offset:2048\n"
    "v_mfma_f32_16x16x32_bf16 v[20:23], a[20:23], v[68:71], v[20:23]\n"
    "ds_read_b128 v[44:47], v7 offset:3072\n"
    "v_mfma_f32_16x16x32_bf16 v[24:27], a[24:27], v[68:71], v[24:27]\n"
    "v_mfma_f32_16x16x32_bf16 v[28:31], a[28:31], v[68:71], v[28:31]\n"
    "buffer_load_dwordx4 v[68:71], v5, s[40:43], s61 offen offset:2048 sc1\n"
    "s_add_u32 s61, s61, 4096\n"
    "s_waitcnt vmcnt(17)\n"
    "v_mfma_f32_16x16x32_bf16 v[16:19], a[32:35], v[72:75], v[16:19]\n"
    "v_mfma_f32_16x16x32_bf16 v[20:23], a[36:39], v[72:75], v[20:23]\n"
    "v_mfma_f32_16x16x32_bf16 v[24:27], a[40:43], v[72:75], v[24:27]\n"
    "v_mfma_f32_16x16x32_bf16 v[28:31], a[44:47], v[72:75], v[28:31]\n"
    "buffer_load_dwordx4 v[72:75], v5, s[40:43], s61 offen offset:0 sc1\n"
    "s_waitcnt vmcnt(17)\n"
    "s_waitcnt lgkmcnt(0)\n"
    "v_mfma_f32_16x16x32_bf16 v[16:19], v[32:35], v[76:79], v[16:19]\n"
    "v_mfma_f32_16x16x32_bf16 v[20:23], v[36:39], v[76:79], v[20:23]\n"
    "v_mfma_f32_16x16x32_bf16 v[24:27], v[40:43], v[76:79], v[24:27]\n"
    "v_mfma_f32_16x16x32_bf16 v[28:31], v[44:47], v[76:79], v[28:31]\n"
    "buffer_load_dwordx4 v[76:79], v5, s[40:43], s61 offen offset:2048 sc1\n"
    "s_add_u32 s61, s61, 4096\n"
    "s_waitcnt vmcnt(17)\n"
    "v_mfma_f32_16x16x32_bf16 v[16:19], a[48:51], v[80:83], v[16:19]\n"
    "ds_read_b128 v[32:35], v7 offset:4096\n"
    "v_mfma_f32_16x16x32_bf16 v[20:23], a[52:55], v[80:83], v[20:23]\n"
    "ds_read_b128 v[36:39], v7 offset:5120\n"
    "v_mfma_f32_16x16x32_bf16 v[24:27], a[56:59], v[80:83], v[24:27]\n"
    "v_mfma_f32_16x16x32_bf16 v[28:31], a[60:63], v[80:83], v[28:31]\n"
    "buffer_load_dwordx4 v[80:83], v5, s[40:43], s61 offen offset:0 sc1\n"
    "s_waitcnt vmcnt(17)\n"
    "v_mfma_f32_16x16x32_bf16 v[16:19], a[64:67], v[84:87], v[16:19]\n"
    "ds_read_b128 v[40:43], v7 offset:6144\n"
    "v_mfma_f32_16x16x32_bf16 v[20:23], a[68:71], v[84:87], v[20:23]\n"
    "ds_read_b128 v[44:47], v7 offset:7168\n"
    "v_mfma_f32_16x16x32_bf16 v[24:27], a[72:75], v[84:87], v[24:27]\n"
    "v_mfma_f32_16x16x32_bf16 v[28:31], a[76:79], v[84:87], v[28:31]\n"
    "buffer_load_dwordx4 v[84:87], v5, s[40:43], s61 offen offset:2048 sc1\n"
    "s_add_u32 s61, s61, 4096\n"
    "s_waitcnt vmcnt(17)\n"
    "v_mfma_f32_16x16x32_bf16 v[16:19], a[80:83], v[88:91], v[16:19]\n"
    "v_mfma_f32_16x16x32_bf16 v[20:23], a[84:87], v[88:91], v[20:23]\n"
    "v_mfma_f32_16x16x32_bf16 v[24:27], a[88:91], v[88:91], v[24:27]\n"
    "v_mfma_f32_16x16x32_bf16 v[28:31], a[92:95], v[88:91], v[28:31]\n"
    "buffer_load_dwordx4 v[88:91], v5, s[40:43], s61 offen offset:0 sc1\n"
    "s_waitcnt vmcnt(17)\n"
    "s_waitcnt lgkmcnt(0)\n"
    "v_mfma_f32_16x16x32_bf16 v[16:19], v[32:35], v[92:95], v[16:19]\n"
    "v_mfma_f32_16x16x32_bf16 v[20:23], v[36:39], v[92:95], v[20:23]\n"
    "v_mfma_f32_16x16x32_bf16 v[24:27], v[40:43], v[92:95], v[24:27]\n"
    "v_mfma_f32_16x16x32_bf16 v[28:31], v[44:47], v[92:95], v[28:31]\n"
    "buffer_load_dwordx4 v[92:95], v5, s[40:43], s61 offen offset:2048 sc1\n"
    "s_add_u32 s61, s61, s76\n"
    "s_waitcnt vmcnt(17)\n"
    "v_mfma_f32_16x16x32_bf16 v[16:19], a[96:99], v[96:99], v[16:19]\n"
    "ds_read_b128 v[32:35], v7 offset:8192\n"
    "v_mfma_f32_16x16x32_bf16 v[20:23], a[100:103], v[96:99], v[20:23]\n"
    "ds_read_b128 v[36:39], v7 offset:9216\n"
    "v_mfma_f32_16x16x32_bf16 v[24:27], a[104:107], v[96:99], v[24:27]\n"
    "v_mfma_f32_16x16x32_bf16 v[28:31], a[108:111], v[96:99], v[28:31]\n"
    "buffer_load_dwordx4 v[96:99], v5, s[40:43], s61 offen offset:0 sc1\n"
    "s_waitcnt vmcnt(17)\n"
    "v_mfma_f32_16x16x32_bf16 v[16:19], a[112:115], v[100:103], v[16:19]\n"
    "ds_read_b128 v[40:43], v7 offset:10240\n"
    "v_mfma_f32_16x16x32_bf16 v[20:23], a[116:119], v[100:103], v[20:23]\n"
    "ds_read_b128 v[44:47], v7 offset:11264\n"
    "v_mfma_f32_16x16x32_bf16 v[24:27], a[120:123], v[100:103], v[24:27]\n"
    "v_mfma_f32_16x16x32_bf16 v[28:31], a[124:127], v[100:103], v[28:31]\n"
    "buffer_load_dwordx4 v[100:103], v5, s[40:43], s61 offen offset:2048 sc1\n"
    "s_add_u32 s61, s61, 4096\n"
    "s_waitcnt vmcnt(17)\n"
    "v_mfma_f32_16x16x32_bf16 v[16:19], a[128:131], v[104:107], v[16:19]\n"
    "v_mfma_f32_16x16x32_bf16 v[20:23], a[132:135], v[104:107], v[20:23]\n"
    "v_mfma_f32_16x16x32_bf16 v[24:27], a[136:139], v[104:107], v[24:27]\n"
    "v_mfma_f32_16x16x32_bf16 v[28:31], a[140:143], v[104:107], v[28:31]\n"
    "buffer_load_dwordx4 v[104:107], v5, s[40:43], s61 offen offset:0 sc1\n"
    "s_waitcnt vmcnt(17)\n"
    "s_waitcnt lgkmcnt(0)\n"
    "v_mfma_f32_16x16x32_bf16 v[16:19], v[32:35], v[108:111], v[16:19]\n"
    "v_mfma_f32_16x16x32_bf16 v[20:23], v[36:39], v[108:111], v[20:23]\n"
    "v_mfma_f32_16x16x32_bf16 v[24:27], v[40:43], v[108:111], v[24:27]\n"
    "v_mfma_f32_16x16x32_bf16 v[28:31], v[44:47], v[108:111], v[28:31]\n"
    "buffer_load_dwordx4 v[108:111], v5, s[40:43], s61 offen offset:2048 sc1\n"
    "s_add_u32 s61, s61, 4096\n"
    "s_waitcnt vmcnt(17)\n"
    "v_mfma_f32_16x16x32_bf16 v[16:19], a[144:147], v[112:115], v[16:19]\n"
    "ds_read_b128 v[32:35], v7 offset:12288\n"
    "v_mfma_f32_16x16x32_bf16 v[20:23], a[148:151], v[112:115], v[20:23]\n"
    "ds_read_b128 v[36:39], v7 offset:13312\n"
    "v_mfma_f32_16x16x32_bf16 v[24:27], a[152:155], v[112:115], v[24:27]\n"
    "v_mfma_f32_16x16x32_bf16 v[28:31], a[156:159], v[112:115], v[28:31]\n"
    "buffer_load_dwordx4 v[112:115], v5, s[40:43], s61 offen offset:0 sc1\n"
    "s_waitcnt vmcnt(17)\n"
    "v_mfma_f32_16x16x32_bf16 v[16:19], a[160:163], v[116:119], v[16:19]\n"
    "ds_read_b128 v[40:43], v7 offset:14336\n"
    "v_mfma_f32_16x16x32_bf16 v[20:23], a[164:167], v[116:119], v[20:23]\n"
    "ds_read_b128 v[44:47], v7 offset:15360\n"
    "v_mfma_f32_16x16x32_bf16 v[24:27], a[168:171], v[116:119], v[24:27]\n"
    "v_mfma_f32_16x16x32_bf16 v[28:31], a[172:175], v[116:119], v[28:31]\n"
    "buffer_load_dwordx4 v[116:119], v5, s[40:43], s61 offen offset:2048 sc1\n"
    "s_add_u32 s61, s61, 4096\n"
    "s_waitcnt vmcnt(17)\n"
    "v_mfma_f32_16x16x32_bf16 v[16:19], a[176:179], v[120:123], v[16:19]\n"
    "v_mfma_f32_16x16x32_bf16 v[20:23], a[180:183], v[120:123], v[20:23]\n"
    "v_mfma_f32_16x16x32_bf16 v[24:27], a[184:187], v[120:123], v[24:27]\n"
    "v_mfma_f32_16x16x32_bf16 v[28:31], a[188:191], v[120:123], v[28:31]\n"
    "buffer_load_dwordx4 v[120:123], v5, s[40:43], s61 offen offset:0 sc1\n"
    "s_waitcnt vmcnt(17)\n"
    "s_waitcnt lgkmcnt(0)\n"
    "v_mfma_f32_16x16x32_bf16 v[16:19], v[32:35], v[124:127], v[16:19]\n"
    "v_mfma_f32_16x16x32_bf16 v[20:23], v[36:39], v[124:127], v[20:23]\n"
    "v_mfma_f32_16x16x32_bf16 v[24:27], v[40:43], v[124:127], v[24:27]\n"
    "v_mfma_f32_16x16x32_bf16 v[28:31], v[44:47], v[124:127], v[28:31]\n"
    "buffer_load_dwordx4 v[124:127], v5, s[40:43], s61 offen offset:2048 sc1\n"
    "s_add_u32 s61, s61, s77\n"
    "s_waitcnt vmcnt(16)\n"
    "v_cmp_gt_u32 vcc, 32, v12\n"
    "s_cbranch_vccnz .LS_np%=\n"
    "s_waitcnt vmcnt(15)\n"
    "v_mfma_f32_16x16x32_bf16 v[16:19], a[192:195], v[64:67], v[16:19]\n"
    "ds_read_b128 v[32:35], v7 offset:16384\n"
    "v_mfma_f32_16x16x32_bf16 v[20:23], a[196:199], v[64:67], v[20:23]\n"
    "ds_read_b128 v[36:39], v7 offset:17408\n"
    "v_mfma_f32_16x16x32_bf16 v[24:27], a[200:203], v[64:67], v[24:27]\n"
    "v_mfma_f32_16x16x32_bf16 v[28:31], a[204:207], v[64:67], v[28:31]\n"
    "buffer_load_dwordx4 v[64:67], v5, s[40:43], s61 offen offset:0 sc1\n"
    "s_waitcnt vmcnt(15)\n"
    "v_mfma_f32_16x16x32_bf16 v[16:19], a[208:211], v[68:71], v[16:19]\n"
    "ds_read_b128 v[40:43], v7 offset:18432\n"
    "v_mfma_f32_16x16x32_bf16 v[20:23], a[212:215], v[68:71], v[20:23]\n"
    "ds_read_b128 v[44:47], v7 offset:19456\n"
    "v_mfma_f32_16x16x32_bf16 v[24:27], a[216:219], v[68:71], v[24:27]\n"
    "v_mfma_f32_16x16x32_bf16 v[28:31], a[220:223], v[68:71], v[28:31]\n"
    "buffer_load_dwordx4 v[68:71], v5, s[40:43], s61 offen offset:2048 sc1\n"
    "s_add_u32 s61, s61, 4096\n"
    "s_waitcnt vmcnt(15)\n"
    "v_mfma_f32_16x16x32_bf16 v[16:19], a[224:227], v[72:75], v[16:19]\n"
    "v_mfma_f32_16x16x32_bf16 v[20:23], a[228:231], v[72:75], v[20:23]\n"
    "v_mfma_f32_16x16x32_bf16 v[24:27], a[232:235], v[72:75], v[24:27]\n"
    "v_mfma_f32_16x16x32_bf16 v[28:31], a[236:239], v[72:75], v[28:31]\n"
    "buffer_load_dwordx4 v[72:75], v5, s[40:43], s61 offen offset:0 sc1\n"
    "s_waitcnt vmcnt(15)\n"
    "s_waitcnt lgkmcnt(0)\n"
    "v_mfma_f32_16x16x32_bf16 v[16:19], v[32:35], v[76:79], v[16:19]\n"
    "v_mfma_f32_16x16x32_bf16 v[20:23], v[36:39], v[76:79], v[20:23]\n"
    "v_mfma_f32_16x16x32_bf16 v[24:27], v[40:43], v[76:79], v[24:27]\n"
    "v_mfma_f32_16x16x32_bf16 v[28:31], v[44:47], v[76:79], v[28:31]\n"
    "buffer_load_dwordx4 v[76:79], v5, s[40:43], s61 offen offset:2048 sc1\n"
    "s_add_u32 s61, s61, 4096\n"
    "s_waitcnt vmcnt(15)\n"
    "v_mfma_f32_16x16x32_bf16 v[16:19], a[240:243], v[80:83], v[16:19]\n"
    "ds_read_b128 v[32:35], v7 offset:20480\n"
    "v_mfma_f32_16x16x32_bf16 v[20:23], a[244:247], v[80:83], v[20:23]\n"
    "ds_read_b128 v[36:39], v7 offset:21504\n"
    "v_mfma_f32_16x16x32_bf16 v[24:27], a[248:251], v[80:83], v[24:27]\n"
    "v_mfma_f32_16x16x32_bf16 v[28:31], a[252:255], v[80:83], v[28:31]\n"
    "buffer_load_dwordx4 v[80:83], v5, s[40:43], s61 offen offset:0 sc1\n"
    "s_waitcnt vmcnt(15)\n"
    "v_mfma_f32_16x16x32_bf16 v[16:19], v[128:131], v[84:87], v[16:19]\n"
    "ds_read_b128 v[40:43], v7 offset:22528\n"
    "v_mfma_f32_16x16x32_bf16 v[20:23], v[132:135], v[84:87], v[20:23]\n"
    "ds_read_b128 v[44:47], v7 offset:23552\n"
    "v_mfma_f32_16x16x32_bf16 v[24:27], v[136:139], v[84:87], v[24:27]\n"
    "v_mfma_f32_16x16x32_bf16 v[28:31], v[140:143], v[84:87], v[28:31]\n"
    "buffer_load_dwordx4 v[84:87], v5, s[40:43], s61 offen offset:2048 sc1\n"
    "s_add_u32 s61, s61, 4096\n"
    "s_waitcnt vmcnt(15)\n"
    "v_mfma_f32_16x16x32_bf16 v[16:19], v[144:147], v[88:91], v[16:19]\n"
    "v_mfma_f32_16x16x32_bf16 v[20:23], v[148:151], v[88:91], v[20:23]\n"
    "v_mfma_f32_16x16x32_bf16 v[24:27], v[152:155], v[88:91], v[24:27]\n"
    "v_mfma_f32_16x16x32_bf16 v[28:31], v[156:159], v[88:91], v[28:31]\n"
    "buffer_load_dwordx4 v[88:91], v5, s[40:43], s61 offen offset:0 sc1\n"
    "s_waitcnt vmcnt(15)\n"
    "s_waitcnt lgkmcnt(0)\n"
    "v_mfma_f32_16x16x32_bf16 v[16:19], v[32:35], v[92:95], v[16:19]\n"
    "v_mfma_f32_16x16x32_bf16 v[20:23], v[36:39], v[92:95], v[20:23]\n"
    "v_mfma_f32_16x16x32_bf16 v[24:27], v[40:43], v[92:95], v[24:27]\n"
    "v_mfma_f32_16x16x32_bf16 v[28:31], v[44:47], v[92:95], v[28:31]\n"
    "buffer_load_dwordx4 v[92:95], v5, s[40:43], s61 offen offset:2048 sc1\n"
    "s_add_u32 s61, s61, s74\n"
    "s_waitcnt vmcnt(15)\n"
    "v_mfma_f32_16x16x32_bf16 v[16:19], v[160:163], v[96:99], v[16:19]\n"
    "ds_read_b128 v[32:35], v7 offset:24576\n"
    "v_mfma_f32_16x16x32_bf16 v[20:23], v[164:167], v[96:99], v[20:23]\n"
    "ds_read_b128 v[36:39], v7 offset:25600\n"
    "v_mfma_f32_16x16x32_bf16 v[24:27], v[168:171], v[96:99], v[24:27]\n"
    "v_mfma_f32_16x16x32_bf16 v[28:31], v[172:175], v[96:99], v[28:31]\n"
    "buffer_load_dwordx4 v[96:99], v5, s[40:43], s61 offen offset:0 sc1\n"
    "s_waitcnt vmcnt(15)\n"
    "v_mfma_f32_16x16x32_bf16 v[16:19], v[176:179], v[100:103], v[16:19]\n"
    "ds_read_b128 v[40:43], v7 offset:26624\n"
    "v_mfma_f32_16x16x32_bf16 v[20:23], v[180:183], v[100:103], v[20:23]\n"
    "ds_read_b128 v[44:47], v7 offset:27648\n"
    "v_mfma_f32_16x16x32_bf16 v[24:27], v[184:187], v[100:103], v[24:27]\n"
    "v_mfma_f32_16x16x32_bf16 v[28:31], v[188:191], v[100:103], v[28:31]\n"
    "buffer_load_dwordx4 v[100:103], v5, s[40:43], s61 offen offset:2048 sc1\n"
    "s_add_u32 s61, s61, 4096\n"
    "s_waitcnt vmcnt(15)\n"
    "v_mfma_f32_16x16x32_bf16 v[16:19], v[192:195], v[104:107], v[16:19]\n"
    "v_mfma_f32_16x16x32_bf16 v[20:23], v[196:199], v[104:107], v[20:23]\n"
    "v_mfma_f32_16x16x32_bf16 v[24:27], v[200:203], v[104:107], v[24:27]\n"
    "v_mfma_f32_16x16x32_bf16 v[28:31], v[204:207], v[104:107], v[28:31]\n"
    "buffer_load_dwordx4 v[104:107], v5, s[40:43], s61 offen offset:0 sc1\n"
    "s_waitcnt vmcnt(15)\n"
    "s_waitcnt lgkmcnt(0)\n"
    "v_mfma_f32_16x16x32_bf16 v[16:19], v[32:35], v[108:111], v[16:19]\n"
    "v_mfma_f32_16x16x32_bf16 v[20:23], v[36:39], v[108:111], v[20:23]\n"
    "v_mfma_f32_16x16x32_bf16 v[24:27], v[40:43], v[108:111], v[24:27]\n"
    "v_mfma_f32_16x16x32_bf16 v[28:31], v[44:47], v[108:111], v[28:31]\n"
    "buffer_load_dwordx4 v[108:111], v5, s[40:43], s61 offen offset:2048 sc1\n"
    "s_add_u32 s61, s61, 4096\n"
    "s_waitcnt vmcnt(15)\n"
    "v_mfma_f32_16x16x32_bf16 v[16:19], v[208:211], v[112:115], v[16:19]\n"
    "ds_read_b128 v[32:35], v7 offset:28672\n"
    "v_mfma_f32_16x16x32_bf16 v[20:23], v[212:215], v[112:115], v[20:23]\n"
    "ds_read_b128 v[36:39], v7 offset:29696\n"
    "v_mfma_f32_16x16x32_bf16 v[24:27], v[216:219], v[112:115], v[24:27]\n"
    "v_mfma_f32_16x16x32_bf16 v[28:31], v[220:223], v[112:115], v[28:31]\n"
    "buffer_load_dwordx4 v[112:115], v5, s[40:43], s61 offen offset:0 sc1\n"
    "s_waitcnt vmcnt(15)\n"
    "v_mfma_f32_16x16x32_bf16 v[16:19], v[224:227], v[116:119], v[16:19]\n"
    "ds_read_b128 v[40:43], v7 offset:30720\n"
    "v_mfma_f32_16x16x32_bf16 v[20:23], v[228:231], v[116:119], v[20:23]\n"
    "ds_read_b128 v[44:47], v7 offset:31744\n"
    "v_mfma_f32_16x16x32_bf16 v[24:27], v[232:235], v[116:119], v[24:27]\n"
    "v_mfma_f32_16x16x32_bf16 v[28:31], v[236:239], v[116:119], v[28:31]\n"
    "buffer_load_dwordx4 v[116:119], v5, s[40:43], s61 offen offset:2048 sc1\n"
    "s_add_u32 s61, s61, 4096\n"
    "s_waitcnt vmcnt(15)\n"
    "v_mfma_f32_16x16x32_bf16 v[16:19], v[240:243], v[120:123], v[16:19]\n"
    "v_mfma_f32_16x16x32_bf16 v[20:23], v[244:247], v[120:123], v[20:23]\n"
    "v_mfma_f32_16x16x32_bf16 v[24:27], v[248:251], v[120:123], v[24:27]\n"
    "v_mfma_f32_16x16x32_bf16 v[28:31], v[252:255], v[120:123], v[28:31]\n"
    "buffer_load_dwordx4 v[120:123], v5, s[40:43], s61 offen offset:0 sc1\n"
    "s_waitcnt vmcnt(15)\n"
    "s_waitcnt lgkmcnt(0)\n"
    "v_mfma_f32_16x16x32_bf16 v[16:19], v[32:35], v[124:127], v[16:19]\n"
    "v_mfma_f32_16x16x32_bf16 v[20:23], v[36:39], v[124:127], v[20:23]\n"
    "v_mfma_f32_16x16x32_bf16 v[24:27], v[40:43], v[124:127], v[24:27]\n"
    "v_mfma_f32_16x16x32_bf16 v[28:31], v[44:47], v[124:127], v[28:31]\n"
    "buffer_load_dwordx4 v[124:127], v5, s[40:43], s61 offen offset:2048 sc1\n"
    "s_add_u32 s61, s61, s75\n"
    "ds_write_b128 v8, v[16:19] offset:0\n"
    "ds_write_b128 v8, v[20:23] offset:1024\n"
    "s_nop 0\n"
    "ds_write_b128 v8, v[24:27] offset:2048\n"
    "s_nop 1\n"
    "ds_write_b128 v8, v[28:31] offset:3072\n"
    "s_waitcnt lgkmcnt(0)\n"
    "s_barrier\n"
    "ds_read_b128 v[48:51], v9 offset:0\n"
    "ds_read_b128 v[52:55], v9 offset:4096\n"
    "ds_read_b128 v[56:59], v9 offset:8192\n"
    "ds_read_b128 v[60:63], v9 offset:12288\n"
    "v_xor_b32 v8, 0x4000, v8\n"
    "v_xor_b32 v9, 0x4000, v9\n"
    "s_lshl_b32 s69, s57, 18\n"
    "s_add_u32 s57, s57, 1\n"
    "s_waitcnt lgkmcnt(0)\n"
    "v_pk_add_f32 v[48:49], v[48:49], v[52:53]\n"
    "v_pk_add_f32 v[50:51], v[50:51], v[54:55]\n"
    "v_pk_add_f32 v[56:57], v[56:57], v[60:61]\n"
    "v_pk_add_f32 v[58:59], v[58:59], v[62:63]\n"
    "v_pk_add_f32 v[48:49], v[48:49], v[56:57]\n"
    "v_pk_add_f32 v[50:51], v[50:51], v[58:59]\n"
    "buffer_store_dwordx4 v[48:51], v10, s[48:51], s69 offen sc1\n"
    "s_branch .LS_top%=\n"
    ".LS_np%=:\n"
    "s_waitcnt vmcnt(15)\n"
    "v_mfma_f32_16x16x32_bf16 v[16:19], a[192:195], v[64:67], v[16:19]\n"
    "ds_read_b128 v[32:35], v7 offset:16384\n"
    "v_mfma_f32_16x16x32_bf16 v[20:23], a[196:199], v[64:67], v[20:23]\n"
    "ds_read_b128 v[36:39], v7 offset:17408\n"
    "v_mfma_f32_16x16x32_bf16 v[24:27], a[200:203], v[64:67], v[24:27]\n"
    "v_mfma_f32_16x16x32_bf16 v[28:31], a[204:207], v[64:67], v[28:31]\n"
    "s_waitcnt vmcnt(14)\n"
    "v_mfma_f32_16x16x32_bf16 v[16:19], a[208:211], v[68:71], v[16:19]\n"
    "ds_read_b128 v[40:43], v7 offset:18432\n"
    "v_mfma_f32_16x16x32_bf16 v[20:23], a[212:215], v[68:71], v[20:23]\n"
    "ds_read_b128 v[44:47], v7 offset:19456\n"
    "v_mfma_f32_16x16x32_bf16 v[24:27], a[216:219], v[68:71], v[24:27]\n"
    "v_mfma_f32_16x16x32_bf16 v[28:31], a[220:223], v[68:71], v[28:31]\n"
    "s_waitcnt vmcnt(13)\n"
    "v_mfma_f32_16x16x32_bf16 v[16:19], a[224:227], v[72:75], v[16:19]\n"
    "v_mfma_f32_16x16x32_bf16 v[20:23], a[228:231], v[72:75], v[20:23]\n"
    "v_mfma_f32_16x16x32_bf16 v[24:27], a[232:235], v[72:75], v[24:27]\n"
    "v_mfma_f32_16x16x32_bf16 v[28:31], a[236:239], v[72:75], v[28:31]\n"
    "s_waitcnt vmcnt(12)\n"
    "s_waitcnt lgkmcnt(0)\n"
    "v_mfma_f32_16x16x32_bf16 v[16:19], v[32:35], v[76:79], v[16:19]\n"
    "v_mfma_f32_16x16x32_bf16 v[20:23], v[36:39], v[76:79], v[20:23]\n"
    "v_mfma_f32_16x16x32_bf16 v[24:27], v[40:43], v[76:79], v[24:27]\n"
    "v_mfma_f32_16x16x32_bf16 v[28:31], v[44:47], v[76:79], v[28:31]\n"
    "s_waitcnt vmcnt(11)\n"
    "v_mfma_f32_16x16x32_bf16 v[16:19], a[240:243], v[80:83], v[16:19]\n"
    "ds_read_b128 v[32:35], v7 offset:20480\n"
    "v_mfma_f32_16x16x32_bf16 v[20:23], a[244:247], v[80:83], v[20:23]\n"
    "ds_read_b128 v[36:39], v7 offset:21504\n"
    "v_mfma_f32_16x16x32_bf16 v[24:27], a[248:251], v[80:83], v[24:27]\n"
    "v_mfma_f32_16x16x32_bf16 v[28:31], a[252:255], v[80:83], v[28:31]\n"
    "s_waitcnt vmcnt(10)\n"
    "v_mfma_f32_16x16x32_bf16 v[16:19], v[128:131], v[84:87], v[16:19]\n"
    "ds_read_b128 v[40:43], v7 offset:22528\n"
    "v_mfma_f32_16x16x32_bf16 v[20:23], v[132:135], v[84:87], v[20:23]\n"
    "ds_read_b128 v[44:47], v7 offset:23552\n"
    "v_mfma_f32_16x16x32_bf16 v[24:27], v[136:139], v[84:87], v[24:27]\n"
    "v_mfma_f32_16x16x32_bf16 v[28:31], v[140:143], v[84:87], v[28:31]\n"
    "s_waitcnt vmcnt(9)\n"
    "v_mfma_f32_16x16x32_bf16 v[16:19], v[144:147], v[88:91], v[16:19]\n"
    "v_mfma_f32_16x16x32_bf16 v[20:23], v[148:151], v[88:91], v[20:23]\n"
    "v_mfma_f32_16x16x32_bf16 v[24:27], v[152:155], v[88:91], v[24:27]\n"
    "v_mfma_f32_16x16x32_bf16 v[28:31], v[156:159], v[88:91], v[28:31]\n"
    "s_waitcnt vmcnt(8)\n"
    "s_waitcnt lgkmcnt(0)\n"
    "v_mfma_f32_16x16x32_bf16 v[16:19], v[32:35], v[92:95], v[16:19]\n"
    "v_mfma_f32_16x16x32_bf16 v[20:23], v[36:39], v[92:95], v[20:23]\n"
    "v_mfma_f32_16x16x32_bf16 v[24:27], v[40:43], v[92:95], v[24:27]\n"
    "v_mfma_f32_16x16x32_bf16 v[28:31], v[44:47], v[92:95], v[28:31]\n"
    "s_waitcnt vmcnt(7)\n"
    "v_mfma_f32_16x16x32_bf16 v[16:19], v[160:163], v[96:99], v[16:19]\n"
    "ds_read_b128 v[32:35], v7 offset:24576\n"
    "v_mfma_f32_16x16x32_bf16 v[20:23], v[164:167], v[96:99], v[20:23]\n"
    "ds_read_b128 v[36:39], v7 offset:25600\n"
    "v_mfma_f32_16x16x32_bf16 v[24:27], v[168:171], v[96:99], v[24:27]\n"
    "v_mfma_f32_16x16x32_bf16 v[28:31], v[172:175], v[96:99], v[28:31]\n"
    "s_waitcnt vmcnt(6)\n"
    "v_mfma_f32_16x16x32_bf16 v[16:19], v[176:179], v[100:103], v[16:19]\n"
    "ds_read_b128 v[40:43], v7 offset:26624\n"
    "v_mfma_f32_16x16x32_bf16 v[20:23], v[180:183], v[100:103], v[20:23]\n"
    "ds_read_b128 v[44:47], v7 offset:27648\n"
    "v_mfma_f32_16x16x32_bf16 v[24:27], v[184:187], v[100:103], v[24:27]\n"
    "v_mfma_f32_16x16x32_bf16 v[28:31], v[188:191], v[100:103], v[28:31]\n"
    "s_waitcnt vmcnt(5)\n"
    "v_mfma_f32_16x16x32_bf16 v[16:19], v[192:195], v[104:107], v[16:19]\n"
    "v_mfma_f32_16x16x32_bf16 v[20:23], v[196:199], v[104:107], v[20:23]\n"
    "v_mfma_f32_16x16x32_bf16 v[24:27], v[200:203], v[104:107], v[24:27]\n"
    "v_mfma_f32_16x16x32_bf16 v[28:31], v[204:207], v[104:107], v[28:31]\n"
    "s_waitcnt vmcnt(4)\n"
    "s_waitcnt lgkmcnt(0)\n"
    "v_mfma_f32_16x16x32_bf16 v[16:19], v[32:35], v[108:111], v[16:19]\n"
    "v_mfma_f32_16x16x32_bf16 v[20:23], v[36:39], v[108:111], v[20:23]\n"
    "v_mfma_f32_16x16x32_bf16 v[24:27], v[40:43], v[108:111], v[24:27]\n"
    "v_mfma_f32_16x16x32_bf16 v[28:31], v[44:47], v[108:111], v[28:31]\n"
    "s_waitcnt vmcnt(3)\n"
    "v_mfma_f32_16x16x32_bf16 v[16:19], v[208:211], v[112:115], v[16:19]\n"
    "ds_read_b128 v[32:35], v7 offset:28672\n"
    "v_mfma_f32_16x16x32_bf16 v[20:23], v[212:215], v[112:115], v[20:23]\n"
    "ds_read_b128 v[36:39], v7 offset:29696\n"
    "v_mfma_f32_16x16x32_bf16 v[24:27], v[216:219], v[112:115], v[24:27]\n"
    "v_mfma_f32_16x16x32_bf16 v[28:31], v[220:223], v[112:115], v[28:31]\n"
    "s_waitcnt vmcnt(2)\n"
    "v_mfma_f32_16x16x32_bf16 v[16:19], v[224:227], v[116:119], v[16:19]\n"
    "ds_read_b128 v[40:43], v7 offset:30720\n"
    "v_mfma_f32_16x16x32_bf16 v[20:23], v[228:231], v[116:119], v[20:23]\n"
    "ds_read_b128 v[44:47], v7 offset:31744\n"
    "v_mfma_f32_16x16x32_bf16 v[24:27], v[232:235], v[116:119], v[24:27]\n"
    "v_mfma_f32_16x16x32_bf16 v[28:31], v[236:239], v[116:119], v[28:31]\n"
    "s_waitcnt vmcnt(1)\n"
    "v_mfma_f32_16x16x32_bf16 v[16:19], v[240:243], v[120:123], v[16:19]\n"
    "v_mfma_f32_16x16x32_bf16 v[20:23], v[244:247], v[120:123], v[20:23]\n"
    "v_mfma_f32_16x16x32_bf16 v[24:27], v[248:251], v[120:123], v[24:27]\n"
    "v_mfma_f32_16x16x32_bf16 v[28:31], v[252:255], v[120:123], v[28:31]\n"
    "s_waitcnt vmcnt(0)\n"
    "s_waitcnt lgkmcnt(0)\n"
    "v_mfma_f32_16x16x32_bf16 v[16:19], v[32:35], v[124:127], v[16:19]\n"
    "v_mfma_f32_16x16x32_bf16 v[20:23], v[36:39], v[124:127], v[20:23]\n"
    "v_mfma_f32_16x16x32_bf16 v[24:27], v[40:43], v[124:127], v[24:27]\n"
    "v_mfma_f32_16x16x32_bf16 v[28:31], v[44:47], v[124:127], v[28:31]\n"
    "ds_write_b128 v8, v[16:19] offset:0\n"
    "ds_write_b128 v8, v[20:23] offset:1024\n"
    "s_nop 2\n"
    "ds_write_b128 v8, v[24:27] offset:2048\n"
    "s_nop 1\n"
    "ds_write_b128 v8, v[28:31] offset:3072\n"
    "s_waitcnt lgkmcnt(0)\n"
    "s_barrier\n"
    "ds_read_b128 v[48:51], v9 offset:0\n"
    "ds_read_b128 v[52:55], v9 offset:4096\n"
    "ds_read_b128 v[56:59], v9 offset:8192\n"
    "ds_read_b128 v[60:63], v9 offset:12288\n"
    "v_xor_b32 v8, 0x4000, v8\n"
    "v_xor_b32 v9, 0x4000, v9\n"
    "s_lshl_b32 s69, s57, 18\n"
    "s_add_u32 s57, s57, 1\n"
    "s_waitcnt lgkmcnt(0)\n"
    "v_pk_add_f32 v[48:49], v[48:49], v[52:53]\n"
    "v_pk_add_f32 v[50:51], v[50:51], v[54:55]\n"
    "v_pk_add_f32 v[56:57], v[56:57], v[60:61]\n"
    "v_pk_add_f32 v[58:59], v[58:59], v[62:63]\n"
    "v_pk_add_f32 v[48:49], v[48:49], v[56:57]\n"
    "v_pk_add_f32 v[50:51], v[50:51], v[58:59]\n"
    "buffer_store_dwordx4 v[48:51], v10, s[48:51], s69 offen sc1\n"
    "s_cmp_lt_u32 s57, s58\n"
    "s_cbranch_scc1 .LS_cold%=\n"
    "s_mov_b32 s57, s60\n"
    "s_mov_b32 s60, 64\n"
    "s_cmp_lt_u32 s57, 64\n"
    "s_cbranch_scc1 .LS_cold%=\n"
    "s_not_b32 s65, s67\n"
    "v_mov_b32 v16, s66\n"
    "v_mov_b32 v17, s65\n"
    "v_mov_b32 v18, s66\n"
    "v_mov_b32 v19, s65\n"
    "v_writelane_b32 v17, s67, 49\n"
    "buffer_store_dwordx4 v[16:19], v13, s[40:43], 0 offen sc1\n"
    "s_waitcnt vmcnt(0)\n"
    "s_endpgm\n"
    ".LS_flr%=:\n"
    "s_sleep 1\n"
    "s_add_u32 s68, s68, 1\n"
    "s_cmp_lt_u32 s68, 65536\n"
    "s_cbranch_scc1 .LS_fl%=\n"
    "s_branch .LS_tmo%=\n"
    ".LS_snr%=:\n"
    "s_sleep 8\n"
    "s_add_u32 s68, s68, 1\n"
    "s_cmp_lt_u32 s68, 65536\n"
    "s_cbranch_scc1 .LS_sn%=\n"
    "s_branch .LS_tmo%=\n"
    ".LS_tmo%=:\n"
    "v_mov_b32 v20, 1\n"
    "v_mov_b32 v21, 1572864\n"
    "buffer_store_dword v20, v21, s[40:43], 0 offen sc1\n"
    "s_waitcnt vmcnt(0)\n"
    "s_endpgm\n"
    :
    :
      "{v64}"(vo_src), "{v65}"(a_tr), "{v66}"(vo_of), "{v67}"(vo_cnt), "{v68}"(a_w), "{v69}"(a_rw), "{v70}"(a_rr), "{v71}"(vo_out), "{v72}"(vo_dbg),
      "{v73}"(vo_ep), "{s[40:43]}"(rxv), "{s[44:47]}"(rw), "{s[48:51]}"(ro), "{s56}"(w), "{s57}"(seg0), "{s58}"(send), "{s59}"(rho), "{s60}"(sextra)
    :
      "memory", "vcc", "scc", "v255", "a255", "s101");

  __builtin_amdgcn_endpgm();
}
DI u32 ct_() { return ct(); }
constexpr int NSLOT = 8, MAP = 0, INIT_WK = 159, PROBE_WK = 3;
__global__ void __launch_bounds__(256, 1) k_fused(Params p) {
  extern __shared__ __attribute__((aligned(16))) unsigned char lds_raw[];
  const u32 lb = 0;
  const int x = blockIdx.x & 7, i = blockIdx.x >> 3;
  const int w0 = __builtin_amdgcn_readfirstlane(threadIdx.x >> 6);
  int role = 2, idx = (x - 3) * 32 + i;
  if (MAP == 0) { if (x == 0) { role = 0; idx = i; } else if (x < 3) { role = 1; idx = (x - 1) * 32 + i; } }
  if (MAP == 1 && x < 4) {
    if (i < 8) { role = 0; idx = 8 * x + i; } else if (i < 24) { role = 1; idx = ((i - 8) >> 3) * 32 + 8 * x + (i & 7); } else idx = 8 * x + i - 24;
  }
  if (MAP == 2 && x < 4) {
    if (x == 1 || x == 2) { role = 1; idx = (i >> 4) * 32 + (x - 1) * 16 + (i & 15); } else { role = i < 16 ? 0 : 2; idx = (x ? 16 : 0) + (i & 15); }
  }
  if (MAP == 4 && x < 3) {
    const int na = x < 2 ? 12 : 8;
    if (i < na) { role = 0; idx = 12 * x + i; } else { const int j = i - na; role = 1; idx = ((j >> 1) & 1) * 32 + 10 * x + (j >> 2) * 2 + (j & 1); }
  }
  if (MAP == 5 && x < 3) { if (x == 0) { role = 0; idx = i; } else { role = 1; idx = (i >> 4) * 32 + (x - 1) * 16 + (i & 15); } }
  if (role < 2) {
    if (role == 0) role_A(p, lb, idx, w0, threadIdx.x & 63);
    else role_BR(p, lb, idx, w0, threadIdx.x & 63);
  }
  const int wk = idx;
  const CAS Params* pk0 = (const CAS Params*)__builtin_amdgcn_kernarg_segment_ptr();
  u64 plan_lo = *(const CAS u64*)(pk0->plan + 4 * (wk >> 5)), plan_hi = *(const CAS u64*)(pk0->plan + 4 * (wk >> 5) + 2);
  if (wk == INIT_WK) clear_control(*pk0);
  if (2 && (wk >> 5) == (INIT_WK >> 5)) warm_slow_columns(*pk0, wk & 31, w0, threadIdx.x & 63);
  if (wk == PROBE_WK) probe_placement(*pk0, w0);
  if (0 && (wk >> 5) == (INIT_WK >> 5)) for (int k = 0; k < 0; ++k) __builtin_amdgcn_s_sleep(36);
  if (2 > 1 && (wk >> 5) == (INIT_WK >> 5)) warm_inputs(*pk0, wk & 31, w0, threadIdx.x & 63);
  bool first = true;
  for (int k = 0; k < NSLOT; ++k) {
    const u32 t = (u32)plan_lo & 0xffffu;
    plan_lo = (plan_lo >> 16) | (plan_hi << 48); plan_hi >>= 16;
    const u32 j = (t & 7) * 32 + (wk & 31), nr = (t >> 3) & 15, q = nr == 3 ? (j * 43) >> 7 : j >> (31 - __builtin_clz(nr | 1));
    int ty = t >> 12, rt = ((t >> 7) & 31) + j - q * nr, ct = q;
    if (ty == 1) { ty = 1 + (ct & 1); ct >>= 1; }
    if (ty == 0) break;
    int lane = threadIdx.x & 63, w = w0;
    const CAS Params* pp = (const CAS Params*)__builtin_amdgcn_kernarg_segment_ptr();
    asm volatile("" : "+v"(lane), "+s"(w), "+s"(pp));
    const CAS Params& p = *pp;
    if (ty == 1) task_K(p, rt, ct, w, lane, first);
    else if (ty == 2) task_V(p, rt, ct, w, lane, first);
    else if (ty == 3) task_Q(p, rt, ct, w, lane);
    else if (ty == 4) task_U(p, rt, ct, w, lane, first);
    else if (ty == 5) task_Y(p, rt, ct, w, lane);
    else if (ty == 7) task_M(p, rt, ct, w, lane, first);
    else task_S(p, rt, ct, w, lane);
    first = false;
  }
}
static const int g_par[8] = {64, 58, 36, 0xff, 0};
extern "C" void kernel_launch(void* const* d_in, const int* in_sizes, int n_in, void* d_out, int out_size, void* d_ws, size_t ws_size, hipStream_t stream) {
  Params p;
  p.xA = (const uint16_t*)d_in[0]; p.Lre = (const uint16_t*)d_in[1]; p.Lim = (const uint16_t*)d_in[2]; p.Bre = (const uint16_t*)d_in[3]; p.Bim = (const uint16_t*)d_in[4];
  p.Cre = (const uint16_t*)d_in[5]; p.Cim = (const uint16_t*)d_in[6]; p.ldt = (const uint16_t*)d_in[7]; p.Dsk = (const uint16_t*)d_in[8]; p.xB = (const uint16_t*)d_in[9];
  p.mem0 = (const uint16_t*)d_in[10]; p.Wq = (const uint16_t*)d_in[11]; p.Wk = (const uint16_t*)d_in[12]; p.Wv = (const uint16_t*)d_in[13]; p.Wo = (const uint16_t*)d_in[14];
  p.out = (float*)d_out;
  p.wb = (unsigned char*)(((uintptr_t)d_ws + (2u << 20) - 1) & ~(uintptr_t)((2u << 20) - 1)) + ((size_t)g_par[4] << 21);
  p.da = g_par[0]; p.db = g_par[1]; p.dr = g_par[2]; p.mode = g_par[3];
  static const u32 plan[20] = {0x11207010u, 0x30201320u, 0x32201520u, 0x36203420u, 0x11217011u, 0x30211321u, 0x32211521u, 0x36213421u, 0x11227012u, 0x15221322u, 0x17104620u, 0x5440u, 0x11237013u, 0x15231323u, 0x17115040u, 0x6088u, 0x42204020u, 0x60084420u, 0x0u, 0x0u};
  for (int i = 0; i < 20; ++i) p.plan[i] = plan[i];
  (void)hipFuncSetAttribute((const void*)k_fused, hipFuncAttributeMaxDynamicSharedMemorySize, LDS_TOTAL);
  k_fused<<<256, 256, LDS_TOTAL, stream>>>(p);
}
```
